```python
import torch

def _softmax(x, axis=-1):
    return torch.softmax(x, dim=axis)
def _gelu(x):
    return torch.nn.functional.gelu(x, approximate="tanh")
def _silu(x):
    return torch.nn.functional.silu(x)
def _sigmoid(x):
    return torch.sigmoid(x)
def _layernorm(x, axis=-1, eps=1e-5):
    m = x.mean(dim=axis, keepdim=True)
    v = x.var(dim=axis, keepdim=True, unbiased=False)
    return (x - m) / torch.sqrt(v + eps)
def _scatter_add(tgt, idx, src, axis=0):
    return tgt.clone().index_add_(axis, idx, src)
def _segment_sum(src, seg, n):
    out = torch.zeros((n,) + src.shape[1:], dtype=src.dtype)
    return out.index_add_(0, seg, src)
def _pad(x, pw):
    flat = tuple(v for pair in reversed(pw) for v in pair)
    return torch.nn.functional.pad(x, flat)

kernel_name = 'interleave_layers__sliding_local_dilated__swiglu_ffn__prompt'

def setup_inputs():

    _w = (lambda a: (lambda t: t.to(torch.bfloat16) if t.is_floating_point() else t)(torch.as_tensor(a)))
    def _A_setup():
        B, S, D = 1, 2048, 448
        return {
            "x": _w((lambda *s: torch.randn(*s, dtype=torch.bfloat16) * 0.05)(B, S, D)),
            "wqkv": _w((lambda *s: torch.randn(*s, dtype=torch.bfloat16) * 0.05)(D, 3 * D)),
            "wo": _w((lambda *s: torch.randn(*s, dtype=torch.bfloat16) * 0.05)(D, D)),
        }
    _A_in = _A_setup()

    def _B_setup():
        B, S, D, F = 1, 2048, 448, 1792
        return {
            "x": _w((lambda *s: torch.randn(*s, dtype=torch.bfloat16) * 0.05)(B, S, D)),
            "w_gate": _w((lambda *s: torch.randn(*s, dtype=torch.bfloat16) * 0.05)(D, F)),
            "w_up": _w((lambda *s: torch.randn(*s, dtype=torch.bfloat16) * 0.05)(D, F)),
            "w_down": _w((lambda *s: torch.randn(*s, dtype=torch.bfloat16) * 0.05)(F, D)),
        }
    _B_in = _B_setup()

    return {
        "A_x": _A_in["x"],
        "A_wqkv": _A_in["wqkv"],
        "A_wo": _A_in["wo"],
        "B_x": _B_in["x"],
        "B_w_gate": _B_in["w_gate"],
        "B_w_up": _B_in["w_up"],
        "B_w_down": _B_in["w_down"],
    }

def reference(A_x, A_wqkv, A_wo, B_x, B_w_gate, B_w_up, B_w_down):
    def _A_ref(x, wqkv, wo):
        B, S, D = x.shape
        H = 16; Dh = D // H
        W, dil = 8, 2
        qkv = (lambda x, *d: x.permute(*d))((x @ wqkv).reshape(B, S, 3, H, Dh), 2, 0, 3, 1, 4)
        q, k, v = qkv[0], qkv[1], qkv[2]
        scale = Dh ** -0.5
        pos = torch.arange(S)
        off = torch.arange(W) * dil
        idx = pos[:, None] - off[None, :]
        mask = idx >= 0
        idx_c = torch.where(mask, idx, 0).reshape(-1)
        kg = k[:, :, idx_c, :].reshape(B, H, S, W, Dh)
        vg = v[:, :, idx_c, :].reshape(B, H, S, W, Dh)
        logits = torch.einsum("bhsd,bhswd->bhsw", q, kg) * scale
        logits = torch.where(mask[None, None, :, :], logits, -1e9)
        attn = _softmax(logits, axis=-1)
        o = torch.einsum("bhsw,bhswd->bhsd", attn, vg)
        return (lambda x, *d: x.permute(*d))(o, 0, 2, 1, 3).reshape(B, S, D) @ wo

    def _B_ref(x, w_gate, w_up, w_down):
        g = _silu(x @ w_gate)
        u = x @ w_up
        return (g * u) @ w_down

    x = A_x
    x = x + _A_ref(_layernorm(x, axis=-1), A_wqkv, A_wo)
    x = x + _B_ref(_layernorm(x, axis=-1), B_w_gate, B_w_up, B_w_down)
    x = x + _A_ref(_layernorm(x, axis=-1), A_wqkv, A_wo)
    x = x + _B_ref(_layernorm(x, axis=-1), B_w_gate, B_w_up, B_w_down)
    return x
```

```cpp
#include <hip/hip_runtime.h>
#include <hip/hip_bf16.h>

typedef unsigned v4u __attribute__((ext_vector_type(4)));
typedef unsigned v2u __attribute__((ext_vector_type(2)));
typedef float f32x4 __attribute__((ext_vector_type(4)));
typedef float f32x2 __attribute__((ext_vector_type(2)));
typedef float f32x16 __attribute__((ext_vector_type(16)));
typedef __bf16 bf16x8 __attribute__((ext_vector_type(8)));
typedef __bf16 bf16x2 __attribute__((ext_vector_type(2)));
#define LAS __attribute__((address_space(3)))
#define GAS __attribute__((address_space(1)))
#define DEV __device__ __forceinline__

constexpr int S_ = 2048, D_ = 448, F_ = 1792;
constexpr int OFF_FLAG = 0;
constexpr int OFF_WA = 32768;
constexpr int OFF_WB = OFF_WA + 16 * 84 * 1024;
constexpr int OFF_WC = OFF_WB + 7 * 64 * 1024;
constexpr int OFF_WD = OFF_WC + 16 * 196 * 1024;
constexpr int OFF_CS = OFF_WD + 7 * 224 * 1024;
constexpr int XBUF = 64 * 28 * 1024, SBUF = 64 * 14 * 256, OBUF = 64 * 32 * 1024, HBUF = 64 * 112 * 1024;
constexpr int OFF_X = OFF_CS + 16 * 3072;
constexpr int OFF_ST = OFF_X + 4 * XBUF;
constexpr int OFF_O = OFF_ST + 4 * SBUF;
constexpr int OFF_H = OFF_O + 2 * OBUF;
constexpr int WS_BYTES = OFF_H + 2 * HBUF;
constexpr int SLOT = 28 * 1024;
constexpr int L_EX = 5 * SLOT, L_CS = L_EX + 16384, L_RS = L_CS + 3072, LDS_BYTES = L_RS + 512;
static_assert(LDS_BYTES <= 163840, "lds");
constexpr int AUX_SC1 = 16;

DEV unsigned pk(float a, float b) { bf16x2 t = {(__bf16)a, (__bf16)b}; return __builtin_bit_cast(unsigned, t); }
DEV float asf(unsigned u) { return __builtin_bit_cast(float, u); }
DEV float blo(unsigned u) { return __builtin_bit_cast(float, u << 16); }
DEV float bhi(unsigned u) { return __builtin_bit_cast(float, u & 0xffff0000u); }
DEV f32x16 mfma32(v4u a, v4u b, f32x16 c) {
  return __builtin_amdgcn_mfma_f32_32x32x16_bf16(__builtin_bit_cast(bf16x8, a), __builtin_bit_cast(bf16x8, b), c, 0, 0, 0);
}
DEV void mfma32p(v4u a, v4u b, f32x16& c) { c = mfma32(a, b, c); asm volatile("" : "+v"(c)); }
DEV f32x4 mfma16(v4u a, v4u b, f32x4 c) {
  return __builtin_amdgcn_mfma_f32_16x16x32_bf16(__builtin_bit_cast(bf16x8, a), __builtin_bit_cast(bf16x8, b), c, 0, 0, 0);
}
DEV float xsum32(float v) {
  auto r = __builtin_amdgcn_permlane32_swap(__builtin_bit_cast(unsigned, v), __builtin_bit_cast(unsigned, v), false, false);
  return __builtin_bit_cast(float, (unsigned)r[0]) + __builtin_bit_cast(float, (unsigned)r[1]);
}
DEV float xmax32(float v) {
  auto r = __builtin_amdgcn_permlane32_swap(__builtin_bit_cast(unsigned, v), __builtin_bit_cast(unsigned, v), false, false);
  return fmaxf(__builtin_bit_cast(float, (unsigned)r[0]), __builtin_bit_cast(float, (unsigned)r[1]));
}
DEV void split3(float v, unsigned& hi, unsigned& mid, unsigned& lo) {
  hi = __builtin_bit_cast(unsigned, v) & 0xffff0000u;
  float r = v - __builtin_bit_cast(float, hi);
  mid = __builtin_bit_cast(unsigned, r) & 0xffff0000u;
  float r2 = r - __builtin_bit_cast(float, mid);
  lo = __builtin_bit_cast(unsigned, r2) & 0xffff0000u;
}
DEV v4u aug_x(float negmean, bool on) {
  unsigned hi, mid, lo; split3(negmean, hi, mid, lo);
  v4u r = {hi | (hi >> 16), mid | (mid >> 16), lo | (hi >> 16), 0u};
  if (!on) r = v4u{0u, 0u, 0u, 0u};
  return r;
}
DEV v4u aug_w(v2u c) { return v4u{c.x, c.x, c.y, 0u}; }

__global__ __launch_bounds__(512) void k_pack(const unsigned short* __restrict__ x, const unsigned short* __restrict__ wqkv,
                                              const unsigned short* __restrict__ wo, const unsigned short* __restrict__ wg,
                                              const unsigned short* __restrict__ wu, const unsigned short* __restrict__ wd,
                                              char* __restrict__ ws) {
  __shared__ float red[8][32];
  const int u = blockIdx.x, tid = threadIdx.x, w = tid >> 6, lane = tid & 63, n = lane & 31, h = lane >> 5;
  if (u < 64 && tid < 128) ((unsigned*)(ws + OFF_FLAG))[u * 128 + tid] = 0u;
  if (u >= 230) {
    if (w >= 7) return;
    const int tt = u - 230, g = tt >> 2, T = tt & 3;
    const int s = 2 * (128 * (g & 7) + 32 * T + n) + (g >> 3);
    const unsigned short* row = x + (size_t)s * D_ + 64 * w + 4 * h;
    v2u a[8];
#pragma unroll
    for (int i = 0; i < 8; ++i) a[i] = *(const v2u*)(row + 8 * i);
#pragma unroll
    for (int i = 0; i < 4; ++i)
      __builtin_nontemporal_store(v4u{a[2 * i].x, a[2 * i].y, a[2 * i + 1].x, a[2 * i + 1].y},
                                  (v4u*)(ws + OFF_X + (tt * 28 + 4 * w + i) * 1024 + lane * 16));
#pragma unroll
    for (int q = 0; q < 2; ++q) {
      float sm = 0.f;
#pragma unroll
      for (int i = 0; i < 4; ++i) sm += blo(a[4 * q + i].x) + bhi(a[4 * q + i].x) + blo(a[4 * q + i].y) + bhi(a[4 * q + i].y);
      sm = xsum32(sm);
      const float mu = sm * (1.f / 32.f);
      float m2 = 0.f;
#pragma unroll
      for (int i = 0; i < 4; ++i) {
        float d0 = blo(a[4 * q + i].x) - mu, d1 = bhi(a[4 * q + i].x) - mu, d2 = blo(a[4 * q + i].y) - mu, d3 = bhi(a[4 * q + i].y) - mu;
        m2 += d0 * d0 + d1 * d1 + d2 * d2 + d3 * d3;
      }
      m2 = xsum32(m2);
      if (h == 0) *(f32x2*)(ws + OFF_ST + (tt * 14 + 2 * w + q) * 256 + n * 8) = f32x2{sm, m2};
    }
    return;
  }
  const unsigned short* src; int ld; bool lane_ok = true; int cs_off = -1; int kind;
  if (u < 48) { kind = 0; const int m = u / 3, t = u % 3; src = wqkv + t * D_ + 28 * m + (n < 28 ? n : 0); lane_ok = n < 28; ld = 3 * D_; cs_off = m * 3072 + t * 256; }
  else if (u < 62) { kind = 1; src = wo + 32 * (u - 48) + n; ld = D_; }
  else if (u < 174) { kind = 2; const int v = u - 62, m = v / 7, t = v % 7; src = (n < 16 ? wg : wu) + 16 * v + (n & 15); ld = F_; cs_off = m * 3072 + (3 + t) * 256; }
  else { kind = 3; const int v = u - 174; src = wd + (size_t)(v & 3) * 448 * D_ + 32 * (v >> 2) + n; ld = D_; }
  float csum = 0.f;
  if (w < 7 || kind == 1) {
    unsigned short e[4][8];
#pragma unroll
    for (int i = 0; i < 4; ++i) {
      const int s = 4 * w + i;
#pragma unroll
      for (int j = 0; j < 8; ++j) {
        int kk = 8 * (j >> 2) + 4 * h + (j & 3), k; bool ok = lane_ok;
        if (kind == 1) { const int dd = 16 * (s & 1) + kk; ok = dd < 28; k = 28 * (s >> 1) + (ok ? dd : 0); }
        else k = 16 * s + kk;
        const unsigned short v = src[(size_t)k * ld];
        e[i][j] = ok ? v : (unsigned short)0;
      }
    }
#pragma unroll
    for (int i = 0; i < 4; ++i) {
      const int s = 4 * w + i;
      int dst;
      if (kind == 0) dst = OFF_WA + (u * 28 + s) * 1024;
      else if (kind == 1) { const int nt = u - 48, kh = s >> 4, ks = s & 15, c = ks / 7, kk = ks % 7;
        dst = OFF_WB + ((nt >> 1) * 64 + c * 28 + kh * (c < 2 ? 14 : 4) + kk * 2 + (nt & 1)) * 1024; }
      else if (kind == 2) dst = OFF_WC + ((u - 62) * 28 + s) * 1024;
      else { const int v = u - 174, nt = v >> 2, sg = (v & 3) * 28 + s, kh = sg / 56, ks = sg % 56, c = ks / 7, kk = ks % 7;
        dst = OFF_WD + ((nt >> 1) * 224 + c * 28 + kh * 14 + kk * 2 + (nt & 1)) * 1024; }
      v4u o;
#pragma unroll
      for (int p = 0; p < 4; ++p) {
        o[p] = (unsigned)e[i][2 * p] | ((unsigned)e[i][2 * p + 1] << 16);
        csum += blo(o[p]) + bhi(o[p]);
      }
      __builtin_nontemporal_store(o, (v4u*)(ws + dst + lane * 16));
    }
  }
  if (cs_off >= 0) {
    csum = xsum32(csum);
    if (h == 0) red[w][n] = csum;
    __syncthreads();
    if (tid < 32) {
      float c = 0.f;
#pragma unroll
      for (int i = 0; i < 7; ++i) c += red[i][tid];
      unsigned hi, mid, lo; split3(c, hi, mid, lo);
      *(v2u*)(ws + OFF_CS + cs_off + tid * 8) = v2u{(hi >> 16) | mid, (lo >> 16) | hi};
    }
  }
}

#define BAR() asm volatile("s_waitcnt lgkmcnt(0)\n\ts_barrier" ::: "memory")
#define VMWAIT(n) asm volatile("s_waitcnt vmcnt(" #n ")" ::: "memory")
typedef __amdgpu_buffer_rsrc_t rsrc_t;
DEV v4u ld16(rsrc_t rs, int voff, int soff) { return __builtin_amdgcn_raw_buffer_load_b128(rs, voff, soff, AUX_SC1); }
DEV v2u ld8(rsrc_t rs, int voff, int soff) { return __builtin_amdgcn_raw_buffer_load_b64(rs, voff, soff, AUX_SC1); }
DEV void st16(v4u v, rsrc_t rs, int voff, int soff) { __builtin_amdgcn_raw_buffer_store_b128(v, rs, voff, soff, AUX_SC1); }
DEV void st8(v2u v, rsrc_t rs, int voff, int soff) { __builtin_amdgcn_raw_buffer_store_b64(v, rs, voff, soff, AUX_SC1); }
DEV v4u ldf(rsrc_t rs, int lane, int base, int i) { return ld16(rs, lane * 16 + (i & 3) * 1024, base + (i >> 2) * 4096); }
DEV v4u lds16(LAS char* p) { return *(LAS v4u*)p; }

DEV void poll(char* ws, int g, int m0, int cnt, unsigned want, int lane) {
  GAS unsigned* f = (GAS unsigned*)(ws + OFF_FLAG) + (g * 16 + m0 + (lane < cnt ? lane : 0)) * 32;
  for (int spin = 0; spin < (1 << 20); ++spin) {
    const unsigned v = __hip_atomic_load(f, __ATOMIC_RELAXED, __HIP_MEMORY_SCOPE_AGENT);
    if (__all(v >= want)) break;
    __builtin_amdgcn_s_sleep(1);
  }
  __builtin_amdgcn_fence(__ATOMIC_ACQUIRE, "wavefront");
}
DEV void publish(char* ws, int g, int m, unsigned val) {
  __hip_atomic_store((GAS unsigned*)(ws + OFF_FLAG) + (g * 16 + m) * 32, val, __ATOMIC_RELAXED, __HIP_MEMORY_SCOPE_AGENT);
}
template <int N> DEV void dma(rsrc_t rs, int src, LAS char* dst, int hw, int lane16) {
#pragma unroll
  for (int i = 0; i < N; ++i)
    __builtin_amdgcn_raw_ptr_buffer_load_lds(rs, (LAS void*)(dst + (hw * N + i) * 1024), 16, lane16, src + (hw * N + i) * 1024, 0, 0);
}
#define OPQ_S(x) asm volatile("" : "+s"(x))
#define OPQ_V(x) asm volatile("" : "+v"(x))
template <int I> struct IC { static constexpr int v = I; };
template <int I, int N, class F> DEV void sfor(F& f) { if constexpr (I < N) { f(IC<I>{}); sfor<I + 1, N>(f); } }
template <int N, int PF, class RD, class ST> DEV void pipe(RD rd, ST st) {
  v4u wb[PF];
  auto pro = [&](auto I) __attribute__((always_inline)) { wb[I.v] = rd(I); };
  sfor<0, PF>(pro);
  __builtin_amdgcn_sched_barrier(0);
  auto body = [&](auto I) __attribute__((always_inline)) {
    constexpr int i = I.v;
    st(I, wb[i % PF]);
    if constexpr (i + PF < N) wb[i % PF] = rd(IC<i + PF>{});
    if constexpr (i + PF == N) (void)rd(IC<N>{});
    __builtin_amdgcn_sched_barrier(0);
  };
  sfor<0, N>(body);
}
DEV int slot_of(int c) { return (c % 5) * SLOT; }
DEV void fin_stats(const v2u (&st)[7], float& mean, float& rstd) {
  float sm = 0.f;
#pragma unroll
  for (int i = 0; i < 7; ++i) sm += asf(st[i].x);
  mean = xsum32(sm) * (1.f / D_);
  float m2 = 0.f;
#pragma unroll
  for (int i = 0; i < 7; ++i) {
    const float d = asf(st[i].x) * (1.f / 32.f) - mean;
    m2 += asf(st[i].y) + 32.f * d * d;
  }
  rstd = __builtin_amdgcn_rsqf(xsum32(m2) * (1.f / D_) + 1e-5f);
}
DEV v4u pack8(const f32x16& a, int o, float sc) {
  return v4u{pk(a[o] * sc, a[o + 1] * sc), pk(a[o + 2] * sc, a[o + 3] * sc), pk(a[o + 4] * sc, a[o + 5] * sc), pk(a[o + 6] * sc, a[o + 7] * sc)};
}
DEV v4u swiglu(const f32x16& a, float rstd) {
  float hv[8];
#pragma unroll
  for (int i = 0; i < 8; ++i) {
    const float g = a[i] * rstd, up = a[8 + i] * rstd;
    const float e = __builtin_amdgcn_exp2f(g * -1.4426950408889634f);
    hv[i] = g * up * __builtin_amdgcn_rcpf(1.f + e);
  }
  return v4u{pk(hv[0], hv[1]), pk(hv[2], hv[3]), pk(hv[4], hv[5]), pk(hv[6], hv[7])};
}
#define TS(i)

DEV void res_epilogue(f32x16 (&acc)[2], v4u (&res)[2], LAS char* lds, rsrc_t rs, char* ws, unsigned short* out, int w, int lane,
                      int g, int m, int tt, int nt, int xb, bool active, bool last, unsigned flagval, int tid) {
#pragma unroll
  for (int q = 0; q < 4; ++q)
    *(LAS f32x4*)(lds + L_EX + w * 4096 + q * 1024 + lane * 16) = f32x4{acc[1][4 * q], acc[1][4 * q + 1], acc[1][4 * q + 2], acc[1][4 * q + 3]};
  BAR();
  float v[16];
#pragma unroll
  for (int q = 0; q < 4; ++q) {
    const f32x4 p = *(LAS f32x4*)(lds + L_EX + (w ^ 2) * 4096 + q * 1024 + lane * 16);
    const unsigned r0 = res[q >> 1][(q & 1) * 2], r1 = res[q >> 1][(q & 1) * 2 + 1];
    v[4 * q] = acc[0][4 * q] + p[0] + blo(r0); v[4 * q + 1] = acc[0][4 * q + 1] + p[1] + bhi(r0);
    v[4 * q + 2] = acc[0][4 * q + 2] + p[2] + blo(r1); v[4 * q + 3] = acc[0][4 * q + 3] + p[3] + bhi(r1);
  }
#pragma unroll
  for (int q = 0; q < 8; ++q) res[q >> 2][q & 3] = pk(v[2 * q], v[2 * q + 1]);
  if (last) {
    if (active) {
      const int s = 2 * (128 * (g & 7) + 32 * (tt & 3) + (lane & 31)) + (g >> 3);
      unsigned short* rowp = out + (size_t)s * D_ + 32 * nt + 8 * (lane >> 5);
#pragma unroll
      for (int q = 0; q < 2; ++q) {
        auto a = __builtin_amdgcn_permlane32_swap(res[q][0], res[q][2], false, false);
        auto b = __builtin_amdgcn_permlane32_swap(res[q][1], res[q][3], false, false);
        *(v4u*)(rowp + 16 * q) = v4u{(unsigned)a[0], (unsigned)b[0], (unsigned)a[1], (unsigned)b[1]};
      }
    }
    BAR();
    return;
  }
  if (active) {
    st16(res[0], rs, lane * 16, OFF_X + xb * XBUF + (tt * 28 + 2 * nt) * 1024);
    st16(res[1], rs, lane * 16, OFF_X + xb * XBUF + (tt * 28 + 2 * nt + 1) * 1024);
  }
  float sm = 0.f;
#pragma unroll
  for (int q = 0; q < 8; ++q) { v[2 * q] = blo(res[q >> 2][q & 3]); v[2 * q + 1] = bhi(res[q >> 2][q & 3]); sm += v[2 * q] + v[2 * q + 1]; }
  sm = xsum32(sm);
  const float mu = sm * (1.f / 32.f);
  float m2 = 0.f;
#pragma unroll
  for (int q = 0; q < 16; ++q) { const float d = v[q] - mu; m2 += d * d; }
  m2 = xsum32(m2);
  if (active && lane < 32)
    st8(v2u{__builtin_bit_cast(unsigned, sm), __builtin_bit_cast(unsigned, m2)}, rs, lane * 8, OFF_ST + xb * SBUF + (tt * 14 + nt) * 256);
  VMWAIT(0);
  BAR();
  if (tid == 0 && active) publish(ws, g, m, flagval);
}

__global__ __launch_bounds__(512, 2) void k_main(char* ws, unsigned short* out) {
  extern __shared__ __attribute__((aligned(16))) char smem[];
  LAS char* lds = (LAS char*)smem;
  const int tid = threadIdx.x; int lane = tid & 63;
  int w = __builtin_amdgcn_readfirstlane(tid >> 6);
  const int b = blockIdx.x, xcd = b & 7, idx = b >> 3;
  int g = (xcd >> 1) * 4 + (idx >> 3), m = (xcd & 1) * 8 + (idx & 7);
  const int half = m >> 3, j = (m & 7) < 7 ? (m & 7) : 6;
  const bool active = (m & 7) < 7;
  const rsrc_t rs = __builtin_amdgcn_make_buffer_rsrc((void*)ws, 0, WS_BYTES, 0x00020000);
  TS(0);
  if (w >= 4) {
    int hw = w - 4;
    int wa = OFF_WA + m * 84 * 1024, wb = OFF_WB + j * 64 * 1024, wc = OFF_WC + m * 196 * 1024, wd = OFF_WD + j * 224 * 1024;
    const int l16 = lane * 16;
    dma<1>(rs, OFF_CS + m * 3072, lds + L_CS, hw < 3 ? hw : 2, l16);
    dma<7>(rs, wa, lds + slot_of(0), hw, l16); dma<7>(rs, wa + SLOT, lds + slot_of(1), hw, l16); dma<7>(rs, wa + 2 * SLOT, lds + slot_of(2), hw, l16);
    for (int L = 0; L < 2; ++L) {
      const int c0 = 21 * L;
      OPQ_S(hw); OPQ_S(wa); OPQ_S(wb); OPQ_S(wc); OPQ_S(wd); OPQ_V(lane);
      {
        const bool hasprev = (g & 7) > 0;
        const int gp = hasprev ? g - 1 : g, ttp = gp * 4 + 3;
        if (L) poll(ws, gp, 8, 7, 4, lane);
        const int t16 = lane & 15, gq = lane >> 4, tk = 24 + (t16 & 7), isv = hw >> 1, nh = hw & 1;
        v4u xf[14]; v2u st[4];
#pragma unroll
        for (int i = 0; i < 4; ++i) st[i] = ld8(rs, ((gq + 4 * i < 14 ? gq + 4 * i : 0) * 32 + tk) * 8, OFF_ST + 2 * L * SBUF + ttp * 14 * 256);
#pragma unroll
        for (int i = 0; i < 14; ++i) xf[i] = ld16(rs, (gq >> 1) * 1024 + ((gq & 1) * 32 + tk) * 16, OFF_X + 2 * L * XBUF + (ttp * 28 + 2 * i) * 1024);
        VMWAIT(0);
        BAR();
        float sm = 0.f;
#pragma unroll
        for (int i = 0; i < 4; ++i) sm += (gq + 4 * i < 14) ? asf(st[i].x) : 0.f;
        sm += __shfl_xor(sm, 16); sm = xsum32(sm);
        const float mean = sm * (1.f / D_);
        float m2 = 0.f;
#pragma unroll
        for (int i = 0; i < 4; ++i) {
          const float d = asf(st[i].x) * (1.f / 32.f) - mean;
          m2 += (gq + 4 * i < 14) ? asf(st[i].y) + 32.f * d * d : 0.f;
        }
        m2 += __shfl_xor(m2, 16); m2 = xsum32(m2);
        const float rstd = __builtin_amdgcn_rsqf(m2 * (1.f / D_) + 1e-5f);
        LAS char* wp = lds + slot_of(c0 + 1 + isv) + (gq >> 1) * 1024 + ((gq & 1) * 32 + 16 * nh + t16) * 16;
        const v4u xa = aug_x(-mean, gq == 0), wg_ = aug_w(*(LAS v2u*)(lds + L_CS + (1 + isv) * 256 + (16 * nh + t16) * 8));
        f32x4 a = {0.f, 0.f, 0.f, 0.f};
        if (isv) {
#pragma unroll
          for (int i = 0; i < 14; ++i) a = mfma16(xf[i], lds16(wp + i * 2048), a);
          a = mfma16(xa, wg_, a);
          float r[4];
#pragma unroll
          for (int i = 0; i < 4; ++i) r[i] = __shfl(rstd, 4 * gq + i);
          *(LAS v2u*)(lds + L_EX + 2048 + (16 * nh + t16 + 32 * (gq & 1)) * 16 + 8 * (gq >> 1)) = v2u{pk(a[0] * r[0], a[1] * r[1]), pk(a[2] * r[2], a[3] * r[3])};
        } else {
#pragma unroll
          for (int i = 0; i < 14; ++i) a = mfma16(lds16(wp + i * 2048), xf[i], a);
          a = mfma16(wg_, xa, a);
          *(LAS v2u*)(lds + L_EX + nh * 1024 + (16 + t16 + 32 * (gq & 1)) * 16 + 8 * (gq >> 1)) = v2u{pk(a[0] * rstd, a[1] * rstd), pk(a[2] * rstd, a[3] * rstd)};
        }
      }
      asm volatile("s_waitcnt lgkmcnt(0)" ::: "memory");
      dma<7>(rs, wb, lds + slot_of(c0 + 3), hw, l16); dma<7>(rs, wb + SLOT, lds + slot_of(c0 + 4), hw, l16);
      BAR();
      dma<2>(rs, wb + 2 * SLOT, lds + slot_of(c0 + 5), hw, l16);
      dma<7>(rs, wc, lds + slot_of(c0 + 6), hw, l16); dma<7>(rs, wc + SLOT, lds + slot_of(c0 + 7), hw, l16);
      BAR();
      VMWAIT(14);
      BAR();
      BAR();
      dma<7>(rs, wc + 2 * SLOT, lds + slot_of(c0 + 8), hw, l16); dma<7>(rs, wc + 3 * SLOT, lds + slot_of(c0 + 9), hw, l16); dma<7>(rs, wc + 4 * SLOT, lds + slot_of(c0 + 10), hw, l16);
      BAR();
      VMWAIT(14);
      BAR();
      VMWAIT(0);
      BAR();
      dma<7>(rs, wc + 5 * SLOT, lds + slot_of(c0 + 11), hw, l16); dma<7>(rs, wc + 6 * SLOT, lds + slot_of(c0 + 12), hw, l16); dma<7>(rs, wd, lds + slot_of(c0 + 13), hw, l16);
      VMWAIT(7);
      BAR();
      dma<7>(rs, wd + SLOT, lds + slot_of(c0 + 14), hw, l16); dma<7>(rs, wd + 2 * SLOT, lds + slot_of(c0 + 15), hw, l16);
      BAR();
      dma<7>(rs, wd + 3 * SLOT, lds + slot_of(c0 + 16), hw, l16); dma<7>(rs, wd + 4 * SLOT, lds + slot_of(c0 + 17), hw, l16);
      BAR();
      VMWAIT(28);
      BAR();
      VMWAIT(21);
      BAR();
      dma<7>(rs, wd + 5 * SLOT, lds + slot_of(c0 + 18), hw, l16);
      VMWAIT(21);
      BAR();
      dma<7>(rs, wd + 6 * SLOT, lds + slot_of(c0 + 19), hw, l16);
      VMWAIT(21);
      BAR();
      dma<7>(rs, wd + 7 * SLOT, lds + slot_of(c0 + 20), hw, l16);
      VMWAIT(21);
      BAR();
      VMWAIT(14);
      BAR();
      VMWAIT(7);
      BAR();
      VMWAIT(0);
      BAR();
      if (L == 0) { dma<7>(rs, wa, lds + slot_of(21), hw, l16); dma<7>(rs, wa + SLOT, lds + slot_of(22), hw, l16); dma<7>(rs, wa + 2 * SLOT, lds + slot_of(23), hw, l16); }
      BAR();
      BAR();
    }
    return;
  }
  v4u res[2];
  res[0] = ld16(rs, lane * 16, OFF_X + ((g * 4 + 2 * half + (w & 1)) * 28 + 2 * (2 * j + (w >> 1))) * 1024);
  res[1] = ld16(rs, lane * 16 + 1024, OFF_X + ((g * 4 + 2 * half + (w & 1)) * 28 + 2 * (2 * j + (w >> 1))) * 1024);
  for (int L = 0; L < 2; ++L) {
    const int c0 = 21 * L;
    OPQ_S(w); OPQ_S(g); OPQ_S(m); OPQ_V(lane);
    const int t = lane & 31, h = lane >> 5;
    const int mw = w & 1, kh = w >> 1;
    const int ttA = g * 4 + w;
    const int ttB = g * 4 + 2 * half + mw;
    const int ntB = 2 * j + kh;
    {
      if (L) poll(ws, g, 8 * (w >> 1), 7, 4, lane);
      TS(1 + 32 * L);
      v4u xf[28]; v2u st[7];
#pragma unroll
      for (int i = 0; i < 28; ++i) xf[i] = ldf(rs, lane, OFF_X + 2 * L * XBUF + ttA * 28 * 1024, i);
#pragma unroll
      for (int i = 0; i < 7; ++i) st[i] = ld8(rs, ((7 * h + i) * 32 + t) * 8, OFF_ST + 2 * L * SBUF + ttA * 14 * 256);
      TS(2 + 32 * L);
      BAR();
      TS(3 + 32 * L);
      LAS char* wq = lds + slot_of(c0) + lane * 16; LAS char* wk = lds + slot_of(c0 + 1) + lane * 16; LAS char* wv = lds + slot_of(c0 + 2) + lane * 16;
      f32x16 aq = {}, ak = {}, av = {};
      float mean, rstd; v4u xa;
      pipe<84, 6>([&](auto I) __attribute__((always_inline)) { constexpr int i = I.v; return i < 84 ? lds16((i % 3 == 0 ? wk : i % 3 == 1 ? wv : wq) + (i / 3) * 1024) : v4u{}; },
                  [&](auto I, v4u wf) __attribute__((always_inline)) { constexpr int i = I.v;
                    if constexpr (i % 3 == 0) mfma32p(wf, xf[i / 3], ak);
                    else if constexpr (i % 3 == 1) mfma32p(xf[i / 3], wf, av);
                    else mfma32p(wf, xf[i / 3], aq);
                    if constexpr (i == 78) fin_stats(st, mean, rstd);
                    if constexpr (i == 80) { xa = aug_x(-mean, h == 0); *(LAS float*)(lds + L_RS + w * 128 + t * 4) = rstd; }
                  });
      ak = mfma32(aug_w(*(LAS v2u*)(lds + L_CS + 256 + t * 8)), xa, ak);
      TS(4 + 32 * L);
      av = mfma32(xa, aug_w(*(LAS v2u*)(lds + L_CS + 512 + t * 8)), av);
      aq = mfma32(aug_w(*(LAS v2u*)(lds + L_CS + t * 8)), xa, aq);
      v4u vf[2];
#pragma unroll
      for (int q = 0; q < 4; ++q) {
        const f32x4 r = *(LAS f32x4*)(lds + L_RS + w * 128 + (8 * q + 4 * h) * 4);
        vf[q >> 1][(q & 1) * 2] = pk(av[4 * q] * r[0], av[4 * q + 1] * r[1]);
        vf[q >> 1][(q & 1) * 2 + 1] = pk(av[4 * q + 2] * r[2], av[4 * q + 3] * r[3]);
      }
      const v4u kf0 = pack8(ak, 0, rstd), kf1 = pack8(ak, 8, rstd), qf0 = pack8(aq, 0, rstd), qf1 = pack8(aq, 8, rstd);
      if (w < 3) {
        LAS char* e = lds + L_EX + (w + 1) * 3072 + lane * 16;
        *(LAS v4u*)e = kf0; *(LAS v4u*)(e + 1024) = kf1; *(LAS v4u*)(e + 2048) = vf[1];
      }
      f32x16 sc = {};
      sc = mfma32(kf0, qf0, sc); sc = mfma32(kf1, qf1, sc);
      TS(5 + 32 * L);
      BAR();
      TS(6 + 32 * L);
      LAS char* e = lds + L_EX + w * 3072 + lane * 16;
      f32x16 sp = {};
      sp = mfma32(lds16(e), qf0, sp); sp = mfma32(lds16(e + 1024), qf1, sp);
      const v4u vp = lds16(e + 2048);
      const bool hasprev = w > 0 || (g & 7) > 0;
      const float NEG = -__builtin_inff();
      float p[16], pp[4], mx = NEG;
#pragma unroll
      for (int r = 0; r < 16; ++r) { const int jj = (r & 3) + 8 * (r >> 2) + 4 * h; p[r] = ((unsigned)(t - jj) <= 7u) ? sc[r] : NEG; mx = fmaxf(mx, p[r]); }
#pragma unroll
      for (int r = 0; r < 4; ++r) { const int jj = 24 + r + 4 * h; pp[r] = (hasprev && jj >= t + 25) ? sp[12 + r] : NEG; mx = fmaxf(mx, pp[r]); }
      mx = xmax32(mx);
      const float cf = 0.18898223650461362f * 1.4426950408889634f;
      float sum = 0.f;
#pragma unroll
      for (int r = 0; r < 16; ++r) { p[r] = __builtin_amdgcn_exp2f((p[r] - mx) * cf); sum += p[r]; }
#pragma unroll
      for (int r = 0; r < 4; ++r) { pp[r] = __builtin_amdgcn_exp2f((pp[r] - mx) * cf); sum += pp[r]; }
      const float inv = __builtin_amdgcn_rcpf(xsum32(sum));
      const v4u pf0 = {pk(p[0] * inv, p[1] * inv), pk(p[2] * inv, p[3] * inv), pk(p[4] * inv, p[5] * inv), pk(p[6] * inv, p[7] * inv)};
      const v4u pf1 = {pk(p[8] * inv, p[9] * inv), pk(p[10] * inv, p[11] * inv), pk(p[12] * inv, p[13] * inv), pk(p[14] * inv, p[15] * inv)};
      const v4u pfp = {0u, 0u, pk(pp[0] * inv, pp[1] * inv), pk(pp[2] * inv, pp[3] * inv)};
      f32x16 o = {};
      o = mfma32(vf[0], pf0, o); o = mfma32(vf[1], pf1, o); o = mfma32(vp, pfp, o);
      st16(pack8(o, 0, 1.f), rs, lane * 16, OFF_O + L * OBUF + (ttA * 32 + 2 * m) * 1024);
      st16(pack8(o, 8, 1.f), rs, lane * 16, OFF_O + L * OBUF + (ttA * 32 + 2 * m + 1) * 1024);
      TS(7 + 32 * L);
      VMWAIT(0);
      BAR();
      if (tid == 0) publish(ws, g, m, 4 * L + 1);
      TS(8 + 32 * L);
    }
    {
      poll(ws, g, 8 * kh, 8, 4 * L + 1, lane);
      TS(9 + 32 * L);
      v4u of[16];
#pragma unroll
      for (int i = 0; i < 16; ++i) of[i] = ldf(rs, lane, OFF_O + L * OBUF + (ttB * 32 + 16 * kh) * 1024, i);
      TS(10 + 32 * L);
      BAR();
      TS(11 + 32 * L);
      f32x16 acc[2] = {{}, {}};
      {
        LAS char* wp[3][2];
#pragma unroll
        for (int c = 0; c < 3; ++c) {
          wp[c][0] = lds + slot_of(c0 + 3 + c) + kh * (c < 2 ? 15 : 5) * 1024 + lane * 16;
          wp[c][1] = lds + slot_of(c0 + 3 + c) + 1024 + kh * (c < 2 ? 13 : 3) * 1024 + lane * 16;
        }
        pipe<32, 6>([&](auto I) __attribute__((always_inline)) { constexpr int i = I.v; return i < 32 ? lds16(wp[i / 14][i & 1] + ((i % 14) >> 1) * 2048) : v4u{}; },
                    [&](auto I, v4u wf) __attribute__((always_inline)) { constexpr int i = I.v; mfma32p(wf, of[i >> 1], acc[i & 1]); });
      }
      TS(12 + 32 * L);
      res_epilogue(acc, res, lds, rs, ws, out, w, lane, g, m, ttB, ntB, 2 * L + 1, active, false, 4 * L + 2, tid);
      TS(13 + 32 * L);
    }
    {
      poll(ws, g, 8 * (w >> 1), 7, 4 * L + 2, lane);
      TS(14 + 32 * L);
      v4u xf[28]; v2u st[7];
#pragma unroll
      for (int i = 0; i < 28; ++i) xf[i] = ldf(rs, lane, OFF_X + (2 * L + 1) * XBUF + ttA * 28 * 1024, i);
#pragma unroll
      for (int i = 0; i < 7; ++i) st[i] = ld8(rs, ((7 * h + i) * 32 + t) * 8, OFF_ST + (2 * L + 1) * SBUF + ttA * 14 * 256);
      TS(15 + 32 * L);
      BAR();
      TS(16 + 32 * L);
      const int hoff = OFF_H + L * HBUF + (ttA * 112 + 7 * m) * 1024;
      LAS char* cs = lds + L_CS + 768 + t * 8;
      f32x16 a[5] = {{}, {}, {}, {}, {}};
      float mean, rstd; v4u xa; float hv[8]; v2u cw[7];
      auto epi = [&](const f32x16& acc, int e, int tile) __attribute__((always_inline)) {
        if (e >= 0 && e < 8) {
          const float gt = acc[e] * rstd, up = acc[8 + e] * rstd;
          hv[e] = gt * up * __builtin_amdgcn_rcpf(1.f + __builtin_amdgcn_exp2f(gt * -1.4426950408889634f));
          OPQ_V(hv[e]);
        }
        if (e == 8) st16(v4u{pk(hv[0], hv[1]), pk(hv[2], hv[3]), pk(hv[4], hv[5]), pk(hv[6], hv[7])}, rs, lane * 16 + (tile & 3) * 1024, hoff + (tile >> 2) * 4096);
      };
      pipe<196, 4>(
          [&](auto I) __attribute__((always_inline)) { constexpr int i = I.v;
            if constexpr (i == 84 || i == 140 || i == 196) { TS(17 + 2 * ((i - 84) / 56) + 32 * L); BAR(); TS(18 + 2 * ((i - 84) / 56) + 32 * L); }
            constexpr int tile = i < 84 ? i % 3 : i < 140 ? 3 + (i & 1) : i < 168 ? 5 : 6;
            constexpr int ks = i < 84 ? i / 3 : i < 140 ? (i - 84) >> 1 : i < 168 ? i - 140 : i - 168;
            return i < 196 ? lds16(lds + slot_of(c0 + 6 + tile) + lane * 16 + ks * 1024) : v4u{};
          },
          [&](auto I, v4u wf) __attribute__((always_inline)) { constexpr int i = I.v;
            constexpr int tile = i < 84 ? i % 3 : i < 140 ? 3 + (i & 1) : i < 168 ? 5 : 6;
            constexpr int ks = i < 84 ? i / 3 : i < 140 ? (i - 84) >> 1 : i < 168 ? i - 140 : i - 168;
            if constexpr (i == 140 || i == 168) { a[tile - 5] = f32x16{}; mfma32p(aug_w(cw[tile]), xa, a[tile - 5]); }
            if constexpr (i >= 72 && i < 77) cw[i - 72] = *(LAS v2u*)(cs + (i - 72) * 256);
            if constexpr (i == 135 || i == 163) cw[5 + i / 160] = *(LAS v2u*)(cs + (5 + i / 160) * 256);
            mfma32p(wf, xf[ks], a[tile % 5]);
            if constexpr (i == 72) fin_stats(st, mean, rstd);
            if constexpr (i == 74) xa = aug_x(-mean, h == 0);
            if constexpr (i >= 76 && i < 81) mfma32p(aug_w(cw[i - 76]), xa, a[i - 76]);
            if constexpr (i >= 86 && i < 140 && !(i & 1)) epi(a[(i - 86) / 18], ((i - 86) % 18) >> 1, (i - 86) / 18);
            if constexpr (i >= 142 && i < 168) epi(a[3 + (i - 142) / 13], (i - 142) % 13, 3 + (i - 142) / 13);
            if constexpr (i >= 172 && !(i & 1)) epi(a[0], (i - 172) >> 1, 5);
          });
#pragma unroll
      for (int e = 0; e < 9; ++e) epi(a[1], e, 6);
      TS(23 + 32 * L);
      VMWAIT(0);
      BAR();
      if (tid == 0) publish(ws, g, m, 4 * L + 3);
      TS(24 + 32 * L);
    }
    {
      poll(ws, g, 8 * kh, 8, 4 * L + 3, lane);
      TS(25 + 32 * L);
      const int hoff = OFF_H + L * HBUF + (ttB * 112 + 56 * kh) * 1024;
      v4u hf[35];
#pragma unroll
      for (int i = 0; i < 35; ++i) hf[i] = ldf(rs, lane, hoff, i);
      f32x16 acc[2] = {{}, {}};
      pipe<112, 6>(
          [&](auto I) __attribute__((always_inline)) { constexpr int i = I.v;
            if constexpr (i % 14 == 0 && i < 112) { if (i == 0) TS(26 + 32 * L); if (i == 70) TS(28 + 32 * L); BAR(); if (i == 0) TS(27 + 32 * L); if (i == 70) TS(29 + 32 * L); }
            return i < 112 ? lds16(lds + slot_of(c0 + 13 + i / 14) + ((i & 1) ? 1024 + kh * 13 * 1024 : kh * 15 * 1024) + lane * 16 + ((i % 14) >> 1) * 2048) : v4u{};
          },
          [&](auto I, v4u wf) __attribute__((always_inline)) { constexpr int i = I.v;
            constexpr int k = i >> 1;
            mfma32p(wf, hf[k % 35], acc[i & 1]);
            if constexpr ((i & 1) && k + 35 < 56) hf[k % 35] = ldf(rs, lane, hoff, k + 35);
          });
      TS(30 + 32 * L);
      res_epilogue(acc, res, lds, rs, ws, out, w, lane, g, m, ttB, ntB, 2, active, L == 1, 4 * L + 4, tid);
      TS(31 + 32 * L);
    }
  }
}

extern "C" void kernel_launch(void* const* d_in, const int* in_sizes, int n_in, void* d_out, int out_size, void* d_ws,
                              size_t ws_size, hipStream_t stream) {
  (void)in_sizes; (void)n_in; (void)out_size; (void)ws_size;
  (void)hipFuncSetAttribute((const void*)k_main, hipFuncAttributeMaxDynamicSharedMemorySize, LDS_BYTES);
  k_pack<<<294, 512, 0, stream>>>((const unsigned short*)d_in[0], (const unsigned short*)d_in[1], (const unsigned short*)d_in[2],
                                  (const unsigned short*)d_in[4], (const unsigned short*)d_in[5], (const unsigned short*)d_in[6], (char*)d_ws);
  k_main<<<256, 512, LDS_BYTES, stream>>>((char*)d_ws, (unsigned short*)d_out);
}
```

```cpp
#include <hip/hip_runtime.h>
#include <hip/hip_bf16.h>

typedef unsigned v4u __attribute__((ext_vector_type(4)));
typedef unsigned v2u __attribute__((ext_vector_type(2)));
typedef float f32x4 __attribute__((ext_vector_type(4)));
typedef float f32x2 __attribute__((ext_vector_type(2)));
typedef float f32x16 __attribute__((ext_vector_type(16)));
typedef __bf16 bf16x8 __attribute__((ext_vector_type(8)));
typedef __bf16 bf16x2 __attribute__((ext_vector_type(2)));
#define LAS __attribute__((address_space(3)))
#define GAS __attribute__((address_space(1)))
#define DEV __device__ __forceinline__
#define AI __attribute__((always_inline))

constexpr int S_ = 2048, D_ = 448, F_ = 1792;
constexpr int OFF_FLAG = 0;
constexpr int OFF_WA = 65536;
constexpr int OFF_WB = OFF_WA + 16 * 84 * 1024;
constexpr int OFF_WC = OFF_WB + 7 * 64 * 1024;
constexpr int OFF_WD = OFF_WC + 16 * 196 * 1024;
constexpr int OFF_CS = OFF_WD + 7 * 224 * 1024;
constexpr int XBUF = 64 * 28 * 1024, SBUF = 64 * 14 * 256, OBUF = 64 * 32 * 1024, HBUF = 64 * 112 * 1024;
constexpr int OFF_X = OFF_CS + 16 * 3072;
constexpr int OFF_ST = OFF_X + 4 * XBUF;
constexpr int OFF_O = OFF_ST + 4 * SBUF;
constexpr int OFF_H = OFF_O + 2 * OBUF;
constexpr int WS_BYTES = OFF_H + 2 * HBUF;
constexpr int SLOT = 28 * 1024;
constexpr int L_EX = 5 * SLOT, L_CS = L_EX + 16384, L_RS = L_CS + 3072, LDS_BYTES = L_RS + 512;
static_assert(LDS_BYTES <= 163840, "lds");
constexpr int AUX_SC1 = 16;

DEV unsigned pk(float a, float b) { bf16x2 t = {(__bf16)a, (__bf16)b}; return __builtin_bit_cast(unsigned, t); }
DEV float asf(unsigned u) { return __builtin_bit_cast(float, u); }
DEV float blo(unsigned u) { return __builtin_bit_cast(float, u << 16); }
DEV float bhi(unsigned u) { return __builtin_bit_cast(float, u & 0xffff0000u); }
DEV f32x16 mfma32(v4u a, v4u b, f32x16 c) {
  return __builtin_amdgcn_mfma_f32_32x32x16_bf16(__builtin_bit_cast(bf16x8, a), __builtin_bit_cast(bf16x8, b), c, 0, 0, 0);
}
DEV void mfma32p(v4u a, v4u b, f32x16& c) { c = mfma32(a, b, c); asm volatile("" : "+v"(c)); __builtin_amdgcn_sched_barrier(0); }
DEV f32x4 mfma16(v4u a, v4u b, f32x4 c) {
  return __builtin_amdgcn_mfma_f32_16x16x32_bf16(__builtin_bit_cast(bf16x8, a), __builtin_bit_cast(bf16x8, b), c, 0, 0, 0);
}
DEV float xsum32(float v) {
  auto r = __builtin_amdgcn_permlane32_swap(__builtin_bit_cast(unsigned, v), __builtin_bit_cast(unsigned, v), false, false);
  return __builtin_bit_cast(float, (unsigned)r[0]) + __builtin_bit_cast(float, (unsigned)r[1]);
}
DEV float xmax32(float v) {
  auto r = __builtin_amdgcn_permlane32_swap(__builtin_bit_cast(unsigned, v), __builtin_bit_cast(unsigned, v), false, false);
  return fmaxf(__builtin_bit_cast(float, (unsigned)r[0]), __builtin_bit_cast(float, (unsigned)r[1]));
}
DEV void split3(float v, unsigned& hi, unsigned& mid, unsigned& lo) {
  hi = __builtin_bit_cast(unsigned, v) & 0xffff0000u;
  float r = v - __builtin_bit_cast(float, hi);
  mid = __builtin_bit_cast(unsigned, r) & 0xffff0000u;
  float r2 = r - __builtin_bit_cast(float, mid);
  lo = __builtin_bit_cast(unsigned, r2) & 0xffff0000u;
}
DEV v4u aug_x(float negmean, bool on) {
  unsigned hi, mid, lo; split3(negmean, hi, mid, lo);
  v4u r = {hi | (hi >> 16), mid | (mid >> 16), lo | (hi >> 16), 0u};
  if (!on) r = v4u{0u, 0u, 0u, 0u};
  return r;
}
DEV v4u aug_w(v2u c) { return v4u{c.x, c.x, c.y, 0u}; }

typedef short s16x4 __attribute__((ext_vector_type(4)));
typedef __amdgpu_buffer_rsrc_t rsrc_t;
#define PACK_AUX 16
DEV v2u tr8(LAS char* p) { return __builtin_bit_cast(v2u, __builtin_amdgcn_ds_read_tr16_b64_v4i16((LAS s16x4*)p)); }
__global__ __launch_bounds__(256) void k_pack(const char* __restrict__ x, const char* __restrict__ wqkv, const char* __restrict__ wo,
                                              const char* __restrict__ wg, const char* __restrict__ wu, const char* __restrict__ wd,
                                              char* __restrict__ ws) {
  __shared__ __attribute__((aligned(16))) char sm_[32 * 960 + 64];
  LAS char* sm = (LAS char*)sm_;
  const int u = blockIdx.x, tid = threadIdx.x, w = tid >> 6, lane = tid & 63, n = lane & 31, h = lane >> 5;
  const rsrc_t rs = __builtin_amdgcn_make_buffer_rsrc((void*)ws, 0, WS_BYTES, 0x00020000);
  if (u < 77) {
    if (u < 64) ((unsigned*)(ws + OFF_FLAG))[u * 256 + tid] = 0u;
    const char* src; int ld;
    if (u < 21) { src = wqkv + u * 128; ld = 2688; } else if (u < 49) { src = wg + (u - 21) * 128; ld = 3584; } else { src = wu + (u - 49) * 128; ld = 3584; }
    src += (tid >> 3) * ld + (tid & 7) * 16;
    v4u v[14];
#pragma unroll
    for (int i = 0; i < 14; ++i) v[i] = *(const v4u*)(src + i * 32 * ld);
    float c[8] = {0.f, 0.f, 0.f, 0.f, 0.f, 0.f, 0.f, 0.f};
#pragma unroll
    for (int i = 0; i < 14; ++i)
#pragma unroll
      for (int q = 0; q < 4; ++q) { const unsigned e = v[i][q]; c[2 * q] += blo(e); c[2 * q + 1] += bhi(e); }
    *(LAS f32x4*)(sm + (tid >> 3) * 272 + (tid & 7) * 32) = f32x4{c[0], c[1], c[2], c[3]};
    *(LAS f32x4*)(sm + (tid >> 3) * 272 + (tid & 7) * 32 + 16) = f32x4{c[4], c[5], c[6], c[7]};
    __syncthreads();
    if (tid < 64) {
      float cs = 0.f;
#pragma unroll
      for (int i = 0; i < 32; ++i) cs += *(LAS float*)(sm + i * 272 + tid * 4);
      unsigned hi, mid, lo; split3(cs, hi, mid, lo);
      int o;
      if (u < 21) { const int c_ = u * 64 + tid, t = c_ / 448, r = c_ % 448; o = (r / 28) * 3072 + t * 256 + (r % 28) * 8;
        if (r % 28 < 4) *(v2u*)(ws + OFF_CS + o + 28 * 8) = v2u{0u, 0u}; }
      else { const int f = ((u - 21) % 28) * 64 + tid; o = (f / 112) * 3072 + (3 + (f % 112) / 16) * 256 + ((f & 15) + (u >= 49 ? 16 : 0)) * 8; }
      *(v2u*)(ws + OFF_CS + o) = v2u{(hi >> 16) | mid, (lo >> 16) | hi};
    }
    return;
  }
  const bool isx = u >= 119 && u < 183;
  const int lstride = isx ? 912 : 960;
  {
    v4u v[7];
#pragma unroll
    for (int i = 0; i < 7; ++i) {
      const int c = tid + 256 * i, r = c / 56, cc = c % 56;
      const char* src;
      if (u < 119) { const int q = u - 77; src = wqkv + ((q / 3) * 32 + r) * 2688 + (q % 3) * 896; }
      else if (u < 183) { const int tt = u - 119, g = tt >> 2; src = x + (2 * (128 * (g & 7) + 32 * (tt & 3) + r) + (g >> 3)) * 896; }
      else if (u < 199) src = wo + ((u - 183) * 28 + (r < 28 ? r : 0)) * 896;
      else if (u < 311) { const int q = u - 199; src = (r < 16 ? wg : wu) + ((q >> 2) * 16 + (r & 15)) * 3584 + (q & 3) * 896; }
      else src = wd + ((u - 311) * 32 + r) * 896;
      v[i] = *(const v4u*)(src + cc * 16);
      if (u >= 183 && u < 199 && r >= 28) v[i] = v4u{0u, 0u, 0u, 0u};
    }
#pragma unroll
    for (int i = 0; i < 7; ++i) { const int c = tid + 256 * i; *(LAS v4u*)(sm + (c / 56) * lstride + (c % 56) * 16) = v[i]; }
  }
  __syncthreads();
  if (isx) {
    const int tt = u - 119;
    for (int nt = w; nt < 14; nt += 4) {
      v2u a[4];
#pragma unroll
      for (int i = 0; i < 4; ++i) a[i] = *(LAS v2u*)(sm + n * 912 + nt * 64 + i * 16 + h * 8);
      __builtin_amdgcn_raw_buffer_store_b128(v4u{a[0].x, a[0].y, a[1].x, a[1].y}, rs, OFF_X + (tt * 28 + 2 * nt) * 1024 + lane * 16, 0, PACK_AUX);
      __builtin_amdgcn_raw_buffer_store_b128(v4u{a[2].x, a[2].y, a[3].x, a[3].y}, rs, OFF_X + (tt * 28 + 2 * nt + 1) * 1024 + lane * 16, 0, PACK_AUX);
      float sm1 = 0.f;
#pragma unroll
      for (int i = 0; i < 4; ++i) sm1 += blo(a[i].x) + bhi(a[i].x) + blo(a[i].y) + bhi(a[i].y);
      sm1 = xsum32(sm1);
      const float mu = sm1 * (1.f / 32.f);
      float m2 = 0.f;
#pragma unroll
      for (int i = 0; i < 4; ++i) {
        const float d0 = blo(a[i].x) - mu, d1 = bhi(a[i].x) - mu, d2 = blo(a[i].y) - mu, d3 = bhi(a[i].y) - mu;
        m2 += d0 * d0 + d1 * d1 + d2 * d2 + d3 * d3;
      }
      m2 = xsum32(m2);
      if (h == 0) *(f32x2*)(ws + OFF_ST + (tt * 14 + nt) * 256 + n * 8) = f32x2{sm1, m2};
    }
    return;
  }
  const int q4 = (lane & 15) >> 2, p4 = lane & 3, cb = (lane >> 4) & 1;
  const int nfr = u < 119 ? 32 : 28;
  for (int f = w; f < nfr; f += 4) {
    int row0, colb, dst;
    if (u < 119) { const int q = u - 77, m = f >> 1, sl = f & 1; row0 = 16 * sl; colb = 56 * m + 32 * cb;
      dst = OFF_WA + ((m * 3 + q % 3) * 28 + 2 * (q / 3) + sl) * 1024; }
    else if (u < 199) { const int s = 2 * (u - 183) + (f & 1), nt = f >> 1, kh = s >> 4, ks = s & 15, c = ks / 7, kk = ks % 7; row0 = 16 * (f & 1); colb = 64 * nt + 32 * cb;
      dst = OFF_WB + ((nt >> 1) * 64 + c * 28 + kh * (c < 2 ? 14 : 4) + kk * 2 + (nt & 1)) * 1024; }
    else if (u < 311) { const int q = u - 199; row0 = 16 * cb; colb = 32 * f; dst = OFF_WC + (((q & 3) * 28 + f) * 28 + (q >> 2)) * 1024; }
    else { const int s = 2 * (u - 311) + (f & 1), nt = f >> 1, kh = s / 56, ks = s % 56, c = ks / 7, kk = ks % 7; row0 = 16 * (f & 1); colb = 64 * nt + 32 * cb;
      dst = OFF_WD + ((nt >> 1) * 224 + c * 28 + kh * 14 + kk * 2 + (nt & 1)) * 1024; }
    LAS char* pa = sm + (row0 + 4 * h + q4) * 960 + colb + 8 * p4;
    const v2u lo = tr8(pa), hi = tr8(pa + 8 * 960);
    v4u o = {lo.x, lo.y, hi.x, hi.y};
    if (u < 119 && n >= 28) o = v4u{0u, 0u, 0u, 0u};
    __builtin_amdgcn_raw_buffer_store_b128(o, rs, dst + lane * 16, 0, PACK_AUX);
  }
}

#define BAR() asm volatile("s_waitcnt lgkmcnt(0)\n\ts_barrier" ::: "memory")
#define VMWAIT(n) asm volatile("s_waitcnt vmcnt(" #n ")" ::: "memory")
DEV v4u ld16(rsrc_t rs, int voff, int soff) { return __builtin_amdgcn_raw_buffer_load_b128(rs, voff, soff, AUX_SC1); }
DEV v2u ld8(rsrc_t rs, int voff, int soff) { return __builtin_amdgcn_raw_buffer_load_b64(rs, voff, soff, AUX_SC1); }
DEV void st16(v4u v, rsrc_t rs, int voff, int soff) { __builtin_amdgcn_raw_buffer_store_b128(v, rs, voff + soff, 0, AUX_SC1); }
DEV void st8(v2u v, rsrc_t rs, int voff, int soff) { __builtin_amdgcn_raw_buffer_store_b64(v, rs, voff, soff, AUX_SC1); }
DEV v4u ldf(rsrc_t rs, int lane, int base, int i) { return ld16(rs, lane * 16 + (i & 3) * 1024, base + (i >> 2) * 4096); }
DEV v4u lds16(LAS char* p) { return *(LAS v4u*)p; }

DEV void poll(char* ws, int fi, unsigned want) {
  GAS unsigned* f = (GAS unsigned*)(ws + OFF_FLAG) + fi * 16;
  for (int spin = 0; spin < (1 << 20); ++spin) {
    const unsigned v = __hip_atomic_load(f, __ATOMIC_RELAXED, __HIP_MEMORY_SCOPE_AGENT);
    if (__all(v >= want)) break;
  }
  __builtin_amdgcn_fence(__ATOMIC_ACQUIRE, "wavefront");
}
DEV int fi_x(int g, int T, int lane) { const int i = lane < 14 ? lane : 0; return (g * 16 + 8 * (T >> 1) + (i >> 1)) * 4 + (T & 1) + 2 * (i & 1); }
DEV int fi_k(int g, int kh, int T, int lane) { return (g * 16 + 8 * kh + (lane & 7)) * 4 + T; }
DEV void publish(char* ws, int fi, unsigned val, int lane) {
  VMWAIT(0);
  if (lane == 0) __hip_atomic_store((GAS unsigned*)(ws + OFF_FLAG) + fi * 16, val, __ATOMIC_RELAXED, __HIP_MEMORY_SCOPE_AGENT);
}
template <int N, int TH> DEV void dma(rsrc_t rs, int src, int dst, int hw, int lane16) {
#pragma unroll
  for (int i = 0; i < N; ++i) {
    unsigned keep;
    asm volatile("s_mov_b32 %0, m0\n\ts_mov_b32 m0, %1\n\ts_nop 0\n\tbuffer_load_dwordx4 %2, %3, %4 offen lds\n\ts_mov_b32 m0, %0"
                 : "=&s"(keep) : "s"(dst + (hw * N + i) * 1024), "v"(lane16), "s"(rs), "s"(src + (hw * N + i) * 1024) : "memory");
    if constexpr (TH >= 0) asm volatile("s_waitcnt vmcnt(%0)" :: "i"(TH) : "memory");
  }
}
#define THR 4
#define DQ 36
#define DMA(src, c) dma<7, -1>(rs, src, slot_of(c), hw, l16)
#define DMAT(src, c) dma<7, THR>(rs, src, slot_of(c), hw, l16)
#define OPQ_S(x) asm volatile("" : "+s"(x))
#define OPQ_V(x) asm volatile("" : "+v"(x))
template <int I> struct IC { static constexpr int v = I; };
template <int I, int N, class F> DEV void sfor(F& f) { if constexpr (I < N) { f(IC<I>{}); sfor<I + 1, N>(f); } }
template <int N, int PF, class RD, class MID, class ST> DEV void pipe(RD rd, MID mid, ST st) {
  constexpr int R = PF + 1;
  v4u wb[R];
  auto pro = [&](auto I) __attribute__((always_inline)) { wb[I.v] = rd(I); };
  sfor<0, PF>(pro);
  mid();
  __builtin_amdgcn_sched_barrier(0);
  auto body = [&](auto I) __attribute__((always_inline)) {
    constexpr int i = I.v;
    st(I, wb[i % R]);
    if constexpr (i + PF < N) wb[(i + PF) % R] = rd(IC<i + PF>{});
    asm volatile("" :: "v"(wb[i % R]));
    __builtin_amdgcn_sched_barrier(0);
  };
  sfor<0, N>(body);
}
constexpr int slot_of(int c) { return (c % 5) * SLOT; }
DEV void fin_stats(const v2u (&st)[7], float& mean, float& rstd) {
  float sm = 0.f;
#pragma unroll
  for (int i = 0; i < 7; ++i) sm += asf(st[i].x);
  mean = xsum32(sm) * (1.f / D_);
  float m2 = 0.f;
#pragma unroll
  for (int i = 0; i < 7; ++i) {
    const float d = asf(st[i].x) * (1.f / 32.f) - mean;
    m2 += asf(st[i].y) + 32.f * d * d;
  }
  rstd = __builtin_amdgcn_rsqf(xsum32(m2) * (1.f / D_) + 1e-5f);
}
DEV v4u pack8(const f32x16& a, int o, float sc) {
  return v4u{pk(a[o] * sc, a[o + 1] * sc), pk(a[o + 2] * sc, a[o + 3] * sc), pk(a[o + 4] * sc, a[o + 5] * sc), pk(a[o + 6] * sc, a[o + 7] * sc)};
}
DEV v4u swiglu(const f32x16& a, float rstd) {
  float hv[8];
#pragma unroll
  for (int i = 0; i < 8; ++i) {
    const float g = a[i] * rstd, up = a[8 + i] * rstd;
    const float e = __builtin_amdgcn_exp2f(g * -1.4426950408889634f);
    hv[i] = g * up * __builtin_amdgcn_rcpf(1.f + e);
  }
  return v4u{pk(hv[0], hv[1]), pk(hv[2], hv[3]), pk(hv[4], hv[5]), pk(hv[6], hv[7])};
}
#define TS(i)

DEV void res_epilogue(f32x16 (&acc)[2], v4u (&res)[2], LAS char* lds, rsrc_t rs, char* ws, unsigned short* out, int w, int lane,
                      int g, int m, int tt, int nt, int xb, bool active, bool last, unsigned flagval, bool prebar) {
  LAS char* sx = lds + 3 * SLOT + lane * 16;
  float v[16];
#pragma unroll
  for (int q = 0; q < 8; ++q) { const unsigned r = res[q >> 2][q & 3]; v[2 * q] = acc[0][2 * q] + blo(r); v[2 * q + 1] = acc[0][2 * q + 1] + bhi(r); }
  if (prebar) BAR();
#pragma unroll
  for (int q = 0; q < 4; ++q) *(LAS f32x4*)(sx + w * 4096 + q * 1024) = f32x4{acc[1][4 * q], acc[1][4 * q + 1], acc[1][4 * q + 2], acc[1][4 * q + 3]};
  if (w >= 4) {
#pragma unroll
    for (int q = 0; q < 4; ++q) *(LAS f32x4*)(sx + (w + 4) * 4096 + q * 1024) = f32x4{acc[0][4 * q], acc[0][4 * q + 1], acc[0][4 * q + 2], acc[0][4 * q + 3]};
  }
  BAR();
  if (w >= 4) return;
#pragma unroll
  for (int q = 0; q < 4; ++q) {
    const f32x4 p0 = *(LAS f32x4*)(sx + (w ^ 2) * 4096 + q * 1024), p1 = *(LAS f32x4*)(sx + (w ^ 6) * 4096 + q * 1024), p2 = *(LAS f32x4*)(sx + (w + 8) * 4096 + q * 1024);
#pragma unroll
    for (int e = 0; e < 4; ++e) v[4 * q + e] += p0[e] + p1[e] + p2[e];
  }
#pragma unroll
  for (int q = 0; q < 8; ++q) res[q >> 2][q & 3] = pk(v[2 * q], v[2 * q + 1]);
  if (!active) return;
  if (last) {
    const int s = 2 * (128 * (g & 7) + 32 * (tt & 3) + (lane & 31)) + (g >> 3);
    unsigned short* rowp = out + (size_t)s * D_ + 32 * nt + 8 * (lane >> 5);
#pragma unroll
    for (int q = 0; q < 2; ++q) {
      auto a = __builtin_amdgcn_permlane32_swap(res[q][0], res[q][2], false, false);
      auto b = __builtin_amdgcn_permlane32_swap(res[q][1], res[q][3], false, false);
      *(v4u*)(rowp + 16 * q) = v4u{(unsigned)a[0], (unsigned)b[0], (unsigned)a[1], (unsigned)b[1]};
    }
    return;
  }
  st16(res[0], rs, lane * 16, OFF_X + xb * XBUF + (tt * 28 + 2 * nt) * 1024);
  st16(res[1], rs, lane * 16 + 1024, OFF_X + xb * XBUF + (tt * 28 + 2 * nt) * 1024);
  __builtin_amdgcn_sched_barrier(0);
  float s0 = 0.f, s1 = 0.f, q0 = 0.f, q1 = 0.f;
#pragma unroll
  for (int q = 0; q < 8; ++q) { s0 += v[2 * q]; s1 += v[2 * q + 1]; q0 += v[2 * q] * v[2 * q]; q1 += v[2 * q + 1] * v[2 * q + 1]; }
  const float sm = xsum32(s0 + s1), sq = xsum32(q0 + q1);
  const float m2 = fmaxf(sq - sm * sm * (1.f / 32.f), 0.f);
  if (lane < 32) st8(v2u{__builtin_bit_cast(unsigned, sm), __builtin_bit_cast(unsigned, m2)}, rs, lane * 8, OFF_ST + xb * SBUF + (tt * 14 + nt) * 256);
  publish(ws, (g * 16 + m) * 4 + w, flagval, lane);
}

__global__ __launch_bounds__(512, 2) void k_main(char* ws, unsigned short* out) {
  extern __shared__ __attribute__((aligned(16))) char smem[];
  LAS char* lds = (LAS char*)smem;
  const int tid = threadIdx.x; int lane = tid & 63;
  int w = __builtin_amdgcn_readfirstlane(tid >> 6);
  const int b = blockIdx.x, xcd = b & 7, idx = b >> 3;
  int g = (xcd >> 1) * 4 + (idx >> 3), m = (xcd & 1) * 8 + (idx & 7);
  const int half = m >> 3, j = (m & 7) < 7 ? (m & 7) : 6;
  const bool active = (m & 7) < 7;
  const rsrc_t rs = __builtin_amdgcn_make_buffer_rsrc((void*)ws, 0, WS_BYTES, 0x00020000);
  TS(0);
  v4u res[2];
  res[0] = ld16(rs, lane * 16, OFF_X + ((g * 4 + 2 * half + (w & 1)) * 28 + 2 * (2 * j + ((w >> 1) & 1))) * 1024);
  res[1] = ld16(rs, lane * 16 + 1024, OFF_X + ((g * 4 + 2 * half + (w & 1)) * 28 + 2 * (2 * j + ((w >> 1) & 1))) * 1024);
  if (w >= 4) {
    const int hw = w - 4, l16 = lane * 16, wa = OFF_WA + m * 84 * 1024;
    dma<1, -1>(rs, OFF_CS + m * 3072, L_CS, hw < 3 ? hw : 2, l16);
    DMA(wa, 0); DMA(wa + SLOT, 1); DMA(wa + 2 * SLOT, 2);
  }
  for (int L = 0; L < 2; ++L) {
    OPQ_S(w); OPQ_S(g); OPQ_S(m); OPQ_V(lane);
    const int t = lane & 31, h = lane >> 5;
    const int mw = w & 1, kh = (w >> 1) & 1, par = w >> 2, hw = w & 3, l16 = lane * 16;
    const int wa = OFF_WA + m * 84 * 1024, wb = OFF_WB + j * 64 * 1024, wc = OFF_WC + m * 196 * 1024, wd = OFF_WD + j * 224 * 1024;
    const int ttA = g * 4 + w;
    const int ttB = g * 4 + 2 * half + mw;
    const int ntB = 2 * j + kh;
    const int myflag = (g * 16 + m) * 4 + w;
    LAS char* lb[3] = {lds + lane * 16, lds + 2 * SLOT + lane * 16, lds + 4 * SLOT + lane * 16};
    OPQ_V(lb[1]); OPQ_V(lb[2]);
#define LSL(c) (lb[((c) % 5) >> 1] + (((c) % 5) & 1) * SLOT)
    if (w >= 4) {
      {
        const bool hasprev = (g & 7) > 0;
        const int gp = hasprev ? g - 1 : g, ttp = gp * 4 + 3;
        VMWAIT(0);
        BAR();
        if (L) poll(ws, fi_x(gp, 3, lane), 4);
        const int t16 = lane & 15, gq = lane >> 4, tk = 24 + (t16 & 7), isv = hw >> 1, nh = hw & 1;
        v4u xf[14]; v2u st[4];
#pragma unroll
        for (int i = 0; i < 4; ++i) st[i] = ld8(rs, ((gq + 4 * i < 14 ? gq + 4 * i : 0) * 32 + tk) * 8, OFF_ST + 2 * L * SBUF + ttp * 14 * 256);
#pragma unroll
        for (int i = 0; i < 14; ++i) xf[i] = ld16(rs, (gq >> 1) * 1024 + ((gq & 1) * 32 + tk) * 16, OFF_X + 2 * L * XBUF + (ttp * 28 + 2 * i) * 1024);
        float sm = 0.f;
#pragma unroll
        for (int i = 0; i < 4; ++i) sm += (gq + 4 * i < 14) ? asf(st[i].x) : 0.f;
        sm += __shfl_xor(sm, 16); sm = xsum32(sm);
        const float mean = sm * (1.f / D_);
        float m2 = 0.f;
#pragma unroll
        for (int i = 0; i < 4; ++i) {
          const float d = asf(st[i].x) * (1.f / 32.f) - mean;
          m2 += (gq + 4 * i < 14) ? asf(st[i].y) + 32.f * d * d : 0.f;
        }
        m2 += __shfl_xor(m2, 16); m2 = xsum32(m2);
        const float rstd = __builtin_amdgcn_rsqf(m2 * (1.f / D_) + 1e-5f);
        LAS char* wp = lds + (1 + isv) * SLOT + (gq >> 1) * 1024 + ((gq & 1) * 32 + 16 * nh + t16) * 16;
        const v4u xa = aug_x(-mean, gq == 0), wg_ = aug_w(*(LAS v2u*)(lds + L_CS + (1 + isv) * 256 + (16 * nh + t16) * 8));
        f32x4 a = {0.f, 0.f, 0.f, 0.f};
        if (isv) {
#pragma unroll
          for (int i = 0; i < 14; ++i) a = mfma16(xf[i], lds16(wp + i * 2048), a);
          a = mfma16(xa, wg_, a);
          float r[4];
#pragma unroll
          for (int i = 0; i < 4; ++i) r[i] = __shfl(rstd, 4 * gq + i);
          *(LAS v2u*)(lds + L_EX + 2048 + (16 * nh + t16 + 32 * (gq & 1)) * 16 + 8 * (gq >> 1)) = v2u{pk(a[0] * r[0], a[1] * r[1]), pk(a[2] * r[2], a[3] * r[3])};
        } else {
#pragma unroll
          for (int i = 0; i < 14; ++i) a = mfma16(lds16(wp + i * 2048), xf[i], a);
          a = mfma16(wg_, xa, a);
          *(LAS v2u*)(lds + L_EX + nh * 1024 + (16 + t16 + 32 * (gq & 1)) * 16 + 8 * (gq >> 1)) = v2u{pk(a[0] * rstd, a[1] * rstd), pk(a[2] * rstd, a[3] * rstd)};
        }
      }
      DMA(wb, 3); DMA(wb + SLOT, 4);
      BAR();
      DMA(wc, 5); DMA(wc + SLOT, 6); DMA(wc + 2 * SLOT, 7);
      VMWAIT(21);
    } else {
      constexpr int W0 = 8;
      v4u xf[28]; v2u st[7];
      const int xoff = OFF_X + 2 * L * XBUF + ttA * 28 * 1024, soff = OFF_ST + 2 * L * SBUF + ttA * 14 * 256;
      if (L) { BAR(); TS(3 + 32 * L); poll(ws, fi_x(g, w, lane), 4); }
#pragma unroll
      for (int i = 0; i < W0; ++i) { xf[i] = ldf(rs, lane, xoff, i); __builtin_amdgcn_sched_barrier(0); }
      if (!L) BAR();
      TS(1 + 32 * L);
      f32x16 aq = {}, ak = {}, av = {};
      float mean, rstd; v4u xa; v2u cw[3]; f32x4 rv[4];
      pipe<84, 6>([&](auto I) AI { constexpr int i = I.v; return i < 84 ? lds16(LSL((i + 1) % 3) + (i / 3) * 1024) : v4u{}; },
                  [&]() AI {},
                  [&](auto I, v4u wf) AI { constexpr int i = I.v;
                    if constexpr (i % 3 == 0) mfma32p(wf, xf[i / 3], ak);
                    else if constexpr (i % 3 == 1) mfma32p(xf[i / 3], wf, av);
                    else mfma32p(wf, xf[i / 3], aq);
                    if constexpr (i % 3 == 0 && i / 3 + W0 < 28) xf[i / 3 + W0] = ldf(rs, lane, xoff, i / 3 + W0);
                    if constexpr (i % 3 == 1 && i / 3 < 7) st[i / 3] = ld8(rs, ((7 * h + i / 3) * 32 + t) * 8, soff);
                    if constexpr (i == 62) { fin_stats(st, mean, rstd); OPQ_V(mean); OPQ_V(rstd); }
                    if constexpr (i == 66) { xa = aug_x(-mean, h == 0); OPQ_V(xa); *(LAS float*)(lds + L_RS + w * 128 + t * 4) = rstd; }
                    if constexpr (i >= 66 && i < 69) cw[i - 66] = *(LAS v2u*)(lds + L_CS + (i - 66) * 256 + t * 8);
                    if constexpr (i == 72) mfma32p(aug_w(cw[1]), xa, ak);
                    if constexpr (i == 73) mfma32p(xa, aug_w(cw[2]), av);
                    if constexpr (i == 74) mfma32p(aug_w(cw[0]), xa, aq);
                    if constexpr (i >= 78 && i < 82) rv[i - 78] = *(LAS f32x4*)(lds + L_RS + w * 128 + (8 * (i - 78) + 4 * h) * 4);
                  });
      TS(4 + 32 * L);
      const v4u kf0 = pack8(ak, 0, rstd), kf1 = pack8(ak, 8, rstd);
      v4u vf[2];
#pragma unroll
      for (int q = 0; q < 4; ++q) {
        vf[q >> 1][(q & 1) * 2] = pk(av[4 * q] * rv[q][0], av[4 * q + 1] * rv[q][1]);
        vf[q >> 1][(q & 1) * 2 + 1] = pk(av[4 * q + 2] * rv[q][2], av[4 * q + 3] * rv[q][3]);
      }
      if (w < 3) {
        LAS char* e = lds + L_EX + (w + 1) * 3072 + lane * 16;
        *(LAS v4u*)e = kf0; *(LAS v4u*)(e + 1024) = kf1; *(LAS v4u*)(e + 2048) = vf[1];
      }
      const v4u qf0 = pack8(aq, 0, rstd), qf1 = pack8(aq, 8, rstd);
      f32x16 sc = {};
      mfma32p(kf0, qf0, sc); mfma32p(kf1, qf1, sc);
      TS(5 + 32 * L);
      BAR();
      TS(6 + 32 * L);
      LAS char* e = lds + L_EX + w * 3072 + lane * 16;
      f32x16 sp = {};
      mfma32p(lds16(e), qf0, sp); mfma32p(lds16(e + 1024), qf1, sp);
      const v4u vp = lds16(e + 2048);
      const bool hasprev = w > 0 || (g & 7) > 0;
      const float NEG = -__builtin_inff();
      float p[16], pp[4], mx = NEG;
#pragma unroll
      for (int r = 0; r < 16; ++r) { const int jj = (r & 3) + 8 * (r >> 2) + 4 * h; p[r] = ((unsigned)(t - jj) <= 7u) ? sc[r] : NEG; mx = fmaxf(mx, p[r]); }
#pragma unroll
      for (int r = 0; r < 4; ++r) { const int jj = 24 + r + 4 * h; pp[r] = (hasprev && jj >= t + 25) ? sp[12 + r] : NEG; mx = fmaxf(mx, pp[r]); }
      const float cf = 0.18898223650461362f * 1.4426950408889634f;
      const float mc = -xmax32(mx) * cf;
#pragma unroll
      for (int r = 0; r < 16; ++r) p[r] = __builtin_amdgcn_exp2f(__builtin_fmaf(p[r], cf, mc));
#pragma unroll
      for (int r = 0; r < 4; ++r) pp[r] = __builtin_amdgcn_exp2f(__builtin_fmaf(pp[r], cf, mc));
      const v4u pf0 = {pk(p[0], p[1]), pk(p[2], p[3]), pk(p[4], p[5]), pk(p[6], p[7])};
      const v4u pf1 = {pk(p[8], p[9]), pk(p[10], p[11]), pk(p[12], p[13]), pk(p[14], p[15])};
      const v4u pfp = {0u, 0u, pk(pp[0], pp[1]), pk(pp[2], pp[3])};
      f32x16 o = {};
      mfma32p(vf[0], pf0, o); mfma32p(vf[1], pf1, o); mfma32p(vp, pfp, o);
      float s0 = pp[0] + pp[1], s1 = pp[2] + pp[3];
#pragma unroll
      for (int r = 0; r < 8; ++r) { s0 += p[2 * r]; s1 += p[2 * r + 1]; }
      const float inv = __builtin_amdgcn_rcpf(xsum32(s0 + s1));
      st16(pack8(o, 0, inv), rs, lane * 16, OFF_O + L * OBUF + (ttA * 32 + 2 * m) * 1024);
      st16(pack8(o, 8, inv), rs, lane * 16 + 1024, OFF_O + L * OBUF + (ttA * 32 + 2 * m) * 1024);
      TS(7 + 32 * L);
      publish(ws, myflag, 4 * L + 1, lane);
      TS(8 + 32 * L);
    }
    {
      v4u of[8], wl[2];
      const int ooff = OFF_O + L * OBUF + (ttB * 32 + 16 * kh) * 1024;
#pragma unroll
      for (int i = 0; i < 2; ++i) wl[i] = __builtin_amdgcn_raw_buffer_load_b128(rs, l16, wb + (56 + kh * 4 + 2 * par + (i ^ kh ^ 1)) * 1024, 0);
      BAR();
      TS(11 + 32 * L);
      f32x16 acc[2] = {{}, {}};
      LAS char* wp0 = lds + (3 + par) * SLOT + kh * 15 * 1024 + l16; LAS char* wp1 = lds + (3 + par) * SLOT + 1024 + kh * 13 * 1024 + l16;
      pipe<16, 6>([&](auto I) AI { constexpr int i = I.v;
                    if constexpr (i < 14) return lds16(((i & 1) ? wp0 : wp1) + (i >> 1) * 2048);
                    else return wl[i & 1]; },
                  [&]() AI {
                    poll(ws, fi_k(g, kh, ttB & 3, lane), 4 * L + 1);
                    TS(9 + 32 * L);
#pragma unroll
                    for (int i = 0; i < 8; ++i) { of[i] = i < 7 ? ldf(rs, lane, ooff + par * 7168, i) : ld16(rs, l16, ooff + (14 + par) * 1024); __builtin_amdgcn_sched_barrier(0); }
                  },
                  [&](auto I, v4u wf) AI { constexpr int i = I.v; mfma32p(wf, of[i >> 1], acc[(i & 1) ^ 1]); });
      TS(12 + 32 * L);
      res_epilogue(acc, res, lds, rs, ws, out, w, lane, g, m, ttB, ntB, 2 * L + 1, active, false, 4 * L + 2, true);
      TS(13 + 32 * L);
    }
    v4u wr[28];
    auto phase_d = [&](auto PARC) AI {
      constexpr int PAR = PARC.v, W0 = 14;
      const int hoff = OFF_H + L * HBUF + (ttB * 112 + 56 * kh + 7 * PAR) * 1024;
      v4u hf[W0];
      f32x16 acc[2] = {{}, {}};
      LAS char* db[2];
      pipe<56, 6>(
          [&](auto I) AI { constexpr int i = I.v, r = i / 14;
            if constexpr (i % 14 == 0) {
              if constexpr (r < 2) BAR();
              if constexpr (r == 0) TS(27 + 32 * L);
              if constexpr (PAR && r == 0) DMA(wd + 4 * SLOT, 16);
              if constexpr (PAR && r == 1) DMA(wd + 6 * SLOT, 12);
              constexpr int sl = slot_of(r == 3 ? 12 : 12 + PAR + 2 * r);
              db[1] = lds + sl + kh * 15 * 1024 + l16; db[0] = lds + sl + 1024 + kh * 13 * 1024 + l16;
            }
            if constexpr (i == (PAR ? DQ : 42)) { if constexpr (PAR) VMWAIT(3); TS(28 + 32 * L); BAR(); TS(29 + 32 * L); }
            if constexpr (PAR && r >= 2) return wr[i - 28]; else return lds16(db[i & 1] + ((i % 14) >> 1) * 2048);
          },
          [&]() AI {
            poll(ws, (g * 16 + 8 * kh + PAR + 2 * (lane & 3)) * 4 + (ttB & 3), 4 * L + 3);
            TS(25 + 32 * L);
#pragma unroll
            for (int i = 0; i < W0; ++i) { hf[i] = ldf(rs, lane, hoff + (i / 7) * 14336, i % 7); __builtin_amdgcn_sched_barrier(0); }
          },
          [&](auto I, v4u wf) AI { constexpr int i = I.v;
            constexpr int k = i >> 1;
            mfma32p(wf, hf[k % W0], acc[(i & 1) ^ 1]);
            if constexpr ((i & 1) && k + W0 < 28) hf[k % W0] = ldf(rs, lane, hoff + ((k + W0) / 7) * 14336, (k + W0) % 7);
          });
      TS(30 + 32 * L);
      res_epilogue(acc, res, lds, rs, ws, out, w, lane, g, m, ttB, ntB, 2, active, L == 1, 4 * L + 4, false);
      TS(31 + 32 * L);
    };
    if (w >= 4) {
      BAR();
      DMAT(wc + 3 * SLOT, 8); DMAT(wc + 4 * SLOT, 9);
      VMWAIT(0);
      BAR();
      DMA(wc + 5 * SLOT, 10); DMA(wc + 6 * SLOT, 11); DMA(wd, 12);
#pragma unroll
      for (int i = 0; i < 14; ++i) { wr[i] = __builtin_amdgcn_raw_buffer_load_b128(rs, l16, wd + 5 * SLOT + (kh * 14 + (i & ~1) + ((i & 1) ^ kh ^ 1)) * 1024, 0); __builtin_amdgcn_sched_barrier(0); }
      VMWAIT(21);
      BAR();
      DMA(wd + SLOT, 13); DMA(wd + 2 * SLOT, 14);
      BAR();
      DMA(wd + 3 * SLOT, 15);
#pragma unroll
      for (int i = 0; i < 14; ++i) { wr[14 + i] = __builtin_amdgcn_raw_buffer_load_b128(rs, l16, wd + 7 * SLOT + (kh * 14 + (i & ~1) + ((i & 1) ^ kh ^ 1)) * 1024, 0); __builtin_amdgcn_sched_barrier(0); }
      VMWAIT(21);
      phase_d(IC<1>{});
      if (L == 0) { DMAT(wa, 0); DMAT(wa + SLOT, 1); DMAT(wa + 2 * SLOT, 2); }
    } else {
      constexpr int W0 = 8;
      v4u xf[28]; v2u st[7];
      const int xoff = OFF_X + (2 * L + 1) * XBUF + ttA * 28 * 1024, soff = OFF_ST + (2 * L + 1) * SBUF + ttA * 14 * 256;
      BAR();
      TS(16 + 32 * L);
      const int hoff = OFF_H + L * HBUF + (ttA * 112 + 7 * m) * 1024;
      LAS char* cs = lds + L_CS + 768 + t * 8;
      f32x16 a[4] = {{}, {}, {}, {}};
      float mean, rstd, c1, kk; v4u xa; float hv[8], ev = 0.f, tv = 0.f; v2u cw[7];
      auto epi = [&](const f32x16& acc, int e, int tile) AI {
        const int q = e >> 1;
        if (e & 1) {
          if (q > 0) { hv[q - 1] *= ev; OPQ_V(hv[q - 1]); }
          if (q < 8) { ev = __builtin_amdgcn_exp2f(tv); hv[q] = acc[q] * acc[8 + q]; OPQ_V(ev); OPQ_V(hv[q]); }
        } else if (e < 18) {
          if (q > 0) ev = __builtin_fmaf(ev, kk, kk);
          if (q < 8) { tv = acc[q] * c1; OPQ_V(tv); }
          if (q > 0) { ev = __builtin_amdgcn_rcpf(ev); OPQ_V(ev); }
        } else st16(v4u{pk(hv[0], hv[1]), pk(hv[2], hv[3]), pk(hv[4], hv[5]), pk(hv[6], hv[7])}, rs, lane * 16 + (tile & 3) * 1024, hoff + (tile >> 2) * 4096);
      };
      pipe<196, 5>(
          [&](auto I) AI { constexpr int i = I.v;
            if constexpr (i == 84 || i == 140) { TS(17 + 2 * ((i - 84) / 56) + 32 * L); BAR(); TS(18 + 2 * ((i - 84) / 56) + 32 * L); }
            if constexpr (i == 168) BAR();
            constexpr int tile = i < 84 ? i % 3 : i / 28;
            constexpr int ks = i < 84 ? i / 3 : i % 28;
            return i < 196 ? lds16(LSL(5 + tile) + ks * 1024) : v4u{};
          },
          [&]() AI {
            poll(ws, fi_x(g, w, lane), 4 * L + 2);
            TS(14 + 32 * L);
#pragma unroll
            for (int i = 0; i < W0; ++i) { xf[i] = ldf(rs, lane, xoff, i); __builtin_amdgcn_sched_barrier(0); }
          },
          [&](auto I, v4u wf) AI { constexpr int i = I.v;
            constexpr int tile = i < 84 ? i % 3 : i / 28;
            constexpr int ks = i < 84 ? i / 3 : i % 28;
            if constexpr (i >= 84 && ks == 0) { a[tile & 3] = f32x16{}; mfma32p(aug_w(cw[tile]), xa, a[tile & 3]); }
            mfma32p(wf, xf[ks], a[tile & 3]);
            if constexpr (i < 84 && i % 3 == 0 && i / 3 + W0 < 28) xf[i / 3 + W0] = ldf(rs, lane, xoff, i / 3 + W0);
            if constexpr (i % 3 == 1 && i / 3 < 7) st[i / 3] = ld8(rs, ((7 * h + i / 3) * 32 + t) * 8, soff);
            if constexpr (i >= 70 && i < 73) cw[i - 70] = *(LAS v2u*)(cs + (i - 70) * 256);
            if constexpr (i >= 78 && i % 28 == 22 && i < 170) cw[i / 28 + 1] = *(LAS v2u*)(cs + (i / 28 + 1) * 256);
            if constexpr (i == 62) { fin_stats(st, mean, rstd); OPQ_V(mean); OPQ_V(rstd); }
            if constexpr (i == 66) { xa = aug_x(-mean, h == 0); c1 = rstd * -1.4426950408889634f; kk = __builtin_amdgcn_rcpf(rstd * rstd); OPQ_V(xa); OPQ_V(c1); OPQ_V(kk); }
            if constexpr (i >= 75 && i < 78) mfma32p(aug_w(cw[i - 75]), xa, a[i - 75]);
            if constexpr (i >= 83) epi(a[((i - 83) / 19) & 3], (i - 83) % 19, (i - 83) / 19);
          });
      TS(21 + 32 * L);
      epi(a[1], 18, 5);
      st16(swiglu(a[2], rstd), rs, lane * 16 + 2048, hoff + 4096);
      TS(23 + 32 * L);
      publish(ws, myflag, 4 * L + 3, lane);
      TS(24 + 32 * L);
      phase_d(IC<0>{});
    }
  }
}

extern "C" void kernel_launch(void* const* d_in, const int* in_sizes, int n_in, void* d_out, int out_size, void* d_ws,
                              size_t ws_size, hipStream_t stream) {
  (void)in_sizes; (void)n_in; (void)out_size; (void)ws_size;
  (void)hipFuncSetAttribute((const void*)k_main, hipFuncAttributeMaxDynamicSharedMemorySize, LDS_BYTES);
  k_pack<<<367, 256, 0, stream>>>((const char*)d_in[0], (const char*)d_in[1], (const char*)d_in[2], (const char*)d_in[4],
                                  (const char*)d_in[5], (const char*)d_in[6], (char*)d_ws);
  k_main<<<256, 512, LDS_BYTES, stream>>>((char*)d_ws, (unsigned short*)d_out);
}
```

```cpp
#include <hip/hip_runtime.h>
#include <hip/hip_bf16.h>

typedef unsigned v4u __attribute__((ext_vector_type(4)));
typedef unsigned v2u __attribute__((ext_vector_type(2)));
typedef float f32x4 __attribute__((ext_vector_type(4)));
typedef float f32x2 __attribute__((ext_vector_type(2)));
typedef float f32x16 __attribute__((ext_vector_type(16)));
typedef __bf16 bf16x8 __attribute__((ext_vector_type(8)));
typedef __bf16 bf16x2 __attribute__((ext_vector_type(2)));
#define LAS __attribute__((address_space(3)))
#define GAS __attribute__((address_space(1)))
#define DEV __device__ __forceinline__
#define AI __attribute__((always_inline))

constexpr int S_ = 2048, D_ = 448, F_ = 1792;
constexpr int OFF_FLAG = 0;
constexpr int OFF_PF = 65536, OFF_EP = OFF_PF + 32768;
constexpr int OFF_WA = 102400;
constexpr int OFF_WB = OFF_WA + 16 * 84 * 1024;
constexpr int OFF_WC = OFF_WB + 7 * 64 * 1024;
constexpr int OFF_WD = OFF_WC + 16 * 196 * 1024;
constexpr int OFF_CS = OFF_WD + 7 * 224 * 1024;
constexpr int XBUF = 64 * 28 * 1024, SBUF = 64 * 14 * 256, OBUF = 64 * 32 * 1024, HBUF = 64 * 112 * 1024;
constexpr int OFF_X = OFF_CS + 16 * 3072;
constexpr int OFF_ST = OFF_X + 4 * XBUF;
constexpr int OFF_O = OFF_ST + 4 * SBUF;
constexpr int OFF_H = OFF_O + 2 * OBUF;
constexpr int WS_BYTES = OFF_H + 2 * HBUF;
constexpr int SLOT = 28 * 1024;
constexpr int L_EX = 5 * SLOT, L_CS = L_EX + 16384, L_RS = L_CS + 3072, LDS_BYTES = L_RS + 512;
static_assert(LDS_BYTES <= 163840, "lds");
constexpr int AUX_SC1 = 16;

DEV unsigned pk(float a, float b) { bf16x2 t = {(__bf16)a, (__bf16)b}; return __builtin_bit_cast(unsigned, t); }
DEV float asf(unsigned u) { return __builtin_bit_cast(float, u); }
DEV float blo(unsigned u) { return __builtin_bit_cast(float, u << 16); }
DEV float bhi(unsigned u) { return __builtin_bit_cast(float, u & 0xffff0000u); }
DEV f32x16 mfma32(v4u a, v4u b, f32x16 c) {
  return __builtin_amdgcn_mfma_f32_32x32x16_bf16(__builtin_bit_cast(bf16x8, a), __builtin_bit_cast(bf16x8, b), c, 0, 0, 0);
}
DEV void mfma32p(v4u a, v4u b, f32x16& c) { c = mfma32(a, b, c); asm volatile("" : "+v"(c)); __builtin_amdgcn_sched_barrier(0); }
DEV f32x4 mfma16(v4u a, v4u b, f32x4 c) {
  return __builtin_amdgcn_mfma_f32_16x16x32_bf16(__builtin_bit_cast(bf16x8, a), __builtin_bit_cast(bf16x8, b), c, 0, 0, 0);
}
DEV float xsum32(float v) {
  auto r = __builtin_amdgcn_permlane32_swap(__builtin_bit_cast(unsigned, v), __builtin_bit_cast(unsigned, v), false, false);
  return __builtin_bit_cast(float, (unsigned)r[0]) + __builtin_bit_cast(float, (unsigned)r[1]);
}
DEV float xmax32(float v) {
  auto r = __builtin_amdgcn_permlane32_swap(__builtin_bit_cast(unsigned, v), __builtin_bit_cast(unsigned, v), false, false);
  return fmaxf(__builtin_bit_cast(float, (unsigned)r[0]), __builtin_bit_cast(float, (unsigned)r[1]));
}
DEV void split3(float v, unsigned& hi, unsigned& mid, unsigned& lo) {
  hi = __builtin_bit_cast(unsigned, v) & 0xffff0000u;
  float r = v - __builtin_bit_cast(float, hi);
  mid = __builtin_bit_cast(unsigned, r) & 0xffff0000u;
  float r2 = r - __builtin_bit_cast(float, mid);
  lo = __builtin_bit_cast(unsigned, r2) & 0xffff0000u;
}
DEV v4u aug_x(float negmean, bool on) {
  unsigned hi, mid, lo; split3(negmean, hi, mid, lo);
  v4u r = {hi | (hi >> 16), mid | (mid >> 16), lo | (hi >> 16), 0u};
  if (!on) r = v4u{0u, 0u, 0u, 0u};
  return r;
}
DEV v4u aug_w(v2u c) { return v4u{c.x, c.x, c.y, 0u}; }

typedef short s16x4 __attribute__((ext_vector_type(4)));
typedef __amdgpu_buffer_rsrc_t rsrc_t;
#define BAR() asm volatile("s_waitcnt lgkmcnt(0)\n\ts_barrier" ::: "memory")
#define VMWAIT(n) asm volatile("s_waitcnt vmcnt(" #n ")" ::: "memory")
DEV v4u ld16(rsrc_t rs, int voff, int soff) { return __builtin_amdgcn_raw_buffer_load_b128(rs, voff, soff, AUX_SC1); }
DEV v2u ld8(rsrc_t rs, int voff, int soff) { return __builtin_amdgcn_raw_buffer_load_b64(rs, voff, soff, AUX_SC1); }
DEV void st16(v4u v, rsrc_t rs, int voff, int soff) { __builtin_amdgcn_raw_buffer_store_b128(v, rs, voff + soff, 0, AUX_SC1); }
DEV void st8(v2u v, rsrc_t rs, int voff, int soff) { __builtin_amdgcn_raw_buffer_store_b64(v, rs, voff, soff, AUX_SC1); }
DEV v4u ldf(rsrc_t rs, int lane, int base, int i) { return ld16(rs, lane * 16 + (i & 3) * 1024, base + (i >> 2) * 4096); }
DEV v4u lds16(LAS char* p) { return *(LAS v4u*)p; }

DEV v2u tr8(LAS char* p) { return __builtin_bit_cast(v2u, __builtin_amdgcn_ds_read_tr16_b64_v4i16((LAS s16x4*)p)); }
DEV void pack_unit(int u, int tid, LAS char* sm, rsrc_t rs, const char* x, const char* wqkv, const char* wo, const char* wg,
                   const char* wu, const char* wd) {
  const int w = __builtin_amdgcn_readfirstlane(tid >> 6), lane = tid & 63, n = lane & 31, h = lane >> 5;
  if (u < 0) { BAR(); return; }
  if (u < 77) {
    const char* src; int ld;
    if (u < 21) { src = wqkv + u * 128; ld = 2688; } else if (u < 49) { src = wg + (u - 21) * 128; ld = 3584; } else { src = wu + (u - 49) * 128; ld = 3584; }
    src += (tid >> 3) * ld + (tid & 7) * 16;
    v4u v[14];
#pragma unroll
    for (int i = 0; i < 14; ++i) v[i] = *(const v4u*)(src + i * 32 * ld);
    float c[8] = {0.f, 0.f, 0.f, 0.f, 0.f, 0.f, 0.f, 0.f};
#pragma unroll
    for (int i = 0; i < 14; ++i)
#pragma unroll
      for (int q = 0; q < 4; ++q) { const unsigned e = v[i][q]; c[2 * q] += blo(e); c[2 * q + 1] += bhi(e); }
    *(LAS f32x4*)(sm + (tid >> 3) * 272 + (tid & 7) * 32) = f32x4{c[0], c[1], c[2], c[3]};
    *(LAS f32x4*)(sm + (tid >> 3) * 272 + (tid & 7) * 32 + 16) = f32x4{c[4], c[5], c[6], c[7]};
    BAR();
    if (tid < 64) {
      float cs = 0.f;
#pragma unroll
      for (int i = 0; i < 32; ++i) cs += *(LAS float*)(sm + i * 272 + tid * 4);
      unsigned hi, mid, lo; split3(cs, hi, mid, lo);
      int o;
      if (u < 21) { const int c_ = u * 64 + tid, t = c_ / 448, r = c_ % 448; o = (r / 28) * 3072 + t * 256 + (r % 28) * 8;
        if (r % 28 < 4) st8(v2u{0u, 0u}, rs, OFF_CS + o + 28 * 8, 0); }
      else { const int f = ((u - 21) % 28) * 64 + tid; o = (f / 112) * 3072 + (3 + (f % 112) / 16) * 256 + ((f & 15) + (u >= 49 ? 16 : 0)) * 8; }
      st8(v2u{(hi >> 16) | mid, (lo >> 16) | hi}, rs, OFF_CS + o, 0);
    }
    return;
  }
  const bool isx = u >= 119 && u < 183;
  const int lstride = isx ? 912 : 960;
  int dst0 = 0, dstep = 0, cstep = 128, col0 = 64 * (w >> 1), row0 = 16 * (w & 1);
  {
    const char* src; int ld = 896;
    if (u < 119) { const int q = u - 77; ld = 2688; src = wqkv + ((q / 3) * 32 + 8 * w) * 2688 + (q % 3) * 896;
      dst0 = OFF_WA + (((w >> 1) * 3 + q % 3) * 28 + 2 * (q / 3) + (w & 1)) * 1024; dstep = 168 * 1024; cstep = 112; col0 = 56 * (w >> 1); }
    else if (u < 183) { const int tt = u - 119, g = tt >> 2; ld = 1792; src = x + (2 * (128 * (g & 7) + 32 * (tt & 3) + 8 * w) + (g >> 3)) * 896; }
    else if (u < 199) { const int s = 2 * (u - 183) + (w & 1), kh = s >> 4, ks = s & 15, c = ks / 7; src = wo + ((u - 183) * 28 + 8 * w) * 896;
      dst0 = OFF_WB + (c * 28 + kh * (c < 2 ? 14 : 4) + (ks % 7) * 2 + (w >> 1)) * 1024; dstep = 64 * 1024; }
    else if (u < 311) { const int q = u - 199; ld = 3584; src = (w < 2 ? wg : wu) + ((q >> 2) * 16 + 8 * (w & 1)) * 3584 + (q & 3) * 896;
      dst0 = OFF_WC + (((q & 3) * 28 + w) * 28 + (q >> 2)) * 1024; dstep = 112 * 1024; col0 = 32 * w; }
    else { const int s = 2 * (u - 311) + (w & 1), kh = s / 56, ks = s % 56; src = wd + ((u - 311) * 32 + 8 * w) * 896;
      dst0 = OFF_WD + ((ks / 7) * 28 + kh * 14 + (ks % 7) * 2 + (w >> 1)) * 1024; dstep = 224 * 1024; }
    const bool pad = u >= 183 && u < 199 && w == 3;
    if (lane < 56) {
      v4u v[8];
#pragma unroll
      for (int i = 0; i < 8; ++i) v[i] = *(const v4u*)(src + ((pad && i >= 4) ? 0 : i * ld) + lane * 16);
#pragma unroll
      for (int i = 0; i < 8; ++i) *(LAS v4u*)(sm + (8 * w + i) * lstride + lane * 16) = (pad && i >= 4) ? v4u{0u, 0u, 0u, 0u} : v[i];
    }
  }
  BAR();
  if (isx) {
    const int tt = u - 119;
#pragma unroll
    for (int it = 0; it < 4; ++it) {
      const int nt = w + 4 * it;
      if (nt >= 14) break;
      v2u a[4];
#pragma unroll
      for (int i = 0; i < 4; ++i) a[i] = *(LAS v2u*)(sm + n * 912 + nt * 64 + i * 16 + h * 8);
      __builtin_amdgcn_raw_buffer_store_b128(v4u{a[0].x, a[0].y, a[1].x, a[1].y}, rs, OFF_X + (tt * 28 + 2 * nt) * 1024 + lane * 16, 0, AUX_SC1);
      __builtin_amdgcn_raw_buffer_store_b128(v4u{a[2].x, a[2].y, a[3].x, a[3].y}, rs, OFF_X + (tt * 28 + 2 * nt + 1) * 1024 + lane * 16, 0, AUX_SC1);
      float sm1 = 0.f;
#pragma unroll
      for (int i = 0; i < 4; ++i) sm1 += blo(a[i].x) + bhi(a[i].x) + blo(a[i].y) + bhi(a[i].y);
      sm1 = xsum32(sm1);
      const float mu = sm1 * (1.f / 32.f);
      float m2 = 0.f;
#pragma unroll
      for (int i = 0; i < 4; ++i) {
        const float d0 = blo(a[i].x) - mu, d1 = bhi(a[i].x) - mu, d2 = blo(a[i].y) - mu, d3 = bhi(a[i].y) - mu;
        m2 += d0 * d0 + d1 * d1 + d2 * d2 + d3 * d3;
      }
      m2 = xsum32(m2);
      if (h == 0) st8(v2u{__builtin_bit_cast(unsigned, sm1), __builtin_bit_cast(unsigned, m2)}, rs, OFF_ST + (tt * 14 + nt) * 256 + n * 8, 0);
    }
    return;
  }
  const int q4 = (lane & 15) >> 2, p4 = lane & 3, cb = (lane >> 4) & 1;
  const bool gu = u >= 199 && u < 311;
  LAS char* pa = sm + ((gu ? 16 * cb : row0) + 4 * h + q4) * 960 + col0 + (gu ? 0 : 32 * cb) + 8 * p4;
  v2u lo[8], hi[8];
#pragma unroll
  for (int i = 0; i < 8; ++i) { LAS char* q = pa + ((i < 7 || u < 119) ? i * cstep : 0); lo[i] = tr8(q); hi[i] = tr8(q + 8 * 960); }
  const bool z = u < 119 && n >= 28;
#pragma unroll
  for (int i = 0; i < 8; ++i)
    if (i < 7 || u < 119) st16(z ? v4u{0u, 0u, 0u, 0u} : v4u{lo[i].x, lo[i].y, hi[i].x, hi[i].y}, rs, lane * 16, dst0 + i * dstep);
}

DEV void poll(char* ws, int fi, unsigned want) {
  GAS unsigned* f = (GAS unsigned*)(ws + OFF_FLAG) + fi * 16;
  for (int spin = 0; spin < (1 << 20); ++spin) {
    const unsigned v = __hip_atomic_load(f, __ATOMIC_RELAXED, __HIP_MEMORY_SCOPE_AGENT);
    if (__all(v >= want)) break;
  }
  __builtin_amdgcn_fence(__ATOMIC_ACQUIRE, "wavefront");
}
DEV int fi_x(int g, int T, int lane) { const int i = lane < 14 ? lane : 0; return (g * 16 + 8 * (T >> 1) + (i >> 1)) * 4 + (T & 1) + 2 * (i & 1); }
DEV int fi_k(int g, int kh, int T, int lane) { return (g * 16 + 8 * kh + (lane & 7)) * 4 + T; }
DEV void publish(char* ws, int fi, unsigned val, int lane) {
  VMWAIT(0);
  if (lane == 0) __hip_atomic_store((GAS unsigned*)(ws + OFF_FLAG) + fi * 16, val, __ATOMIC_RELAXED, __HIP_MEMORY_SCOPE_AGENT);
}
template <int N, int TH> DEV void dma(rsrc_t rs, int src, int dst, int hw, int lane16) {
#pragma unroll
  for (int i = 0; i < N; ++i) {
    unsigned keep;
    asm volatile("s_mov_b32 %0, m0\n\ts_mov_b32 m0, %1\n\ts_nop 0\n\tbuffer_load_dwordx4 %2, %3, %4 offen sc1 lds\n\ts_mov_b32 m0, %0"
                 : "=&s"(keep) : "s"(dst + (hw * N + i) * 1024), "v"(lane16), "s"(rs), "s"(src + (hw * N + i) * 1024) : "memory");
    if constexpr (TH >= 0) asm volatile("s_waitcnt vmcnt(%0)" :: "i"(TH) : "memory");
  }
}
#define THR 4
#define DQ 42
#define QPRIO 1
#define DMA(src, c) dma<7, -1>(rs, src, slot_of(c), hw, l16)
#define DMAT(src, c) dma<7, THR>(rs, src, slot_of(c), hw, l16)
#define OPQ_S(x) asm volatile("" : "+s"(x))
#define OPQ_V(x) asm volatile("" : "+v"(x))
template <int I> struct IC { static constexpr int v = I; };
template <int I, int N, class F> DEV void sfor(F& f) { if constexpr (I < N) { f(IC<I>{}); sfor<I + 1, N>(f); } }
template <int N, int PF, class RD, class MID, class ST> DEV void pipe(RD rd, MID mid, ST st) {
  constexpr int R = PF + 1;
  v4u wb[R];
  auto pro = [&](auto I) __attribute__((always_inline)) { wb[I.v] = rd(I); };
  sfor<0, PF>(pro);
  mid();
  __builtin_amdgcn_sched_barrier(0);
  auto body = [&](auto I) __attribute__((always_inline)) {
    constexpr int i = I.v;
    st(I, wb[i % R]);
    if constexpr (i + PF < N) wb[(i + PF) % R] = rd(IC<i + PF>{});
    asm volatile("" :: "v"(wb[i % R]));
    __builtin_amdgcn_sched_barrier(0);
  };
  sfor<0, N>(body);
}
constexpr int slot_of(int c) { return (c % 5) * SLOT; }
DEV void fin_stats(const v2u (&st)[7], float& mean, float& rstd) {
  float sm = 0.f;
#pragma unroll
  for (int i = 0; i < 7; ++i) sm += asf(st[i].x);
  mean = xsum32(sm) * (1.f / D_);
  float m2 = 0.f;
#pragma unroll
  for (int i = 0; i < 7; ++i) {
    const float d = asf(st[i].x) * (1.f / 32.f) - mean;
    m2 += asf(st[i].y) + 32.f * d * d;
  }
  rstd = __builtin_amdgcn_rsqf(xsum32(m2) * (1.f / D_) + 1e-5f);
}
DEV v4u pack8(const f32x16& a, int o, float sc) {
  return v4u{pk(a[o] * sc, a[o + 1] * sc), pk(a[o + 2] * sc, a[o + 3] * sc), pk(a[o + 4] * sc, a[o + 5] * sc), pk(a[o + 6] * sc, a[o + 7] * sc)};
}
DEV v4u swiglu(const f32x16& a, float rstd) {
  float hv[8];
#pragma unroll
  for (int i = 0; i < 8; ++i) {
    const float g = a[i] * rstd, up = a[8 + i] * rstd;
    const float e = __builtin_amdgcn_exp2f(g * -1.4426950408889634f);
    hv[i] = g * up * __builtin_amdgcn_rcpf(1.f + e);
  }
  return v4u{pk(hv[0], hv[1]), pk(hv[2], hv[3]), pk(hv[4], hv[5]), pk(hv[6], hv[7])};
}
#define TS(i)

template <bool W8> DEV void res_epilogue(f32x16 (&acc)[2], v4u (&res)[2], LAS char* lds, rsrc_t rs, char* ws, unsigned short* out, int w, int lane,
                      int g, int m, int tt, int nt, int xb, bool active, bool last, unsigned flagval) {
  LAS char* sx = lds + (W8 ? 3 * SLOT : L_EX) + lane * 16;
  float v[16];
#pragma unroll
  for (int q = 0; q < 8; ++q) { const unsigned r = res[q >> 2][q & 3]; v[2 * q] = acc[0][2 * q] + blo(r); v[2 * q + 1] = acc[0][2 * q + 1] + bhi(r); }
#pragma unroll
  for (int q = 0; q < 4; ++q) *(LAS f32x4*)(sx + w * 4096 + q * 1024) = f32x4{acc[1][4 * q], acc[1][4 * q + 1], acc[1][4 * q + 2], acc[1][4 * q + 3]};
  if (W8 && w >= 4) {
#pragma unroll
    for (int q = 0; q < 4; ++q) *(LAS f32x4*)(sx + (w + 4) * 4096 + q * 1024) = f32x4{acc[0][4 * q], acc[0][4 * q + 1], acc[0][4 * q + 2], acc[0][4 * q + 3]};
  }
  BAR();
  if (W8 && w >= 4) return;
#pragma unroll
  for (int q = 0; q < 4; ++q) {
    f32x4 p0 = *(LAS f32x4*)(sx + (w ^ 2) * 4096 + q * 1024);
    if (W8) p0 += *(LAS f32x4*)(sx + (w ^ 6) * 4096 + q * 1024) + *(LAS f32x4*)(sx + (w + 8) * 4096 + q * 1024);
#pragma unroll
    for (int e = 0; e < 4; ++e) v[4 * q + e] += p0[e];
  }
#pragma unroll
  for (int q = 0; q < 8; ++q) res[q >> 2][q & 3] = pk(v[2 * q], v[2 * q + 1]);
  if (!active) return;
  if (last) {
    const int s = 2 * (128 * (g & 7) + 32 * (tt & 3) + (lane & 31)) + (g >> 3);
    unsigned short* rowp = out + (size_t)s * D_ + 32 * nt + 8 * (lane >> 5);
#pragma unroll
    for (int q = 0; q < 2; ++q) {
      auto a = __builtin_amdgcn_permlane32_swap(res[q][0], res[q][2], false, false);
      auto b = __builtin_amdgcn_permlane32_swap(res[q][1], res[q][3], false, false);
      *(v4u*)(rowp + 16 * q) = v4u{(unsigned)a[0], (unsigned)b[0], (unsigned)a[1], (unsigned)b[1]};
    }
    return;
  }
  st16(res[0], rs, lane * 16, OFF_X + xb * XBUF + (tt * 28 + 2 * nt) * 1024);
  st16(res[1], rs, lane * 16 + 1024, OFF_X + xb * XBUF + (tt * 28 + 2 * nt) * 1024);
  __builtin_amdgcn_sched_barrier(0);
  float s0 = 0.f, s1 = 0.f, q0 = 0.f, q1 = 0.f;
#pragma unroll
  for (int q = 0; q < 8; ++q) { s0 += v[2 * q]; s1 += v[2 * q + 1]; q0 += v[2 * q] * v[2 * q]; q1 += v[2 * q + 1] * v[2 * q + 1]; }
  const float sm = xsum32(s0 + s1), sq = xsum32(q0 + q1);
  const float m2 = fmaxf(sq - sm * sm * (1.f / 32.f), 0.f);
  if (lane < 32) st8(v2u{__builtin_bit_cast(unsigned, sm), __builtin_bit_cast(unsigned, m2)}, rs, lane * 8, OFF_ST + xb * SBUF + (tt * 14 + nt) * 256);
  publish(ws, (g * 16 + m) * 4 + w, flagval, lane);
}

__global__ __launch_bounds__(512, 2) void k_main(const char* x, const char* wqkv, const char* wo, const char* wg_, const char* wu_,
                                                  const char* wd_, char* ws, unsigned short* out) {
  extern __shared__ __attribute__((aligned(16))) char smem[];
  LAS char* lds = (LAS char*)smem;
  const int tid = threadIdx.x; int lane = tid & 63;
  int w = __builtin_amdgcn_readfirstlane(tid >> 6);
  const int b = blockIdx.x, xcd = b & 7, idx = b >> 3;
  int g = (xcd >> 1) * 4 + (idx >> 3), m = (xcd & 1) * 8 + (idx & 7);
  const int half = m >> 3, j = (m & 7) < 7 ? (m & 7) : 6;
  const bool active = (m & 7) < 7;
  const rsrc_t rs = __builtin_amdgcn_make_buffer_rsrc((void*)ws, 0, WS_BYTES, 0x00020000);
  TS(0);
  {
    GAS unsigned* ep = (GAS unsigned*)(ws + OFF_EP);
    unsigned tag = 0;
    if (w == 0) tag = __hip_atomic_load(ep, __ATOMIC_RELAXED, __HIP_MEMORY_SCOPE_AGENT) + 1u;
    if (w < 4 && lane == 0)
      __hip_atomic_store((GAS unsigned*)(ws + OFF_FLAG) + ((g * 16 + m) * 4 + w) * 16, 0u, __ATOMIC_RELAXED, __HIP_MEMORY_SCOPE_AGENT);
    pack_unit(w < 4 ? 111 + b : (b < 111 ? b : -1), tid & 255, lds + (w >> 2) * 32768, rs, x, wqkv, wo, wg_, wu_, wd_);
    VMWAIT(0);
    BAR();
    TS(2);
    if (w == 0) {
      if (lane == 0) __hip_atomic_store((GAS unsigned*)(ws + OFF_PF) + b * 32, tag, __ATOMIC_RELAXED, __HIP_MEMORY_SCOPE_AGENT);
      for (int spin = 0; spin < (1 << 20); ++spin) {
        unsigned v[4];
#pragma unroll
        for (int i = 0; i < 4; ++i) v[i] = __builtin_amdgcn_raw_buffer_load_b32(rs, lane * 128, OFF_PF + i * 8192, AUX_SC1);
        if (__all(v[0] == tag && v[1] == tag && v[2] == tag && v[3] == tag)) break;
        asm volatile("s_sleep 2" ::: "memory");
      }
      if (b == 0 && lane == 0) __hip_atomic_store(ep, tag + 1u, __ATOMIC_RELAXED, __HIP_MEMORY_SCOPE_AGENT);
    }
    BAR();
  }
  v4u res[2];
  res[0] = ld16(rs, lane * 16, OFF_X + ((g * 4 + 2 * half + (w & 1)) * 28 + 2 * (2 * j + ((w >> 1) & 1))) * 1024);
  res[1] = ld16(rs, lane * 16 + 1024, OFF_X + ((g * 4 + 2 * half + (w & 1)) * 28 + 2 * (2 * j + ((w >> 1) & 1))) * 1024);
  if (w >= 4) {
    const int hw = w - 4, l16 = lane * 16, wa = OFF_WA + m * 84 * 1024;
    dma<1, -1>(rs, OFF_CS + m * 3072, L_CS, hw < 3 ? hw : 2, l16);
    DMA(wa, 0); DMA(wa + SLOT, 1); DMA(wa + 2 * SLOT, 2);
  }
  for (int L = 0; L < 2; ++L) {
    OPQ_S(w); OPQ_S(g); OPQ_S(m); OPQ_V(lane);
    const int t = lane & 31, h = lane >> 5;
    const int mw = w & 1, kh = (w >> 1) & 1, par = w >> 2, hw = w & 3, l16 = lane * 16;
    const int wa = OFF_WA + m * 84 * 1024, wb = OFF_WB + j * 64 * 1024, wc = OFF_WC + m * 196 * 1024, wd = OFF_WD + j * 224 * 1024;
    const int ttA = g * 4 + w;
    const int ttB = g * 4 + 2 * half + mw;
    const int ntB = 2 * j + kh;
    const int myflag = (g * 16 + m) * 4 + w;
    LAS char* lb[3] = {lds + lane * 16, lds + 2 * SLOT + lane * 16, lds + 4 * SLOT + lane * 16};
    OPQ_V(lb[1]); OPQ_V(lb[2]);
#define LSL(c) (lb[((c) % 5) >> 1] + (((c) % 5) & 1) * SLOT)
    if (w >= 4) {
      {
        const bool hasprev = (g & 7) > 0;
        const int gp = hasprev ? g - 1 : g, ttp = gp * 4 + 3;
        VMWAIT(0);
        BAR();
        if (L) poll(ws, fi_x(gp, 3, lane), 4);
        const int t16 = lane & 15, gq = lane >> 4, tk = 24 + (t16 & 7), isv = hw >> 1, nh = hw & 1;
        v4u xf[14]; v2u st[4];
#pragma unroll
        for (int i = 0; i < 4; ++i) st[i] = ld8(rs, ((gq + 4 * i < 14 ? gq + 4 * i : 0) * 32 + tk) * 8, OFF_ST + 2 * L * SBUF + ttp * 14 * 256);
#pragma unroll
        for (int i = 0; i < 14; ++i) xf[i] = ld16(rs, (gq >> 1) * 1024 + ((gq & 1) * 32 + tk) * 16, OFF_X + 2 * L * XBUF + (ttp * 28 + 2 * i) * 1024);
        float sm = 0.f;
#pragma unroll
        for (int i = 0; i < 4; ++i) sm += (gq + 4 * i < 14) ? asf(st[i].x) : 0.f;
        sm += __shfl_xor(sm, 16); sm = xsum32(sm);
        const float mean = sm * (1.f / D_);
        float m2 = 0.f;
#pragma unroll
        for (int i = 0; i < 4; ++i) {
          const float d = asf(st[i].x) * (1.f / 32.f) - mean;
          m2 += (gq + 4 * i < 14) ? asf(st[i].y) + 32.f * d * d : 0.f;
        }
        m2 += __shfl_xor(m2, 16); m2 = xsum32(m2);
        const float rstd = __builtin_amdgcn_rsqf(m2 * (1.f / D_) + 1e-5f);
        LAS char* wp = lds + (1 + isv) * SLOT + (gq >> 1) * 1024 + ((gq & 1) * 32 + 16 * nh + t16) * 16;
        const v4u xa = aug_x(-mean, gq == 0), wg_ = aug_w(*(LAS v2u*)(lds + L_CS + (1 + isv) * 256 + (16 * nh + t16) * 8));
        f32x4 a = {0.f, 0.f, 0.f, 0.f};
        if (isv) {
#pragma unroll
          for (int i = 0; i < 14; ++i) a = mfma16(xf[i], lds16(wp + i * 2048), a);
          a = mfma16(xa, wg_, a);
          float r[4];
#pragma unroll
          for (int i = 0; i < 4; ++i) r[i] = __shfl(rstd, 4 * gq + i);
          *(LAS v2u*)(lds + L_EX + 2048 + (16 * nh + t16 + 32 * (gq & 1)) * 16 + 8 * (gq >> 1)) = v2u{pk(a[0] * r[0], a[1] * r[1]), pk(a[2] * r[2], a[3] * r[3])};
        } else {
#pragma unroll
          for (int i = 0; i < 14; ++i) a = mfma16(lds16(wp + i * 2048), xf[i], a);
          a = mfma16(wg_, xa, a);
          *(LAS v2u*)(lds + L_EX + nh * 1024 + (16 + t16 + 32 * (gq & 1)) * 16 + 8 * (gq >> 1)) = v2u{pk(a[0] * rstd, a[1] * rstd), pk(a[2] * rstd, a[3] * rstd)};
        }
      }
      DMA(wb, 3); DMA(wb + SLOT, 4);
      BAR();
      DMA(wc, 5); DMA(wc + SLOT, 6); DMA(wc + 2 * SLOT, 7);
      VMWAIT(21);
    } else {
      constexpr int W0 = 8;
      v4u xf[28]; v2u st[7];
      const int xoff = OFF_X + 2 * L * XBUF + ttA * 28 * 1024, soff = OFF_ST + 2 * L * SBUF + ttA * 14 * 256;
      if (L) poll(ws, fi_x(g, w, lane), 4);
      TS(3 + 32 * L);
#pragma unroll
      for (int i = 0; i < W0; ++i) { xf[i] = ldf(rs, lane, xoff, i); __builtin_amdgcn_sched_barrier(0); }
      BAR();
      TS(1 + 32 * L);
      f32x16 aq = {}, ak = {}, av = {};
      float mean, rstd; v4u xa; v2u cw[3]; f32x4 rv[4];
      pipe<84, 6>([&](auto I) AI { constexpr int i = I.v; return i < 84 ? lds16(LSL((i + 1) % 3) + (i / 3) * 1024) : v4u{}; },
                  [&]() AI {},
                  [&](auto I, v4u wf) AI { constexpr int i = I.v;
                    if constexpr (i % 3 == 0) mfma32p(wf, xf[i / 3], ak);
                    else if constexpr (i % 3 == 1) mfma32p(xf[i / 3], wf, av);
                    else mfma32p(wf, xf[i / 3], aq);
                    if constexpr (i % 3 == 0 && i / 3 + W0 < 28) xf[i / 3 + W0] = ldf(rs, lane, xoff, i / 3 + W0);
                    if constexpr (i % 3 == 1 && i / 3 < 7) st[i / 3] = ld8(rs, ((7 * h + i / 3) * 32 + t) * 8, soff);
                    if constexpr (i == 62) { fin_stats(st, mean, rstd); OPQ_V(mean); OPQ_V(rstd); }
                    if constexpr (i == 66) { xa = aug_x(-mean, h == 0); OPQ_V(xa); *(LAS float*)(lds + L_RS + w * 128 + t * 4) = rstd; }
                    if constexpr (i >= 66 && i < 69) cw[i - 66] = *(LAS v2u*)(lds + L_CS + (i - 66) * 256 + t * 8);
                    if constexpr (i == 72) mfma32p(aug_w(cw[1]), xa, ak);
                    if constexpr (i == 73) mfma32p(xa, aug_w(cw[2]), av);
                    if constexpr (i == 74) mfma32p(aug_w(cw[0]), xa, aq);
                    if constexpr (i >= 78 && i < 82) rv[i - 78] = *(LAS f32x4*)(lds + L_RS + w * 128 + (8 * (i - 78) + 4 * h) * 4);
                  });
      TS(4 + 32 * L);
      const v4u kf0 = pack8(ak, 0, rstd), kf1 = pack8(ak, 8, rstd);
      v4u vf[2];
#pragma unroll
      for (int q = 0; q < 4; ++q) {
        vf[q >> 1][(q & 1) * 2] = pk(av[4 * q] * rv[q][0], av[4 * q + 1] * rv[q][1]);
        vf[q >> 1][(q & 1) * 2 + 1] = pk(av[4 * q + 2] * rv[q][2], av[4 * q + 3] * rv[q][3]);
      }
      if (w < 3) {
        LAS char* e = lds + L_EX + (w + 1) * 3072 + lane * 16;
        *(LAS v4u*)e = kf0; *(LAS v4u*)(e + 1024) = kf1; *(LAS v4u*)(e + 2048) = vf[1];
      }
      const v4u qf0 = pack8(aq, 0, rstd), qf1 = pack8(aq, 8, rstd);
      f32x16 sc = {};
      mfma32p(kf0, qf0, sc); mfma32p(kf1, qf1, sc);
      TS(5 + 32 * L);
      BAR();
      TS(6 + 32 * L);
      LAS char* e = lds + L_EX + w * 3072 + lane * 16;
      f32x16 sp = {};
      mfma32p(lds16(e), qf0, sp); mfma32p(lds16(e + 1024), qf1, sp);
      const v4u vp = lds16(e + 2048);
      const bool hasprev = w > 0 || (g & 7) > 0;
      const float NEG = -__builtin_inff();
      float p[16], pp[4], mx = NEG;
#pragma unroll
      for (int r = 0; r < 16; ++r) { const int jj = (r & 3) + 8 * (r >> 2) + 4 * h; p[r] = ((unsigned)(t - jj) <= 7u) ? sc[r] : NEG; mx = fmaxf(mx, p[r]); }
#pragma unroll
      for (int r = 0; r < 4; ++r) { const int jj = 24 + r + 4 * h; pp[r] = (hasprev && jj >= t + 25) ? sp[12 + r] : NEG; mx = fmaxf(mx, pp[r]); }
      const float cf = 0.18898223650461362f * 1.4426950408889634f;
      const float mc = -xmax32(mx) * cf;
#pragma unroll
      for (int r = 0; r < 16; ++r) p[r] = __builtin_amdgcn_exp2f(__builtin_fmaf(p[r], cf, mc));
#pragma unroll
      for (int r = 0; r < 4; ++r) pp[r] = __builtin_amdgcn_exp2f(__builtin_fmaf(pp[r], cf, mc));
      const v4u pf0 = {pk(p[0], p[1]), pk(p[2], p[3]), pk(p[4], p[5]), pk(p[6], p[7])};
      const v4u pf1 = {pk(p[8], p[9]), pk(p[10], p[11]), pk(p[12], p[13]), pk(p[14], p[15])};
      const v4u pfp = {0u, 0u, pk(pp[0], pp[1]), pk(pp[2], pp[3])};
      f32x16 o = {};
      mfma32p(vf[0], pf0, o); mfma32p(vf[1], pf1, o); mfma32p(vp, pfp, o);
      float s0 = pp[0] + pp[1], s1 = pp[2] + pp[3];
#pragma unroll
      for (int r = 0; r < 8; ++r) { s0 += p[2 * r]; s1 += p[2 * r + 1]; }
      const float inv = __builtin_amdgcn_rcpf(xsum32(s0 + s1));
      st16(pack8(o, 0, inv), rs, lane * 16, OFF_O + L * OBUF + (ttA * 32 + 2 * m) * 1024);
      st16(pack8(o, 8, inv), rs, lane * 16 + 1024, OFF_O + L * OBUF + (ttA * 32 + 2 * m) * 1024);
      TS(7 + 32 * L);
      publish(ws, myflag, 4 * L + 1, lane);
      TS(8 + 32 * L);
    }
    if (w >= 4) {
      BAR();
      BAR();
      VMWAIT(0);
    } else {
      constexpr int W0 = 8;
      v4u of[16];
      const int ooff = OFF_O + L * OBUF + (ttB * 32 + 16 * kh) * 1024;
      v4u wl[4];
#pragma unroll
      for (int i = 0; i < 4; ++i) wl[i] = __builtin_amdgcn_raw_buffer_load_b128(rs, lane * 16 + (i & 2) * 1024, OFF_WB + (j * 64 + 56 + kh * 4 + ((i & 1) ^ kh ^ 1)) * 1024, AUX_SC1);
      BAR();
      TS(11 + 32 * L);
      f32x16 acc[2] = {{}, {}};
      LAS char* wp0 = lds + slot_of(3) + kh * 15 * 1024 + lane * 16; LAS char* wp1 = lds + slot_of(3) + 1024 + kh * 13 * 1024 + lane * 16;
      OPQ_V(wp0); OPQ_V(wp1);
      pipe<32, 6>([&](auto I) AI { constexpr int i = I.v;
                    if constexpr (i < 28) return lds16(((i & 1) ? wp0 : wp1) + (i / 14) * SLOT + ((i % 14) >> 1) * 2048);
                    else return wl[i & 3]; },
                  [&]() AI {
                    poll(ws, fi_k(g, kh, ttB & 3, lane), 4 * L + 1);
                    TS(9 + 32 * L);
#pragma unroll
                    for (int i = 0; i < W0; ++i) { of[i] = ldf(rs, lane, ooff, i); __builtin_amdgcn_sched_barrier(0); }
                  },
                  [&](auto I, v4u wf) AI { constexpr int i = I.v;
                    mfma32p(wf, of[i >> 1], acc[(i & 1) ^ 1]);
                    if constexpr (i + W0 < 16) of[i + W0] = ldf(rs, lane, ooff, i + W0);
                  });
      TS(12 + 32 * L);
      res_epilogue<false>(acc, res, lds, rs, ws, out, w, lane, g, m, ttB, ntB, 2 * L + 1, active, false, 4 * L + 2);
      TS(13 + 32 * L);
    }
    v4u wr[28];
    auto phase_d = [&](auto PARC) AI {
      constexpr int PAR = PARC.v, W0 = 14;
      const int hoff = OFF_H + L * HBUF + (ttB * 112 + 56 * kh + 7 * PAR) * 1024;
      v4u hf[W0];
      f32x16 acc[2] = {{}, {}};
      LAS char* db[2];
      pipe<56, 6>(
          [&](auto I) AI { constexpr int i = I.v, r = i / 14;
            if constexpr (i % 14 == 0) {
              if constexpr (r < 2) BAR();
              if constexpr (r == 0) TS(27 + 32 * L);
              if constexpr (PAR && r == 0) DMA(wd + 4 * SLOT, 16);
              if constexpr (PAR && r == 1) DMA(wd + 6 * SLOT, 12);
              constexpr int sl = slot_of(r == 3 ? 12 : 12 + PAR + 2 * r);
              db[1] = lds + sl + kh * 15 * 1024 + l16; db[0] = lds + sl + 1024 + kh * 13 * 1024 + l16;
            }
            if constexpr (i == (PAR ? DQ : 42)) { if constexpr (PAR) VMWAIT(3); TS(28 + 32 * L); BAR(); TS(29 + 32 * L); }
            if constexpr (PAR && r >= 2) return wr[i - 28]; else return lds16(db[i & 1] + ((i % 14) >> 1) * 2048);
          },
          [&]() AI {
            poll(ws, (g * 16 + 8 * kh + PAR + 2 * (lane & 3)) * 4 + (ttB & 3), 4 * L + 3);
            TS(25 + 32 * L);
#pragma unroll
            for (int i = 0; i < W0; ++i) { hf[i] = ldf(rs, lane, hoff + (i / 7) * 14336, i % 7); __builtin_amdgcn_sched_barrier(0); }
          },
          [&](auto I, v4u wf) AI { constexpr int i = I.v;
            constexpr int k = i >> 1;
            mfma32p(wf, hf[k % W0], acc[(i & 1) ^ 1]);
            if constexpr ((i & 1) && k + W0 < 28) hf[k % W0] = ldf(rs, lane, hoff + ((k + W0) / 7) * 14336, (k + W0) % 7);
          });
      TS(30 + 32 * L);
      if constexpr (!PAR) { if (L == 0) { DMA(wa, 0); DMA(wa + SLOT, 1); } }
      res_epilogue<true>(acc, res, lds, rs, ws, out, w, lane, g, m, ttB, ntB, 2, active, L == 1, 4 * L + 4);
      TS(31 + 32 * L);
    };
    if (w >= 4) {
      BAR();
      DMAT(wc + 3 * SLOT, 8); DMAT(wc + 4 * SLOT, 9);
#pragma unroll
      for (int i = 0; i < 14; ++i) { wr[14 + i] = __builtin_amdgcn_raw_buffer_load_b128(rs, l16, wd + 7 * SLOT + (kh * 14 + (i & ~1) + ((i & 1) ^ kh ^ 1)) * 1024, AUX_SC1); __builtin_amdgcn_sched_barrier(0); }
      VMWAIT(14);
      BAR();
      DMA(wc + 5 * SLOT, 10); DMA(wc + 6 * SLOT, 11); DMA(wd, 12);
#pragma unroll
      for (int i = 0; i < 14; ++i) { wr[i] = __builtin_amdgcn_raw_buffer_load_b128(rs, l16, wd + 5 * SLOT + (kh * 14 + (i & ~1) + ((i & 1) ^ kh ^ 1)) * 1024, AUX_SC1); __builtin_amdgcn_sched_barrier(0); }
      VMWAIT(21);
      BAR();
      DMA(wd + SLOT, 13); DMA(wd + 2 * SLOT, 14);
      BAR();
      DMA(wd + 3 * SLOT, 15);
      VMWAIT(7);
      __builtin_amdgcn_s_setprio(QPRIO);
      phase_d(IC<1>{});
      __builtin_amdgcn_s_setprio(0);
      if (L == 0) DMAT(wa + 2 * SLOT, 2);
    } else {
      constexpr int W0 = 8;
      v4u xf[28]; v2u st[7];
      const int xoff = OFF_X + (2 * L + 1) * XBUF + ttA * 28 * 1024, soff = OFF_ST + (2 * L + 1) * SBUF + ttA * 14 * 256;
      BAR();
      TS(16 + 32 * L);
      const int hoff = OFF_H + L * HBUF + (ttA * 112 + 7 * m) * 1024;
      LAS char* cs = lds + L_CS + 768 + t * 8;
      f32x16 a[4] = {{}, {}, {}, {}};
      float mean, rstd, c1, kk; v4u xa; float hv[8], ev = 0.f, tv = 0.f; v2u cw[7];
      auto epi = [&](const f32x16& acc, int e, int tile) AI {
        const int q = e >> 1;
        if (e & 1) {
          if (q > 0) { hv[q - 1] *= ev; OPQ_V(hv[q - 1]); }
          if (q < 8) { ev = __builtin_amdgcn_exp2f(tv); hv[q] = acc[q] * acc[8 + q]; OPQ_V(ev); OPQ_V(hv[q]); }
        } else if (e < 18) {
          if (q > 0) ev = __builtin_fmaf(ev, kk, kk);
          if (q < 8) { tv = acc[q] * c1; OPQ_V(tv); }
          if (q > 0) { ev = __builtin_amdgcn_rcpf(ev); OPQ_V(ev); }
        } else st16(v4u{pk(hv[0], hv[1]), pk(hv[2], hv[3]), pk(hv[4], hv[5]), pk(hv[6], hv[7])}, rs, lane * 16 + (tile & 3) * 1024, hoff + (tile >> 2) * 4096);
      };
      pipe<196, 5>(
          [&](auto I) AI { constexpr int i = I.v;
            if constexpr (i == 84 || i == 140) { TS(17 + 2 * ((i - 84) / 56) + 32 * L); BAR(); TS(18 + 2 * ((i - 84) / 56) + 32 * L); }
            if constexpr (i == 168) BAR();
            constexpr int tile = i < 84 ? i % 3 : i / 28;
            constexpr int ks = i < 84 ? i / 3 : i % 28;
            return i < 196 ? lds16(LSL(5 + tile) + ks * 1024) : v4u{};
          },
          [&]() AI {
            poll(ws, fi_x(g, w, lane), 4 * L + 2);
            TS(14 + 32 * L);
#pragma unroll
            for (int i = 0; i < W0; ++i) { xf[i] = ldf(rs, lane, xoff, i); __builtin_amdgcn_sched_barrier(0); }
          },
          [&](auto I, v4u wf) AI { constexpr int i = I.v;
            constexpr int tile = i < 84 ? i % 3 : i / 28;
            constexpr int ks = i < 84 ? i / 3 : i % 28;
            if constexpr (i >= 84 && ks == 0) { a[tile & 3] = f32x16{}; mfma32p(aug_w(cw[tile]), xa, a[tile & 3]); }
            mfma32p(wf, xf[ks], a[tile & 3]);
            if constexpr (i < 84 && i % 3 == 0 && i / 3 + W0 < 28) xf[i / 3 + W0] = ldf(rs, lane, xoff, i / 3 + W0);
            if constexpr (i % 3 == 1 && i / 3 < 7) st[i / 3] = ld8(rs, ((7 * h + i / 3) * 32 + t) * 8, soff);
            if constexpr (i >= 70 && i < 73) cw[i - 70] = *(LAS v2u*)(cs + (i - 70) * 256);
            if constexpr (i >= 78 && i % 28 == 22 && i < 170) cw[i / 28 + 1] = *(LAS v2u*)(cs + (i / 28 + 1) * 256);
            if constexpr (i == 62) { fin_stats(st, mean, rstd); OPQ_V(mean); OPQ_V(rstd); }
            if constexpr (i == 66) { xa = aug_x(-mean, h == 0); c1 = rstd * -1.4426950408889634f; kk = __builtin_amdgcn_rcpf(rstd * rstd); OPQ_V(xa); OPQ_V(c1); OPQ_V(kk); }
            if constexpr (i >= 75 && i < 78) mfma32p(aug_w(cw[i - 75]), xa, a[i - 75]);
            if constexpr (i >= 83) epi(a[((i - 83) / 19) & 3], (i - 83) % 19, (i - 83) / 19);
          });
      TS(21 + 32 * L);
      epi(a[1], 18, 5);
      st16(swiglu(a[2], rstd), rs, lane * 16 + 2048, hoff + 4096);
      TS(23 + 32 * L);
      publish(ws, myflag, 4 * L + 3, lane);
      TS(24 + 32 * L);
      phase_d(IC<0>{});
    }
  }
}

extern "C" void kernel_launch(void* const* d_in, const int* in_sizes, int n_in, void* d_out, int out_size, void* d_ws,
                              size_t ws_size, hipStream_t stream) {
  (void)in_sizes; (void)n_in; (void)out_size; (void)ws_size;
  (void)hipFuncSetAttribute((const void*)k_main, hipFuncAttributeMaxDynamicSharedMemorySize, LDS_BYTES);
  k_main<<<256, 512, LDS_BYTES, stream>>>((const char*)d_in[0], (const char*)d_in[1], (const char*)d_in[2], (const char*)d_in[4],
                                          (const char*)d_in[5], (const char*)d_in[6], (char*)d_ws, (unsigned short*)d_out);
}
```

```cpp
#include <hip/hip_runtime.h>
#include <hip/hip_bf16.h>

typedef unsigned v4u __attribute__((ext_vector_type(4)));
typedef unsigned v2u __attribute__((ext_vector_type(2)));
typedef float f32x4 __attribute__((ext_vector_type(4)));
typedef float f32x2 __attribute__((ext_vector_type(2)));
typedef float f32x16 __attribute__((ext_vector_type(16)));
typedef __bf16 bf16x8 __attribute__((ext_vector_type(8)));
typedef __bf16 bf16x2 __attribute__((ext_vector_type(2)));
#define LAS __attribute__((address_space(3)))
#define GAS __attribute__((address_space(1)))
#define DEV __device__ __forceinline__
#define AI __attribute__((always_inline))

constexpr int S_ = 2048, D_ = 448, F_ = 1792;
constexpr int OFF_FLAG = 0;
constexpr int OFF_PF = 65536, OFF_EP = OFF_PF + 65536;
constexpr int OFF_WA = 135168;
constexpr int OFF_WB = OFF_WA + 16 * 84 * 1024;
constexpr int OFF_WC = OFF_WB + 7 * 64 * 1024;
constexpr int OFF_WD = OFF_WC + 16 * 196 * 1024;
constexpr int OFF_CS = OFF_WD + 7 * 224 * 1024;
constexpr int XBUF = 64 * 28 * 1024, SBUF = 64 * 14 * 256, OBUF = 64 * 32 * 1024, HBUF = 64 * 112 * 1024;
constexpr int OFF_X = OFF_CS + 16 * 3072;
constexpr int OFF_ST = OFF_X + 4 * XBUF;
constexpr int OFF_O = OFF_ST + 4 * SBUF;
constexpr int OFF_H = OFF_O + 2 * OBUF;
constexpr int WS_BYTES = OFF_H + 2 * HBUF;
constexpr int SLOT = 28 * 1024;
constexpr int L_EX = 5 * SLOT, L_CS = L_EX + 16384, L_RS = L_CS + 3072, LDS_BYTES = L_RS + 512;
static_assert(LDS_BYTES <= 163840, "lds");
constexpr int AUX_SC1 = 16;

DEV unsigned pk(float a, float b) { bf16x2 t = {(__bf16)a, (__bf16)b}; return __builtin_bit_cast(unsigned, t); }
DEV float asf(unsigned u) { return __builtin_bit_cast(float, u); }
DEV float blo(unsigned u) { return __builtin_bit_cast(float, u << 16); }
DEV float bhi(unsigned u) { return __builtin_bit_cast(float, u & 0xffff0000u); }
DEV f32x16 mfma32(v4u a, v4u b, f32x16 c) {
  return __builtin_amdgcn_mfma_f32_32x32x16_bf16(__builtin_bit_cast(bf16x8, a), __builtin_bit_cast(bf16x8, b), c, 0, 0, 0);
}
DEV void mfma32p(v4u a, v4u b, f32x16& c) { c = mfma32(a, b, c); asm volatile("" : "+v"(c)); __builtin_amdgcn_sched_barrier(0); }
DEV f32x4 mfma16(v4u a, v4u b, f32x4 c) {
  return __builtin_amdgcn_mfma_f32_16x16x32_bf16(__builtin_bit_cast(bf16x8, a), __builtin_bit_cast(bf16x8, b), c, 0, 0, 0);
}
DEV float xsum32(float v) {
  auto r = __builtin_amdgcn_permlane32_swap(__builtin_bit_cast(unsigned, v), __builtin_bit_cast(unsigned, v), false, false);
  return __builtin_bit_cast(float, (unsigned)r[0]) + __builtin_bit_cast(float, (unsigned)r[1]);
}
DEV float xmax32(float v) {
  auto r = __builtin_amdgcn_permlane32_swap(__builtin_bit_cast(unsigned, v), __builtin_bit_cast(unsigned, v), false, false);
  return fmaxf(__builtin_bit_cast(float, (unsigned)r[0]), __builtin_bit_cast(float, (unsigned)r[1]));
}
DEV void split3(float v, unsigned& hi, unsigned& mid, unsigned& lo) {
  hi = __builtin_bit_cast(unsigned, v) & 0xffff0000u;
  float r = v - __builtin_bit_cast(float, hi);
  mid = __builtin_bit_cast(unsigned, r) & 0xffff0000u;
  float r2 = r - __builtin_bit_cast(float, mid);
  lo = __builtin_bit_cast(unsigned, r2) & 0xffff0000u;
}
DEV v4u aug_x(float negmean, bool on) {
  unsigned hi, mid, lo; split3(negmean, hi, mid, lo);
  v4u r = {hi | (hi >> 16), mid | (mid >> 16), lo | (hi >> 16), 0u};
  if (!on) r = v4u{0u, 0u, 0u, 0u};
  return r;
}
DEV v4u aug_w(v2u c) { return v4u{c.x, c.x, c.y, 0u}; }

typedef short s16x4 __attribute__((ext_vector_type(4)));
typedef __amdgpu_buffer_rsrc_t rsrc_t;
#define BAR() asm volatile("s_waitcnt lgkmcnt(0)\n\ts_barrier" ::: "memory")
#define VMWAIT(n) asm volatile("s_waitcnt vmcnt(" #n ")" ::: "memory")
DEV v4u ld16(rsrc_t rs, int voff, int soff) { return __builtin_amdgcn_raw_buffer_load_b128(rs, voff, soff, AUX_SC1); }
DEV v2u ld8(rsrc_t rs, int voff, int soff) { return __builtin_amdgcn_raw_buffer_load_b64(rs, voff, soff, AUX_SC1); }
DEV void st16(v4u v, rsrc_t rs, int voff, int soff) { __builtin_amdgcn_raw_buffer_store_b128(v, rs, voff + soff, 0, AUX_SC1); }
DEV void st8(v2u v, rsrc_t rs, int voff, int soff) { __builtin_amdgcn_raw_buffer_store_b64(v, rs, voff, soff, AUX_SC1); }
DEV v4u ldf(rsrc_t rs, int lane, int base, int i) { return ld16(rs, lane * 16 + (i & 3) * 1024, base + (i >> 2) * 4096); }
DEV v4u lds16(LAS char* p) { return *(LAS v4u*)p; }

DEV v2u tr8(LAS char* p) { return __builtin_bit_cast(v2u, __builtin_amdgcn_ds_read_tr16_b64_v4i16((LAS s16x4*)p)); }
template <class MID> DEV void pack_unit(int u, int tid, LAS char* sm, rsrc_t rs, const char* x, const char* wqkv, const char* wo, const char* wg,
                   const char* wu, const char* wd, MID mid) {
  const int w = __builtin_amdgcn_readfirstlane(tid >> 6), lane = tid & 63, n = lane & 31, h = lane >> 5;
  if (u < 0) { BAR(); mid(); return; }
  if (u < 77) {
    const char* src; int ld;
    if (u < 21) { src = wqkv + u * 128; ld = 2688; } else if (u < 49) { src = wg + (u - 21) * 128; ld = 3584; } else { src = wu + (u - 49) * 128; ld = 3584; }
    src += (tid >> 3) * ld + (tid & 7) * 16;
    v4u v[14];
#pragma unroll
    for (int i = 0; i < 14; ++i) v[i] = *(const v4u*)(src + i * 32 * ld);
    float c[8] = {0.f, 0.f, 0.f, 0.f, 0.f, 0.f, 0.f, 0.f};
#pragma unroll
    for (int i = 0; i < 14; ++i)
#pragma unroll
      for (int q = 0; q < 4; ++q) { const unsigned e = v[i][q]; c[2 * q] += blo(e); c[2 * q + 1] += bhi(e); }
    *(LAS f32x4*)(sm + (tid >> 3) * 272 + (tid & 7) * 32) = f32x4{c[0], c[1], c[2], c[3]};
    *(LAS f32x4*)(sm + (tid >> 3) * 272 + (tid & 7) * 32 + 16) = f32x4{c[4], c[5], c[6], c[7]};
    BAR(); mid();
    if (tid < 64) {
      float cs = 0.f;
#pragma unroll
      for (int i = 0; i < 32; ++i) cs += *(LAS float*)(sm + i * 272 + tid * 4);
      unsigned hi, mid, lo; split3(cs, hi, mid, lo);
      int o;
      if (u < 21) { const int c_ = u * 64 + tid, t = c_ / 448, r = c_ % 448; o = (r / 28) * 3072 + t * 256 + (r % 28) * 8;
        if (r % 28 < 4) st8(v2u{0u, 0u}, rs, OFF_CS + o + 28 * 8, 0); }
      else { const int f = ((u - 21) % 28) * 64 + tid; o = (f / 112) * 3072 + (3 + (f % 112) / 16) * 256 + ((f & 15) + (u >= 49 ? 16 : 0)) * 8; }
      st8(v2u{(hi >> 16) | mid, (lo >> 16) | hi}, rs, OFF_CS + o, 0);
    }
    return;
  }
  const bool isx = u >= 119 && u < 183;
  const int lstride = isx ? 912 : 960;
  int dst0 = 0, dstep = 0, cstep = 128, col0 = 64 * (w >> 1), row0 = 16 * (w & 1);
  {
    const char* src; int ld = 896;
    if (u < 119) { const int q = u - 77; ld = 2688; src = wqkv + ((q / 3) * 32 + 8 * w) * 2688 + (q % 3) * 896;
      dst0 = OFF_WA + (((w >> 1) * 3 + q % 3) * 28 + 2 * (q / 3) + (w & 1)) * 1024; dstep = 168 * 1024; cstep = 112; col0 = 56 * (w >> 1); }
    else if (u < 183) { const int tt = u - 119, g = tt >> 2; ld = 1792; src = x + (2 * (128 * (g & 7) + 32 * (tt & 3) + 8 * w) + (g >> 3)) * 896; }
    else if (u < 199) { const int s = 2 * (u - 183) + (w & 1), kh = s >> 4, ks = s & 15, c = ks / 7; src = wo + ((u - 183) * 28 + 8 * w) * 896;
      dst0 = OFF_WB + (c * 28 + kh * (c < 2 ? 14 : 4) + (ks % 7) * 2 + (w >> 1)) * 1024; dstep = 64 * 1024; }
    else if (u < 311) { const int q = u - 199; ld = 3584; src = (w < 2 ? wg : wu) + ((q >> 2) * 16 + 8 * (w & 1)) * 3584 + (q & 3) * 896;
      dst0 = OFF_WC + (((q & 3) * 28 + w) * 28 + (q >> 2)) * 1024; dstep = 112 * 1024; col0 = 32 * w; }
    else { const int s = 2 * (u - 311) + (w & 1), kh = s / 56, ks = s % 56; src = wd + ((u - 311) * 32 + 8 * w) * 896;
      dst0 = OFF_WD + ((ks / 7) * 28 + kh * 14 + (ks % 7) * 2 + (w >> 1)) * 1024; dstep = 224 * 1024; }
    const bool pad = u >= 183 && u < 199 && w == 3;
    if (lane < 56) {
      v4u v[8];
#pragma unroll
      for (int i = 0; i < 8; ++i) v[i] = *(const v4u*)(src + ((pad && i >= 4) ? 0 : i * ld) + lane * 16);
#pragma unroll
      for (int i = 0; i < 8; ++i) *(LAS v4u*)(sm + (8 * w + i) * lstride + lane * 16) = (pad && i >= 4) ? v4u{0u, 0u, 0u, 0u} : v[i];
    }
  }
  BAR(); mid();
  if (isx) {
    const int tt = u - 119;
#pragma unroll
    for (int it = 0; it < 4; ++it) {
      const int nt = w + 4 * it;
      if (nt >= 14) break;
      v2u a[4];
#pragma unroll
      for (int i = 0; i < 4; ++i) a[i] = *(LAS v2u*)(sm + n * 912 + nt * 64 + i * 16 + h * 8);
      __builtin_amdgcn_raw_buffer_store_b128(v4u{a[0].x, a[0].y, a[1].x, a[1].y}, rs, OFF_X + (tt * 28 + 2 * nt) * 1024 + lane * 16, 0, AUX_SC1);
      __builtin_amdgcn_raw_buffer_store_b128(v4u{a[2].x, a[2].y, a[3].x, a[3].y}, rs, OFF_X + (tt * 28 + 2 * nt + 1) * 1024 + lane * 16, 0, AUX_SC1);
      float sm1 = 0.f;
#pragma unroll
      for (int i = 0; i < 4; ++i) sm1 += blo(a[i].x) + bhi(a[i].x) + blo(a[i].y) + bhi(a[i].y);
      sm1 = xsum32(sm1);
      const float mu = sm1 * (1.f / 32.f);
      float m2 = 0.f;
#pragma unroll
      for (int i = 0; i < 4; ++i) {
        const float d0 = blo(a[i].x) - mu, d1 = bhi(a[i].x) - mu, d2 = blo(a[i].y) - mu, d3 = bhi(a[i].y) - mu;
        m2 += d0 * d0 + d1 * d1 + d2 * d2 + d3 * d3;
      }
      m2 = xsum32(m2);
      if (h == 0) st8(v2u{__builtin_bit_cast(unsigned, sm1), __builtin_bit_cast(unsigned, m2)}, rs, OFF_ST + (tt * 14 + nt) * 256 + n * 8, 0);
    }
    return;
  }
  const int q4 = (lane & 15) >> 2, p4 = lane & 3, cb = (lane >> 4) & 1;
  const bool gu = u >= 199 && u < 311;
  LAS char* pa = sm + ((gu ? 16 * cb : row0) + 4 * h + q4) * 960 + col0 + (gu ? 0 : 32 * cb) + 8 * p4;
  v2u lo[8], hi[8];
#pragma unroll
  for (int i = 0; i < 8; ++i) { LAS char* q = pa + ((i < 7 || u < 119) ? i * cstep : 0); lo[i] = tr8(q); hi[i] = tr8(q + 8 * 960); }
  const bool z = u < 119 && n >= 28;
#pragma unroll
  for (int i = 0; i < 8; ++i)
    if (i < 7 || u < 119) st16(z ? v4u{0u, 0u, 0u, 0u} : v4u{lo[i].x, lo[i].y, hi[i].x, hi[i].y}, rs, lane * 16, dst0 + i * dstep);
}

DEV void poll(char* ws, int fi, unsigned want) {
  GAS unsigned* f = (GAS unsigned*)(ws + OFF_FLAG) + fi * 16;
  for (int spin = 0; spin < (1 << 20); ++spin) {
    const unsigned v = __hip_atomic_load(f, __ATOMIC_RELAXED, __HIP_MEMORY_SCOPE_AGENT);
    if (__all(v >= want)) break;
  }
  __builtin_amdgcn_fence(__ATOMIC_ACQUIRE, "wavefront");
}
DEV int fi_x(int g, int T, int lane) { const int i = lane < 14 ? lane : 0; return (g * 16 + 8 * (T >> 1) + (i >> 1)) * 4 + (T & 1) + 2 * (i & 1); }
DEV int fi_k(int g, int kh, int T, int lane) { return (g * 16 + 8 * kh + (lane & 7)) * 4 + T; }
DEV void publish(char* ws, int fi, unsigned val, int lane) {
  VMWAIT(0);
  if (lane == 0) __hip_atomic_store((GAS unsigned*)(ws + OFF_FLAG) + fi * 16, val, __ATOMIC_RELAXED, __HIP_MEMORY_SCOPE_AGENT);
}
template <int N, int TH> DEV void dma(rsrc_t rs, int src, int dst, int hw, int lane16) {
#pragma unroll
  for (int i = 0; i < N; ++i) {
    unsigned keep;
    asm volatile("s_mov_b32 %0, m0\n\ts_mov_b32 m0, %1\n\ts_nop 0\n\tbuffer_load_dwordx4 %2, %3, %4 offen sc1 lds\n\ts_mov_b32 m0, %0"
                 : "=&s"(keep) : "s"(dst + (hw * N + i) * 1024), "v"(lane16), "s"(rs), "s"(src + (hw * N + i) * 1024) : "memory");
    if constexpr (TH >= 0) asm volatile("s_waitcnt vmcnt(%0)" :: "i"(TH) : "memory");
  }
}
#define THR 4
#define DQ 42
#define QPRIO 1
#define QDELAY 30
#define DMA(src, c) dma<7, -1>(rs, src, slot_of(c), hw, l16)
#define DMAT(src, c) dma<7, THR>(rs, src, slot_of(c), hw, l16)
#define OPQ_S(x) asm volatile("" : "+s"(x))
#define OPQ_V(x) asm volatile("" : "+v"(x))
template <int I> struct IC { static constexpr int v = I; };
template <int I, int N, class F> DEV void sfor(F& f) { if constexpr (I < N) { f(IC<I>{}); sfor<I + 1, N>(f); } }
template <int N, int PF, class RD, class MID, class ST> DEV void pipe(RD rd, MID mid, ST st) {
  constexpr int R = PF + 1;
  v4u wb[R];
  auto pro = [&](auto I) __attribute__((always_inline)) { wb[I.v] = rd(I); };
  sfor<0, PF>(pro);
  mid();
  __builtin_amdgcn_sched_barrier(0);
  auto body = [&](auto I) __attribute__((always_inline)) {
    constexpr int i = I.v;
    st(I, wb[i % R]);
    if constexpr (i + PF < N) wb[(i + PF) % R] = rd(IC<i + PF>{});
    asm volatile("" :: "v"(wb[i % R]));
    __builtin_amdgcn_sched_barrier(0);
  };
  sfor<0, N>(body);
}
constexpr int slot_of(int c) { return (c % 5) * SLOT; }
DEV void fin_stats(const v2u (&st)[7], float& mean, float& rstd) {
  float sm = 0.f;
#pragma unroll
  for (int i = 0; i < 7; ++i) sm += asf(st[i].x);
  mean = xsum32(sm) * (1.f / D_);
  float m2 = 0.f;
#pragma unroll
  for (int i = 0; i < 7; ++i) {
    const float d = asf(st[i].x) * (1.f / 32.f) - mean;
    m2 += asf(st[i].y) + 32.f * d * d;
  }
  rstd = __builtin_amdgcn_rsqf(xsum32(m2) * (1.f / D_) + 1e-5f);
}
DEV v4u pack8(const f32x16& a, int o, float sc) {
  return v4u{pk(a[o] * sc, a[o + 1] * sc), pk(a[o + 2] * sc, a[o + 3] * sc), pk(a[o + 4] * sc, a[o + 5] * sc), pk(a[o + 6] * sc, a[o + 7] * sc)};
}
DEV v4u swiglu(const f32x16& a, float rstd) {
  float hv[8];
#pragma unroll
  for (int i = 0; i < 8; ++i) {
    const float g = a[i] * rstd, up = a[8 + i] * rstd;
    const float e = __builtin_amdgcn_exp2f(g * -1.4426950408889634f);
    hv[i] = g * up * __builtin_amdgcn_rcpf(1.f + e);
  }
  return v4u{pk(hv[0], hv[1]), pk(hv[2], hv[3]), pk(hv[4], hv[5]), pk(hv[6], hv[7])};
}
#define TS(i)

template <bool W8> DEV void res_epilogue(f32x16 (&acc)[2], v4u (&res)[2], LAS char* lds, rsrc_t rs, char* ws, unsigned short* out, int w, int lane,
                      int g, int m, int tt, int nt, int xb, bool active, bool last, unsigned flagval) {
  LAS char* sx = lds + (W8 ? 3 * SLOT : L_EX) + lane * 16;
  float v[16];
#pragma unroll
  for (int q = 0; q < 8; ++q) { const unsigned r = res[q >> 2][q & 3]; v[2 * q] = acc[0][2 * q] + blo(r); v[2 * q + 1] = acc[0][2 * q + 1] + bhi(r); }
#pragma unroll
  for (int q = 0; q < 4; ++q) *(LAS f32x4*)(sx + w * 4096 + q * 1024) = f32x4{acc[1][4 * q], acc[1][4 * q + 1], acc[1][4 * q + 2], acc[1][4 * q + 3]};
  if (W8 && w >= 4) {
#pragma unroll
    for (int q = 0; q < 4; ++q) *(LAS f32x4*)(sx + (w + 4) * 4096 + q * 1024) = f32x4{acc[0][4 * q], acc[0][4 * q + 1], acc[0][4 * q + 2], acc[0][4 * q + 3]};
  }
  BAR();
  if (W8 && w >= 4) return;
#pragma unroll
  for (int q = 0; q < 4; ++q) {
    f32x4 p0 = *(LAS f32x4*)(sx + (w ^ 2) * 4096 + q * 1024);
    if (W8) p0 += *(LAS f32x4*)(sx + (w ^ 6) * 4096 + q * 1024) + *(LAS f32x4*)(sx + (w + 8) * 4096 + q * 1024);
#pragma unroll
    for (int e = 0; e < 4; ++e) v[4 * q + e] += p0[e];
  }
#pragma unroll
  for (int q = 0; q < 8; ++q) res[q >> 2][q & 3] = pk(v[2 * q], v[2 * q + 1]);
  if (!active) return;
  if (last) {
    const int s = 2 * (128 * (g & 7) + 32 * (tt & 3) + (lane & 31)) + (g >> 3);
    unsigned short* rowp = out + (size_t)s * D_ + 32 * nt + 8 * (lane >> 5);
#pragma unroll
    for (int q = 0; q < 2; ++q) {
      auto a = __builtin_amdgcn_permlane32_swap(res[q][0], res[q][2], false, false);
      auto b = __builtin_amdgcn_permlane32_swap(res[q][1], res[q][3], false, false);
      *(v4u*)(rowp + 16 * q) = v4u{(unsigned)a[0], (unsigned)b[0], (unsigned)a[1], (unsigned)b[1]};
    }
    return;
  }
  st16(res[0], rs, lane * 16, OFF_X + xb * XBUF + (tt * 28 + 2 * nt) * 1024);
  st16(res[1], rs, lane * 16 + 1024, OFF_X + xb * XBUF + (tt * 28 + 2 * nt) * 1024);
  __builtin_amdgcn_sched_barrier(0);
  float s0 = 0.f, s1 = 0.f, q0 = 0.f, q1 = 0.f;
#pragma unroll
  for (int q = 0; q < 8; ++q) { s0 += v[2 * q]; s1 += v[2 * q + 1]; q0 += v[2 * q] * v[2 * q]; q1 += v[2 * q + 1] * v[2 * q + 1]; }
  const float sm = xsum32(s0 + s1), sq = xsum32(q0 + q1);
  const float m2 = fmaxf(sq - sm * sm * (1.f / 32.f), 0.f);
  if (lane < 32) st8(v2u{__builtin_bit_cast(unsigned, sm), __builtin_bit_cast(unsigned, m2)}, rs, lane * 8, OFF_ST + xb * SBUF + (tt * 14 + nt) * 256);
  publish(ws, (g * 16 + m) * 4 + w, flagval, lane);
}

__global__ __launch_bounds__(512, 2) void k_main(const char* x, const char* wqkv, const char* wo, const char* wg_, const char* wu_,
                                                  const char* wd_, char* ws, unsigned short* out) {
  extern __shared__ __attribute__((aligned(16))) char smem[];
  LAS char* lds = (LAS char*)smem;
  const int tid = threadIdx.x; int lane = tid & 63;
  int w = __builtin_amdgcn_readfirstlane(tid >> 6);
  const int b = blockIdx.x, xcd = b & 7, idx = b >> 3;
  int g = (xcd >> 1) * 4 + (idx >> 3), m = (xcd & 1) * 8 + (idx & 7);
  const int half = m >> 3, j = (m & 7) < 7 ? (m & 7) : 6;
  const bool active = (m & 7) < 7;
  const rsrc_t rs = __builtin_amdgcn_make_buffer_rsrc((void*)ws, 0, WS_BYTES, 0x00020000);
  TS(0);
  const unsigned tag = __hip_atomic_load((GAS unsigned*)(ws + OFF_EP), __ATOMIC_RELAXED, __HIP_MEMORY_SCOPE_AGENT) + 1u;
  auto poll_setup = [&](int base, auto NC) AI {
    for (int spin = 0; spin < (1 << 20); ++spin) {
      bool ok = true;
#pragma unroll
      for (int i = 0; i < NC.v; ++i) ok &= __builtin_amdgcn_raw_buffer_load_b32(rs, lane * 128, base + i * 8192, AUX_SC1) == tag;
      if (__all(ok)) break;
      asm volatile("s_sleep 2" ::: "memory");
    }
    __builtin_amdgcn_fence(__ATOMIC_ACQUIRE, "wavefront");
  };
  v4u res[2];
  if (w < 4) {
    if (lane == 0) __hip_atomic_store((GAS unsigned*)(ws + OFF_FLAG) + ((g * 16 + m) * 4 + w) * 16, 0u, __ATOMIC_RELAXED, __HIP_MEMORY_SCOPE_AGENT);
    pack_unit(b < 21 ? b : b < 127 ? b + 56 : -1, tid, lds, rs, x, wqkv, wo, wg_, wu_, wd_, []() {});
    VMWAIT(0);
    BAR();
    TS(2);
    if (w == 0) {
      if (lane == 0) __hip_atomic_store((GAS unsigned*)(ws + OFF_PF) + b * 32, tag, __ATOMIC_RELAXED, __HIP_MEMORY_SCOPE_AGENT);
      poll_setup(OFF_PF, IC<2>{});
    }
    BAR();
    res[0] = ld16(rs, lane * 16, OFF_X + ((g * 4 + 2 * half + (w & 1)) * 28 + 2 * (2 * j + (w >> 1))) * 1024);
    res[1] = ld16(rs, lane * 16 + 1024, OFF_X + ((g * 4 + 2 * half + (w & 1)) * 28 + 2 * (2 * j + (w >> 1))) * 1024);
  } else {
    res[0] = res[1] = v4u{0u, 0u, 0u, 0u};
    pack_unit(b < 16 ? -1 : b < 200 ? 167 + b : b - 179, tid & 255, lds + 3 * SLOT, rs, x, wqkv, wo, wg_, wu_, wd_, [&]() AI { BAR(); if (b >= 127) __builtin_amdgcn_s_sleep(QDELAY); });
    VMWAIT(0);
    BAR();
    if (tid == 256) __hip_atomic_store((GAS unsigned*)(ws + OFF_PF + 32768) + b * 32, tag, __ATOMIC_RELAXED, __HIP_MEMORY_SCOPE_AGENT);
    {
      const int hw = w - 4, l16 = lane * 16, wa = OFF_WA + m * 84 * 1024;
      dma<1, -1>(rs, OFF_CS + m * 3072, L_CS, 0, l16);
      DMA(wa, 0); DMA(wa + SLOT, 1); DMA(wa + 2 * SLOT, 2);
    }
  }
  for (int L = 0; L < 2; ++L) {
    OPQ_S(w); OPQ_S(g); OPQ_S(m); OPQ_V(lane);
    const int t = lane & 31, h = lane >> 5;
    const int mw = w & 1, kh = (w >> 1) & 1, par = w >> 2, hw = w & 3, l16 = lane * 16;
    const int wa = OFF_WA + m * 84 * 1024, wb = OFF_WB + j * 64 * 1024, wc = OFF_WC + m * 196 * 1024, wd = OFF_WD + j * 224 * 1024;
    const int ttA = g * 4 + w;
    const int ttB = g * 4 + 2 * half + mw;
    const int ntB = 2 * j + kh;
    const int myflag = (g * 16 + m) * 4 + w;
    LAS char* lb[3] = {lds + lane * 16, lds + 2 * SLOT + lane * 16, lds + 4 * SLOT + lane * 16};
    OPQ_V(lb[1]); OPQ_V(lb[2]);
#define LSL(c) (lb[((c) % 5) >> 1] + (((c) % 5) & 1) * SLOT)
    if (w >= 4) {
      {
        const bool hasprev = (g & 7) > 0;
        const int gp = hasprev ? g - 1 : g, ttp = gp * 4 + 3;
        VMWAIT(0);
        BAR();
        if (L) poll(ws, fi_x(gp, 3, lane), 4);
        const int t16 = lane & 15, gq = lane >> 4, tk = 24 + (t16 & 7), isv = hw >> 1, nh = hw & 1;
        v4u xf[14]; v2u st[4];
#pragma unroll
        for (int i = 0; i < 4; ++i) st[i] = ld8(rs, ((gq + 4 * i < 14 ? gq + 4 * i : 0) * 32 + tk) * 8, OFF_ST + 2 * L * SBUF + ttp * 14 * 256);
#pragma unroll
        for (int i = 0; i < 14; ++i) xf[i] = ld16(rs, (gq >> 1) * 1024 + ((gq & 1) * 32 + tk) * 16, OFF_X + 2 * L * XBUF + (ttp * 28 + 2 * i) * 1024);
        float sm = 0.f;
#pragma unroll
        for (int i = 0; i < 4; ++i) sm += (gq + 4 * i < 14) ? asf(st[i].x) : 0.f;
        sm += __shfl_xor(sm, 16); sm = xsum32(sm);
        const float mean = sm * (1.f / D_);
        float m2 = 0.f;
#pragma unroll
        for (int i = 0; i < 4; ++i) {
          const float d = asf(st[i].x) * (1.f / 32.f) - mean;
          m2 += (gq + 4 * i < 14) ? asf(st[i].y) + 32.f * d * d : 0.f;
        }
        m2 += __shfl_xor(m2, 16); m2 = xsum32(m2);
        const float rstd = __builtin_amdgcn_rsqf(m2 * (1.f / D_) + 1e-5f);
        LAS char* wp = lds + (1 + isv) * SLOT + (gq >> 1) * 1024 + ((gq & 1) * 32 + 16 * nh + t16) * 16;
        const v4u xa = aug_x(-mean, gq == 0), wg_ = aug_w(*(LAS v2u*)(lds + L_CS + (1 + isv) * 256 + (16 * nh + t16) * 8));
        f32x4 a = {0.f, 0.f, 0.f, 0.f};
        if (isv) {
#pragma unroll
          for (int i = 0; i < 14; ++i) a = mfma16(xf[i], lds16(wp + i * 2048), a);
          a = mfma16(xa, wg_, a);
          float r[4];
#pragma unroll
          for (int i = 0; i < 4; ++i) r[i] = __shfl(rstd, 4 * gq + i);
          *(LAS v2u*)(lds + L_EX + 2048 + (16 * nh + t16 + 32 * (gq & 1)) * 16 + 8 * (gq >> 1)) = v2u{pk(a[0] * r[0], a[1] * r[1]), pk(a[2] * r[2], a[3] * r[3])};
        } else {
#pragma unroll
          for (int i = 0; i < 14; ++i) a = mfma16(lds16(wp + i * 2048), xf[i], a);
          a = mfma16(wg_, xa, a);
          *(LAS v2u*)(lds + L_EX + nh * 1024 + (16 + t16 + 32 * (gq & 1)) * 16 + 8 * (gq >> 1)) = v2u{pk(a[0] * rstd, a[1] * rstd), pk(a[2] * rstd, a[3] * rstd)};
        }
      }
      if (L == 0) {
        poll_setup(OFF_PF + 32768 + hw * 8192, IC<1>{});
        BAR();
        if (b == 0 && tid == 256) __hip_atomic_store((GAS unsigned*)(ws + OFF_EP), tag + 1u, __ATOMIC_RELAXED, __HIP_MEMORY_SCOPE_AGENT);
        dma<1, -1>(rs, OFF_CS + m * 3072, L_CS, hw < 3 ? hw : 2, l16);
      }
      DMA(wb, 3); DMA(wb + SLOT, 4);
      BAR();
      DMA(wc, 5); DMA(wc + SLOT, 6); DMA(wc + 2 * SLOT, 7);
      VMWAIT(21);
    } else {
      constexpr int W0 = 8;
      v4u xf[28]; v2u st[7];
      const int xoff = OFF_X + 2 * L * XBUF + ttA * 28 * 1024, soff = OFF_ST + 2 * L * SBUF + ttA * 14 * 256;
      if (L) poll(ws, fi_x(g, w, lane), 4);
      TS(3 + 32 * L);
#pragma unroll
      for (int i = 0; i < W0; ++i) { xf[i] = ldf(rs, lane, xoff, i); __builtin_amdgcn_sched_barrier(0); }
      BAR();
      TS(1 + 32 * L);
      f32x16 aq = {}, ak = {}, av = {};
      float mean, rstd; v4u xa; v2u cw[3]; f32x4 rv[4];
      pipe<84, 6>([&](auto I) AI { constexpr int i = I.v; return i < 84 ? lds16(LSL((i + 1) % 3) + (i / 3) * 1024) : v4u{}; },
                  [&]() AI {},
                  [&](auto I, v4u wf) AI { constexpr int i = I.v;
                    if constexpr (i % 3 == 0) mfma32p(wf, xf[i / 3], ak);
                    else if constexpr (i % 3 == 1) mfma32p(xf[i / 3], wf, av);
                    else mfma32p(wf, xf[i / 3], aq);
                    if constexpr (i % 3 == 0 && i / 3 + W0 < 28) xf[i / 3 + W0] = ldf(rs, lane, xoff, i / 3 + W0);
                    if constexpr (i % 3 == 1 && i / 3 < 7) st[i / 3] = ld8(rs, ((7 * h + i / 3) * 32 + t) * 8, soff);
                    if constexpr (i == 70) { if (L == 0) BAR(); }
                    if constexpr (i == 62) { fin_stats(st, mean, rstd); OPQ_V(mean); OPQ_V(rstd); }
                    if constexpr (i == 66) { xa = aug_x(-mean, h == 0); OPQ_V(xa); *(LAS float*)(lds + L_RS + w * 128 + t * 4) = rstd; }
                    if constexpr (i >= 66 && i < 69) cw[i - 66] = *(LAS v2u*)(lds + L_CS + (i - 66) * 256 + t * 8);
                    if constexpr (i == 72) mfma32p(aug_w(cw[1]), xa, ak);
                    if constexpr (i == 73) mfma32p(xa, aug_w(cw[2]), av);
                    if constexpr (i == 74) mfma32p(aug_w(cw[0]), xa, aq);
                    if constexpr (i >= 78 && i < 82) rv[i - 78] = *(LAS f32x4*)(lds + L_RS + w * 128 + (8 * (i - 78) + 4 * h) * 4);
                  });
      TS(4 + 32 * L);
      const v4u kf0 = pack8(ak, 0, rstd), kf1 = pack8(ak, 8, rstd);
      v4u vf[2];
#pragma unroll
      for (int q = 0; q < 4; ++q) {
        vf[q >> 1][(q & 1) * 2] = pk(av[4 * q] * rv[q][0], av[4 * q + 1] * rv[q][1]);
        vf[q >> 1][(q & 1) * 2 + 1] = pk(av[4 * q + 2] * rv[q][2], av[4 * q + 3] * rv[q][3]);
      }
      if (w < 3) {
        LAS char* e = lds + L_EX + (w + 1) * 3072 + lane * 16;
        *(LAS v4u*)e = kf0; *(LAS v4u*)(e + 1024) = kf1; *(LAS v4u*)(e + 2048) = vf[1];
      }
      const v4u qf0 = pack8(aq, 0, rstd), qf1 = pack8(aq, 8, rstd);
      f32x16 sc = {};
      mfma32p(kf0, qf0, sc); mfma32p(kf1, qf1, sc);
      TS(5 + 32 * L);
      BAR();
      TS(6 + 32 * L);
      LAS char* e = lds + L_EX + w * 3072 + lane * 16;
      f32x16 sp = {};
      mfma32p(lds16(e), qf0, sp); mfma32p(lds16(e + 1024), qf1, sp);
      const v4u vp = lds16(e + 2048);
      const bool hasprev = w > 0 || (g & 7) > 0;
      const float NEG = -__builtin_inff();
      float p[16], pp[4], mx = NEG;
#pragma unroll
      for (int r = 0; r < 16; ++r) { const int jj = (r & 3) + 8 * (r >> 2) + 4 * h; p[r] = ((unsigned)(t - jj) <= 7u) ? sc[r] : NEG; mx = fmaxf(mx, p[r]); }
#pragma unroll
      for (int r = 0; r < 4; ++r) { const int jj = 24 + r + 4 * h; pp[r] = (hasprev && jj >= t + 25) ? sp[12 + r] : NEG; mx = fmaxf(mx, pp[r]); }
      const float cf = 0.18898223650461362f * 1.4426950408889634f;
      const float mc = -xmax32(mx) * cf;
#pragma unroll
      for (int r = 0; r < 16; ++r) p[r] = __builtin_amdgcn_exp2f(__builtin_fmaf(p[r], cf, mc));
#pragma unroll
      for (int r = 0; r < 4; ++r) pp[r] = __builtin_amdgcn_exp2f(__builtin_fmaf(pp[r], cf, mc));
      const v4u pf0 = {pk(p[0], p[1]), pk(p[2], p[3]), pk(p[4], p[5]), pk(p[6], p[7])};
      const v4u pf1 = {pk(p[8], p[9]), pk(p[10], p[11]), pk(p[12], p[13]), pk(p[14], p[15])};
      const v4u pfp = {0u, 0u, pk(pp[0], pp[1]), pk(pp[2], pp[3])};
      f32x16 o = {};
      mfma32p(vf[0], pf0, o); mfma32p(vf[1], pf1, o); mfma32p(vp, pfp, o);
      float s0 = pp[0] + pp[1], s1 = pp[2] + pp[3];
#pragma unroll
      for (int r = 0; r < 8; ++r) { s0 += p[2 * r]; s1 += p[2 * r + 1]; }
      const float inv = __builtin_amdgcn_rcpf(xsum32(s0 + s1));
      st16(pack8(o, 0, inv), rs, lane * 16, OFF_O + L * OBUF + (ttA * 32 + 2 * m) * 1024);
      st16(pack8(o, 8, inv), rs, lane * 16 + 1024, OFF_O + L * OBUF + (ttA * 32 + 2 * m) * 1024);
      TS(7 + 32 * L);
      publish(ws, myflag, 4 * L + 1, lane);
      TS(8 + 32 * L);
    }
    if (w >= 4) {
      BAR();
      BAR();
      VMWAIT(0);
    } else {
      constexpr int W0 = 8;
      v4u of[16];
      const int ooff = OFF_O + L * OBUF + (ttB * 32 + 16 * kh) * 1024;
      BAR();
      TS(11 + 32 * L);
      v4u wl[4];
#pragma unroll
      for (int i = 0; i < 4; ++i) wl[i] = __builtin_amdgcn_raw_buffer_load_b128(rs, lane * 16 + (i & 2) * 1024, OFF_WB + (j * 64 + 56 + kh * 4 + ((i & 1) ^ kh ^ 1)) * 1024, AUX_SC1);
      f32x16 acc[2] = {{}, {}};
      LAS char* wp0 = lds + slot_of(3) + kh * 15 * 1024 + lane * 16; LAS char* wp1 = lds + slot_of(3) + 1024 + kh * 13 * 1024 + lane * 16;
      OPQ_V(wp0); OPQ_V(wp1);
      pipe<32, 6>([&](auto I) AI { constexpr int i = I.v;
                    if constexpr (i < 28) return lds16(((i & 1) ? wp0 : wp1) + (i / 14) * SLOT + ((i % 14) >> 1) * 2048);
                    else return wl[i & 3]; },
                  [&]() AI {
                    poll(ws, fi_k(g, kh, ttB & 3, lane), 4 * L + 1);
                    TS(9 + 32 * L);
#pragma unroll
                    for (int i = 0; i < W0; ++i) { of[i] = ldf(rs, lane, ooff, i); __builtin_amdgcn_sched_barrier(0); }
                  },
                  [&](auto I, v4u wf) AI { constexpr int i = I.v;
                    mfma32p(wf, of[i >> 1], acc[(i & 1) ^ 1]);
                    if constexpr (i + W0 < 16) of[i + W0] = ldf(rs, lane, ooff, i + W0);
                  });
      TS(12 + 32 * L);
      res_epilogue<false>(acc, res, lds, rs, ws, out, w, lane, g, m, ttB, ntB, 2 * L + 1, active, false, 4 * L + 2);
      TS(13 + 32 * L);
    }
    v4u wr[28];
    auto phase_d = [&](auto PARC) AI {
      constexpr int PAR = PARC.v, W0 = 14;
      const int hoff = OFF_H + L * HBUF + (ttB * 112 + 56 * kh + 7 * PAR) * 1024;
      v4u hf[W0];
      f32x16 acc[2] = {{}, {}};
      LAS char* db[2];
      pipe<56, 6>(
          [&](auto I) AI { constexpr int i = I.v, r = i / 14;
            if constexpr (i % 14 == 0) {
              if constexpr (r < 2) BAR();
              if constexpr (r == 0) TS(27 + 32 * L);
              if constexpr (PAR && r == 0) DMA(wd + 4 * SLOT, 16);
              if constexpr (PAR && r == 1) DMA(wd + 6 * SLOT, 12);
              constexpr int sl = slot_of(r == 3 ? 12 : 12 + PAR + 2 * r);
              db[1] = lds + sl + kh * 15 * 1024 + l16; db[0] = lds + sl + 1024 + kh * 13 * 1024 + l16;
            }
            if constexpr (i == (PAR ? DQ : 42)) { if constexpr (PAR) VMWAIT(3); TS(28 + 32 * L); BAR(); TS(29 + 32 * L); }
            if constexpr (PAR && r >= 2) return wr[i - 28]; else return lds16(db[i & 1] + ((i % 14) >> 1) * 2048);
          },
          [&]() AI {
            poll(ws, (g * 16 + 8 * kh + PAR + 2 * (lane & 3)) * 4 + (ttB & 3), 4 * L + 3);
            TS(25 + 32 * L);
#pragma unroll
            for (int i = 0; i < W0; ++i) { hf[i] = ldf(rs, lane, hoff + (i / 7) * 14336, i % 7); __builtin_amdgcn_sched_barrier(0); }
          },
          [&](auto I, v4u wf) AI { constexpr int i = I.v;
            constexpr int k = i >> 1;
            mfma32p(wf, hf[k % W0], acc[(i & 1) ^ 1]);
            if constexpr ((i & 1) && k + W0 < 28) hf[k % W0] = ldf(rs, lane, hoff + ((k + W0) / 7) * 14336, (k + W0) % 7);
          });
      TS(30 + 32 * L);
      if constexpr (!PAR) { if (L == 0) { DMA(wa, 0); DMA(wa + SLOT, 1); } }
      res_epilogue<true>(acc, res, lds, rs, ws, out, w, lane, g, m, ttB, ntB, 2, active, L == 1, 4 * L + 4);
      TS(31 + 32 * L);
    };
    if (w >= 4) {
      BAR();
      DMAT(wc + 3 * SLOT, 8); DMAT(wc + 4 * SLOT, 9);
#pragma unroll
      for (int i = 0; i < 14; ++i) { wr[14 + i] = __builtin_amdgcn_raw_buffer_load_b128(rs, l16, wd + 7 * SLOT + (kh * 14 + (i & ~1) + ((i & 1) ^ kh ^ 1)) * 1024, AUX_SC1); __builtin_amdgcn_sched_barrier(0); }
      VMWAIT(14);
      BAR();
      DMA(wc + 5 * SLOT, 10); DMA(wc + 6 * SLOT, 11); DMA(wd, 12);
#pragma unroll
      for (int i = 0; i < 14; ++i) { wr[i] = __builtin_amdgcn_raw_buffer_load_b128(rs, l16, wd + 5 * SLOT + (kh * 14 + (i & ~1) + ((i & 1) ^ kh ^ 1)) * 1024, AUX_SC1); __builtin_amdgcn_sched_barrier(0); }
      VMWAIT(21);
      BAR();
      DMA(wd + SLOT, 13); DMA(wd + 2 * SLOT, 14);
      BAR();
      DMA(wd + 3 * SLOT, 15);
      VMWAIT(7);
      __builtin_amdgcn_s_setprio(QPRIO);
      phase_d(IC<1>{});
      __builtin_amdgcn_s_setprio(0);
      if (L == 0) DMAT(wa + 2 * SLOT, 2);
    } else {
      constexpr int W0 = 8;
      v4u xf[28]; v2u st[7];
      const int xoff = OFF_X + (2 * L + 1) * XBUF + ttA * 28 * 1024, soff = OFF_ST + (2 * L + 1) * SBUF + ttA * 14 * 256;
      BAR();
      TS(16 + 32 * L);
      const int hoff = OFF_H + L * HBUF + (ttA * 112 + 7 * m) * 1024;
      LAS char* cs = lds + L_CS + 768 + t * 8;
      f32x16 a[4] = {{}, {}, {}, {}};
      float mean, rstd, c1, kk; v4u xa; float hv[8], ev = 0.f, tv = 0.f; v2u cw[7];
      auto epi = [&](const f32x16& acc, int e, int tile) AI {
        const int q = e >> 1;
        if (e & 1) {
          if (q > 0) { hv[q - 1] *= ev; OPQ_V(hv[q - 1]); }
          if (q < 8) { ev = __builtin_amdgcn_exp2f(tv); hv[q] = acc[q] * acc[8 + q]; OPQ_V(ev); OPQ_V(hv[q]); }
        } else if (e < 18) {
          if (q > 0) ev = __builtin_fmaf(ev, kk, kk);
          if (q < 8) { tv = acc[q] * c1; OPQ_V(tv); }
          if (q > 0) { ev = __builtin_amdgcn_rcpf(ev); OPQ_V(ev); }
        } else st16(v4u{pk(hv[0], hv[1]), pk(hv[2], hv[3]), pk(hv[4], hv[5]), pk(hv[6], hv[7])}, rs, lane * 16 + (tile & 3) * 1024, hoff + (tile >> 2) * 4096);
      };
      pipe<196, 5>(
          [&](auto I) AI { constexpr int i = I.v;
            if constexpr (i == 84 || i == 140) { TS(17 + 2 * ((i - 84) / 56) + 32 * L); BAR(); TS(18 + 2 * ((i - 84) / 56) + 32 * L); }
            if constexpr (i == 168) BAR();
            constexpr int tile = i < 84 ? i % 3 : i / 28;
            constexpr int ks = i < 84 ? i / 3 : i % 28;
            return i < 196 ? lds16(LSL(5 + tile) + ks * 1024) : v4u{};
          },
          [&]() AI {
            poll(ws, fi_x(g, w, lane), 4 * L + 2);
            TS(14 + 32 * L);
#pragma unroll
            for (int i = 0; i < W0; ++i) { xf[i] = ldf(rs, lane, xoff, i); __builtin_amdgcn_sched_barrier(0); }
          },
          [&](auto I, v4u wf) AI { constexpr int i = I.v;
            constexpr int tile = i < 84 ? i % 3 : i / 28;
            constexpr int ks = i < 84 ? i / 3 : i % 28;
            if constexpr (i >= 84 && ks == 0) { a[tile & 3] = f32x16{}; mfma32p(aug_w(cw[tile]), xa, a[tile & 3]); }
            mfma32p(wf, xf[ks], a[tile & 3]);
            if constexpr (i < 84 && i % 3 == 0 && i / 3 + W0 < 28) xf[i / 3 + W0] = ldf(rs, lane, xoff, i / 3 + W0);
            if constexpr (i % 3 == 1 && i / 3 < 7) st[i / 3] = ld8(rs, ((7 * h + i / 3) * 32 + t) * 8, soff);
            if constexpr (i >= 70 && i < 73) cw[i - 70] = *(LAS v2u*)(cs + (i - 70) * 256);
            if constexpr (i >= 78 && i % 28 == 22 && i < 170) cw[i / 28 + 1] = *(LAS v2u*)(cs + (i / 28 + 1) * 256);
            if constexpr (i == 62) { fin_stats(st, mean, rstd); OPQ_V(mean); OPQ_V(rstd); }
            if constexpr (i == 66) { xa = aug_x(-mean, h == 0); c1 = rstd * -1.4426950408889634f; kk = __builtin_amdgcn_rcpf(rstd * rstd); OPQ_V(xa); OPQ_V(c1); OPQ_V(kk); }
            if constexpr (i >= 75 && i < 78) mfma32p(aug_w(cw[i - 75]), xa, a[i - 75]);
            if constexpr (i >= 83) epi(a[((i - 83) / 19) & 3], (i - 83) % 19, (i - 83) / 19);
          });
      TS(21 + 32 * L);
      epi(a[1], 18, 5);
      st16(swiglu(a[2], rstd), rs, lane * 16 + 2048, hoff + 4096);
      TS(23 + 32 * L);
      publish(ws, myflag, 4 * L + 3, lane);
      TS(24 + 32 * L);
      phase_d(IC<0>{});
    }
  }
}

extern "C" void kernel_launch(void* const* d_in, const int* in_sizes, int n_in, void* d_out, int out_size, void* d_ws,
                              size_t ws_size, hipStream_t stream) {
  (void)in_sizes; (void)n_in; (void)out_size; (void)ws_size;
  (void)hipFuncSetAttribute((const void*)k_main, hipFuncAttributeMaxDynamicSharedMemorySize, LDS_BYTES);
  k_main<<<256, 512, LDS_BYTES, stream>>>((const char*)d_in[0], (const char*)d_in[1], (const char*)d_in[2], (const char*)d_in[4],
                                          (const char*)d_in[5], (const char*)d_in[6], (char*)d_ws, (unsigned short*)d_out);
}
```

```cpp
#include <hip/hip_runtime.h>
#include <hip/hip_bf16.h>

typedef unsigned v4u __attribute__((ext_vector_type(4)));
typedef unsigned v2u __attribute__((ext_vector_type(2)));
typedef float f32x4 __attribute__((ext_vector_type(4)));
typedef float f32x2 __attribute__((ext_vector_type(2)));
typedef float f32x16 __attribute__((ext_vector_type(16)));
typedef __bf16 bf16x8 __attribute__((ext_vector_type(8)));
typedef __bf16 bf16x2 __attribute__((ext_vector_type(2)));
#define LAS __attribute__((address_space(3)))
#define GAS __attribute__((address_space(1)))
#define DEV __device__ __forceinline__
#define AI __attribute__((always_inline))

constexpr int S_ = 2048, D_ = 448, F_ = 1792;
constexpr int OFF_FLAG = 0;
constexpr int OFF_PF = 65536, OFF_EP = OFF_PF + 65536;
constexpr int OFF_WA = 135168;
constexpr int OFF_WB = OFF_WA + 16 * 84 * 1024;
constexpr int OFF_WC = OFF_WB + 7 * 64 * 1024;
constexpr int OFF_WD = OFF_WC + 16 * 196 * 1024;
constexpr int OFF_CS = OFF_WD + 7 * 224 * 1024;
constexpr int XBUF = 64 * 28 * 1024, SBUF = 64 * 14 * 256, OBUF = 64 * 32 * 1024, HBUF = 64 * 112 * 1024;
constexpr int OFF_X = OFF_CS + 16 * 3072;
constexpr int OFF_ST = OFF_X + 4 * XBUF;
constexpr int OFF_O = OFF_ST + 4 * SBUF;
constexpr int OFF_H = OFF_O + 2 * OBUF;
constexpr int WS_BYTES = OFF_H + 2 * HBUF;
constexpr int SLOT = 28 * 1024;
constexpr int L_EX = 5 * SLOT, L_CS = L_EX + 16384, L_RS = L_CS + 3072, LDS_BYTES = L_RS + 512;
static_assert(LDS_BYTES <= 163840, "lds");
constexpr int AUX_SC1 = 16;

DEV unsigned pk(float a, float b) { bf16x2 t = {(__bf16)a, (__bf16)b}; return __builtin_bit_cast(unsigned, t); }
DEV float asf(unsigned u) { return __builtin_bit_cast(float, u); }
DEV float blo(unsigned u) { return __builtin_bit_cast(float, u << 16); }
DEV float bhi(unsigned u) { return __builtin_bit_cast(float, u & 0xffff0000u); }
DEV f32x16 mfma32(v4u a, v4u b, f32x16 c) {
  return __builtin_amdgcn_mfma_f32_32x32x16_bf16(__builtin_bit_cast(bf16x8, a), __builtin_bit_cast(bf16x8, b), c, 0, 0, 0);
}
DEV void mfma32p(v4u a, v4u b, f32x16& c) { c = mfma32(a, b, c); asm volatile("" : "+v"(c)); __builtin_amdgcn_sched_barrier(0); }
DEV f32x4 mfma16(v4u a, v4u b, f32x4 c) {
  return __builtin_amdgcn_mfma_f32_16x16x32_bf16(__builtin_bit_cast(bf16x8, a), __builtin_bit_cast(bf16x8, b), c, 0, 0, 0);
}
DEV float xsum32(float v) {
  auto r = __builtin_amdgcn_permlane32_swap(__builtin_bit_cast(unsigned, v), __builtin_bit_cast(unsigned, v), false, false);
  return __builtin_bit_cast(float, (unsigned)r[0]) + __builtin_bit_cast(float, (unsigned)r[1]);
}
DEV float xmax32(float v) {
  auto r = __builtin_amdgcn_permlane32_swap(__builtin_bit_cast(unsigned, v), __builtin_bit_cast(unsigned, v), false, false);
  return fmaxf(__builtin_bit_cast(float, (unsigned)r[0]), __builtin_bit_cast(float, (unsigned)r[1]));
}
DEV void split3(float v, unsigned& hi, unsigned& mid, unsigned& lo) {
  hi = __builtin_bit_cast(unsigned, v) & 0xffff0000u;
  float r = v - __builtin_bit_cast(float, hi);
  mid = __builtin_bit_cast(unsigned, r) & 0xffff0000u;
  float r2 = r - __builtin_bit_cast(float, mid);
  lo = __builtin_bit_cast(unsigned, r2) & 0xffff0000u;
}
DEV v4u aug_x(float negmean, bool on) {
  unsigned hi, mid, lo; split3(negmean, hi, mid, lo);
  v4u r = {hi | (hi >> 16), mid | (mid >> 16), lo | (hi >> 16), 0u};
  if (!on) r = v4u{0u, 0u, 0u, 0u};
  return r;
}
DEV v4u aug_w(v2u c) { return v4u{c.x, c.x, c.y, 0u}; }

typedef short s16x4 __attribute__((ext_vector_type(4)));
typedef __amdgpu_buffer_rsrc_t rsrc_t;
#define BAR() asm volatile("s_waitcnt lgkmcnt(0)\n\ts_barrier" ::: "memory")
#define VMWAIT(n) asm volatile("s_waitcnt vmcnt(" #n ")" ::: "memory")
DEV v4u ld16(rsrc_t rs, int voff, int soff) { return __builtin_amdgcn_raw_buffer_load_b128(rs, voff, soff, AUX_SC1); }
DEV v2u ld8(rsrc_t rs, int voff, int soff) { return __builtin_amdgcn_raw_buffer_load_b64(rs, voff, soff, AUX_SC1); }
DEV void st16(v4u v, rsrc_t rs, int voff, int soff) { __builtin_amdgcn_raw_buffer_store_b128(v, rs, voff + soff, 0, AUX_SC1); }
DEV void st8(v2u v, rsrc_t rs, int voff, int soff) { __builtin_amdgcn_raw_buffer_store_b64(v, rs, voff, soff, AUX_SC1); }
DEV v4u ldf(rsrc_t rs, int lane, int base, int i) { return ld16(rs, lane * 16 + (i & 3) * 1024, base + (i >> 2) * 4096); }
DEV v4u lds16(LAS char* p) { return *(LAS v4u*)p; }

DEV v2u tr8(LAS char* p) { return __builtin_bit_cast(v2u, __builtin_amdgcn_ds_read_tr16_b64_v4i16((LAS s16x4*)p)); }
DEV void pack_unit(int u, int tid, LAS char* sm, rsrc_t rs, const char* x, const char* wqkv, const char* wo, const char* wg,
                   const char* wu, const char* wd) {
  const int w = __builtin_amdgcn_readfirstlane(tid >> 6), lane = tid & 63, n = lane & 31, h = lane >> 5;
  if (u < 0) { BAR(); return; }
  if (u < 77) {
    const char* src; int ld;
    if (u < 21) { src = wqkv + u * 128; ld = 2688; } else if (u < 49) { src = wg + (u - 21) * 128; ld = 3584; } else { src = wu + (u - 49) * 128; ld = 3584; }
    src += (tid >> 3) * ld + (tid & 7) * 16;
    v4u v[14];
#pragma unroll
    for (int i = 0; i < 14; ++i) v[i] = *(const v4u*)(src + i * 32 * ld);
    float c[8] = {0.f, 0.f, 0.f, 0.f, 0.f, 0.f, 0.f, 0.f};
#pragma unroll
    for (int i = 0; i < 14; ++i)
#pragma unroll
      for (int q = 0; q < 4; ++q) { const unsigned e = v[i][q]; c[2 * q] += blo(e); c[2 * q + 1] += bhi(e); }
    *(LAS f32x4*)(sm + (tid >> 3) * 272 + (tid & 7) * 32) = f32x4{c[0], c[1], c[2], c[3]};
    *(LAS f32x4*)(sm + (tid >> 3) * 272 + (tid & 7) * 32 + 16) = f32x4{c[4], c[5], c[6], c[7]};
    BAR();
    if (tid < 64) {
      float cs = 0.f;
#pragma unroll
      for (int i = 0; i < 32; ++i) cs += *(LAS float*)(sm + i * 272 + tid * 4);
      unsigned hi, mid, lo; split3(cs, hi, mid, lo);
      int o;
      if (u < 21) { const int c_ = u * 64 + tid, t = c_ / 448, r = c_ % 448; o = (r / 28) * 3072 + t * 256 + (r % 28) * 8;
        if (r % 28 < 4) st8(v2u{0u, 0u}, rs, OFF_CS + o + 28 * 8, 0); }
      else { const int f = ((u - 21) % 28) * 64 + tid; o = (f / 112) * 3072 + (3 + (f % 112) / 16) * 256 + ((f & 15) + (u >= 49 ? 16 : 0)) * 8; }
      st8(v2u{(hi >> 16) | mid, (lo >> 16) | hi}, rs, OFF_CS + o, 0);
    }
    return;
  }
  const bool isx = u >= 119 && u < 183;
  const int lstride = isx ? 912 : 960;
  int dst0 = 0, dstep = 0, cstep = 128, col0 = 64 * (w >> 1), row0 = 16 * (w & 1);
  {
    const char* src; int ld = 896;
    if (u < 119) { const int q = u - 77; ld = 2688; src = wqkv + ((q / 3) * 32 + 8 * w) * 2688 + (q % 3) * 896;
      dst0 = OFF_WA + (((w >> 1) * 3 + q % 3) * 28 + 2 * (q / 3) + (w & 1)) * 1024; dstep = 168 * 1024; cstep = 112; col0 = 56 * (w >> 1); }
    else if (u < 183) { const int tt = u - 119, g = tt >> 2; ld = 1792; src = x + (2 * (128 * (g & 7) + 32 * (tt & 3) + 8 * w) + (g >> 3)) * 896; }
    else if (u < 199) { const int s = 2 * (u - 183) + (w & 1), kh = s >> 4, ks = s & 15, c = ks / 7; src = wo + ((u - 183) * 28 + 8 * w) * 896;
      dst0 = OFF_WB + (c * 28 + kh * (c < 2 ? 14 : 4) + (ks % 7) * 2 + (w >> 1)) * 1024; dstep = 64 * 1024; }
    else if (u < 311) { const int q = u - 199; ld = 3584; src = (w < 2 ? wg : wu) + ((q >> 2) * 16 + 8 * (w & 1)) * 3584 + (q & 3) * 896;
      dst0 = OFF_WC + (((q & 3) * 28 + w) * 28 + (q >> 2)) * 1024; dstep = 112 * 1024; col0 = 32 * w; }
    else { const int s = 2 * (u - 311) + (w & 1), kh = s / 56, ks = s % 56; src = wd + ((u - 311) * 32 + 8 * w) * 896;
      dst0 = OFF_WD + ((ks / 7) * 28 + kh * 14 + (ks % 7) * 2 + (w >> 1)) * 1024; dstep = 224 * 1024; }
    const bool pad = u >= 183 && u < 199 && w == 3;
    if (lane < 56) {
      v4u v[8];
#pragma unroll
      for (int i = 0; i < 8; ++i) v[i] = *(const v4u*)(src + ((pad && i >= 4) ? 0 : i * ld) + lane * 16);
#pragma unroll
      for (int i = 0; i < 8; ++i) *(LAS v4u*)(sm + (8 * w + i) * lstride + lane * 16) = (pad && i >= 4) ? v4u{0u, 0u, 0u, 0u} : v[i];
    }
  }
  BAR();
  if (isx) {
    const int tt = u - 119;
#pragma unroll
    for (int it = 0; it < 4; ++it) {
      const int nt = w + 4 * it;
      if (nt >= 14) break;
      v2u a[4];
#pragma unroll
      for (int i = 0; i < 4; ++i) a[i] = *(LAS v2u*)(sm + n * 912 + nt * 64 + i * 16 + h * 8);
      __builtin_amdgcn_raw_buffer_store_b128(v4u{a[0].x, a[0].y, a[1].x, a[1].y}, rs, OFF_X + (tt * 28 + 2 * nt) * 1024 + lane * 16, 0, AUX_SC1);
      __builtin_amdgcn_raw_buffer_store_b128(v4u{a[2].x, a[2].y, a[3].x, a[3].y}, rs, OFF_X + (tt * 28 + 2 * nt + 1) * 1024 + lane * 16, 0, AUX_SC1);
      float sm1 = 0.f;
#pragma unroll
      for (int i = 0; i < 4; ++i) sm1 += blo(a[i].x) + bhi(a[i].x) + blo(a[i].y) + bhi(a[i].y);
      sm1 = xsum32(sm1);
      const float mu = sm1 * (1.f / 32.f);
      float m2 = 0.f;
#pragma unroll
      for (int i = 0; i < 4; ++i) {
        const float d0 = blo(a[i].x) - mu, d1 = bhi(a[i].x) - mu, d2 = blo(a[i].y) - mu, d3 = bhi(a[i].y) - mu;
        m2 += d0 * d0 + d1 * d1 + d2 * d2 + d3 * d3;
      }
      m2 = xsum32(m2);
      if (h == 0) st8(v2u{__builtin_bit_cast(unsigned, sm1), __builtin_bit_cast(unsigned, m2)}, rs, OFF_ST + (tt * 14 + nt) * 256 + n * 8, 0);
    }
    return;
  }
  const int q4 = (lane & 15) >> 2, p4 = lane & 3, cb = (lane >> 4) & 1;
  const bool gu = u >= 199 && u < 311;
  LAS char* pa = sm + ((gu ? 16 * cb : row0) + 4 * h + q4) * 960 + col0 + (gu ? 0 : 32 * cb) + 8 * p4;
  v2u lo[8], hi[8];
#pragma unroll
  for (int i = 0; i < 8; ++i) { LAS char* q = pa + ((i < 7 || u < 119) ? i * cstep : 0); lo[i] = tr8(q); hi[i] = tr8(q + 8 * 960); }
  const bool z = u < 119 && n >= 28;
#pragma unroll
  for (int i = 0; i < 8; ++i)
    if (i < 7 || u < 119) st16(z ? v4u{0u, 0u, 0u, 0u} : v4u{lo[i].x, lo[i].y, hi[i].x, hi[i].y}, rs, lane * 16, dst0 + i * dstep);
}

DEV void poll(char* ws, int fi, unsigned want) {
  GAS unsigned* f = (GAS unsigned*)(ws + OFF_FLAG) + fi * 16;
  for (int spin = 0; spin < (1 << 20); ++spin) {
    const unsigned v = __hip_atomic_load(f, __ATOMIC_RELAXED, __HIP_MEMORY_SCOPE_AGENT);
    if (__all(v >= want)) break;
  }
  __builtin_amdgcn_fence(__ATOMIC_ACQUIRE, "wavefront");
}
DEV int fi_x(int g, int T, int lane) { const int i = lane < 14 ? lane : 0; return (g * 16 + 8 * (T >> 1) + (i >> 1)) * 4 + (T & 1) + 2 * (i & 1); }
DEV int fi_k(int g, int kh, int T, int lane) { return (g * 16 + 8 * kh + (lane & 7)) * 4 + T; }
DEV void publish(char* ws, int fi, unsigned val, int lane) {
  VMWAIT(0);
  if (lane == 0) __hip_atomic_store((GAS unsigned*)(ws + OFF_FLAG) + fi * 16, val, __ATOMIC_RELAXED, __HIP_MEMORY_SCOPE_AGENT);
}
template <int N, int TH> DEV void dma(rsrc_t rs, int src, int dst, int hw, int lane16) {
#pragma unroll
  for (int i = 0; i < N; ++i) {
    unsigned keep;
    asm volatile("s_mov_b32 %0, m0\n\ts_mov_b32 m0, %1\n\ts_nop 0\n\tbuffer_load_dwordx4 %2, %3, %4 offen sc1 lds\n\ts_mov_b32 m0, %0"
                 : "=&s"(keep) : "s"(dst + (hw * N + i) * 1024), "v"(lane16), "s"(rs), "s"(src + (hw * N + i) * 1024) : "memory");
    if constexpr (TH >= 0) asm volatile("s_waitcnt vmcnt(%0)" :: "i"(TH) : "memory");
  }
}
#define THR 4
#define DQ 42
#define QPRIO 1
#define QDELAY 75
#define DMA(src, c) dma<7, -1>(rs, src, slot_of(c), hw, l16)
#define DMAT(src, c) dma<7, THR>(rs, src, slot_of(c), hw, l16)
#define OPQ_S(x) asm volatile("" : "+s"(x))
#define OPQ_V(x) asm volatile("" : "+v"(x))
template <int I> struct IC { static constexpr int v = I; };
template <int I, int N, class F> DEV void sfor(F& f) { if constexpr (I < N) { f(IC<I>{}); sfor<I + 1, N>(f); } }
template <int N, int PF, class RD, class MID, class ST> DEV void pipe(RD rd, MID mid, ST st) {
  constexpr int R = PF + 1;
  v4u wb[R];
  auto pro = [&](auto I) __attribute__((always_inline)) { wb[I.v] = rd(I); };
  sfor<0, PF>(pro);
  mid();
  __builtin_amdgcn_sched_barrier(0);
  auto body = [&](auto I) __attribute__((always_inline)) {
    constexpr int i = I.v;
    st(I, wb[i % R]);
    if constexpr (i + PF < N) wb[(i + PF) % R] = rd(IC<i + PF>{});
    asm volatile("" :: "v"(wb[i % R]));
    __builtin_amdgcn_sched_barrier(0);
  };
  sfor<0, N>(body);
}
constexpr int slot_of(int c) { return (c % 5) * SLOT; }
DEV void fin_stats(const v2u (&st)[7], float& mean, float& rstd) {
  float sm = 0.f;
#pragma unroll
  for (int i = 0; i < 7; ++i) sm += asf(st[i].x);
  mean = xsum32(sm) * (1.f / D_);
  float m2 = 0.f;
#pragma unroll
  for (int i = 0; i < 7; ++i) {
    const float d = asf(st[i].x) * (1.f / 32.f) - mean;
    m2 += asf(st[i].y) + 32.f * d * d;
  }
  rstd = __builtin_amdgcn_rsqf(xsum32(m2) * (1.f / D_) + 1e-5f);
}
DEV v4u pack8(const f32x16& a, int o, float sc) {
  return v4u{pk(a[o] * sc, a[o + 1] * sc), pk(a[o + 2] * sc, a[o + 3] * sc), pk(a[o + 4] * sc, a[o + 5] * sc), pk(a[o + 6] * sc, a[o + 7] * sc)};
}
DEV v4u swiglu(const f32x16& a, float rstd) {
  float hv[8];
#pragma unroll
  for (int i = 0; i < 8; ++i) {
    const float g = a[i] * rstd, up = a[8 + i] * rstd;
    const float e = __builtin_amdgcn_exp2f(g * -1.4426950408889634f);
    hv[i] = g * up * __builtin_amdgcn_rcpf(1.f + e);
  }
  return v4u{pk(hv[0], hv[1]), pk(hv[2], hv[3]), pk(hv[4], hv[5]), pk(hv[6], hv[7])};
}
#define TS(i)

template <bool W8> DEV void res_epilogue(f32x16 (&acc)[2], v4u (&res)[2], LAS char* lds, rsrc_t rs, char* ws, unsigned short* out, int w, int lane,
                      int g, int m, int tt, int nt, int xb, bool active, bool last, unsigned flagval) {
  LAS char* sx = lds + (W8 ? 3 * SLOT : L_EX) + lane * 16;
  float v[16];
#pragma unroll
  for (int q = 0; q < 8; ++q) { const unsigned r = res[q >> 2][q & 3]; v[2 * q] = acc[0][2 * q] + blo(r); v[2 * q + 1] = acc[0][2 * q + 1] + bhi(r); }
#pragma unroll
  for (int q = 0; q < 4; ++q) *(LAS f32x4*)(sx + w * 4096 + q * 1024) = f32x4{acc[1][4 * q], acc[1][4 * q + 1], acc[1][4 * q + 2], acc[1][4 * q + 3]};
  if (W8 && w >= 4) {
#pragma unroll
    for (int q = 0; q < 4; ++q) *(LAS f32x4*)(sx + (w + 4) * 4096 + q * 1024) = f32x4{acc[0][4 * q], acc[0][4 * q + 1], acc[0][4 * q + 2], acc[0][4 * q + 3]};
  }
  BAR();
  if (W8 && w >= 4) return;
#pragma unroll
  for (int q = 0; q < 4; ++q) {
    f32x4 p0 = *(LAS f32x4*)(sx + (w ^ 2) * 4096 + q * 1024);
    if (W8) p0 += *(LAS f32x4*)(sx + (w ^ 6) * 4096 + q * 1024) + *(LAS f32x4*)(sx + (w + 8) * 4096 + q * 1024);
#pragma unroll
    for (int e = 0; e < 4; ++e) v[4 * q + e] += p0[e];
  }
#pragma unroll
  for (int q = 0; q < 8; ++q) res[q >> 2][q & 3] = pk(v[2 * q], v[2 * q + 1]);
  if (!active) return;
  if (last) {
    const int s = 2 * (128 * (g & 7) + 32 * (tt & 3) + (lane & 31)) + (g >> 3);
    unsigned short* rowp = out + (size_t)s * D_ + 32 * nt + 8 * (lane >> 5);
#pragma unroll
    for (int q = 0; q < 2; ++q) {
      auto a = __builtin_amdgcn_permlane32_swap(res[q][0], res[q][2], false, false);
      auto b = __builtin_amdgcn_permlane32_swap(res[q][1], res[q][3], false, false);
      *(v4u*)(rowp + 16 * q) = v4u{(unsigned)a[0], (unsigned)b[0], (unsigned)a[1], (unsigned)b[1]};
    }
    return;
  }
  st16(res[0], rs, lane * 16, OFF_X + xb * XBUF + (tt * 28 + 2 * nt) * 1024);
  st16(res[1], rs, lane * 16 + 1024, OFF_X + xb * XBUF + (tt * 28 + 2 * nt) * 1024);
  __builtin_amdgcn_sched_barrier(0);
  float s0 = 0.f, s1 = 0.f, q0 = 0.f, q1 = 0.f;
#pragma unroll
  for (int q = 0; q < 8; ++q) { s0 += v[2 * q]; s1 += v[2 * q + 1]; q0 += v[2 * q] * v[2 * q]; q1 += v[2 * q + 1] * v[2 * q + 1]; }
  const float sm = xsum32(s0 + s1), sq = xsum32(q0 + q1);
  const float m2 = fmaxf(sq - sm * sm * (1.f / 32.f), 0.f);
  if (lane < 32) st8(v2u{__builtin_bit_cast(unsigned, sm), __builtin_bit_cast(unsigned, m2)}, rs, lane * 8, OFF_ST + xb * SBUF + (tt * 14 + nt) * 256);
  publish(ws, (g * 16 + m) * 4 + w, flagval, lane);
}

__global__ __launch_bounds__(512, 2) void k_main(const char* x, const char* wqkv, const char* wo, const char* wg_, const char* wu_,
                                                  const char* wd_, char* ws, unsigned short* out) {
  extern __shared__ __attribute__((aligned(16))) char smem[];
  LAS char* lds = (LAS char*)smem;
  const int tid = threadIdx.x; int lane = tid & 63;
  int w = __builtin_amdgcn_readfirstlane(tid >> 6);
  const int b = blockIdx.x, xcd = b & 7, idx = b >> 3;
  int g = (xcd >> 1) * 4 + (idx >> 3), m = (xcd & 1) * 8 + (idx & 7);
  const int half = m >> 3, j = (m & 7) < 7 ? (m & 7) : 6;
  const bool active = (m & 7) < 7;
  const rsrc_t rs = __builtin_amdgcn_make_buffer_rsrc((void*)ws, 0, WS_BYTES, 0x00020000);
  TS(0);
  const unsigned tag = __hip_atomic_load((GAS unsigned*)(ws + OFF_EP), __ATOMIC_RELAXED, __HIP_MEMORY_SCOPE_AGENT) + 1u;
  auto poll_setup = [&](int base, auto NC) AI {
    for (int spin = 0; spin < (1 << 20); ++spin) {
      bool ok = true;
#pragma unroll
      for (int i = 0; i < NC.v; ++i) ok &= __builtin_amdgcn_raw_buffer_load_b32(rs, lane * 128, base + i * 8192, AUX_SC1) == tag;
      if (__all(ok)) break;
      asm volatile("s_sleep 2" ::: "memory");
    }
    __builtin_amdgcn_fence(__ATOMIC_ACQUIRE, "wavefront");
  };
  v4u res[2];
  if (w < 4) {
    if (lane == 0) __hip_atomic_store((GAS unsigned*)(ws + OFF_FLAG) + ((g * 16 + m) * 4 + w) * 16, 0u, __ATOMIC_RELAXED, __HIP_MEMORY_SCOPE_AGENT);
    LAS unsigned* cnt = (LAS unsigned*)(lds + L_RS);
    if (tid == 0) *cnt = 0u;
    pack_unit(b < 21 ? b : b < 127 ? b + 56 : -1, tid, lds, rs, x, wqkv, wo, wg_, wu_, wd_);
    VMWAIT(0);
    if (lane == 0 && __hip_atomic_fetch_add(cnt, 1u, __ATOMIC_RELAXED, __HIP_MEMORY_SCOPE_WORKGROUP) == 3u)
      __hip_atomic_store((GAS unsigned*)(ws + OFF_PF) + b * 32, tag, __ATOMIC_RELAXED, __HIP_MEMORY_SCOPE_AGENT);
    TS(2);
    BAR();
    if (w == 0) poll_setup(OFF_PF, IC<2>{});
    BAR();
    res[0] = ld16(rs, lane * 16, OFF_X + ((g * 4 + 2 * half + (w & 1)) * 28 + 2 * (2 * j + (w >> 1))) * 1024);
    res[1] = ld16(rs, lane * 16 + 1024, OFF_X + ((g * 4 + 2 * half + (w & 1)) * 28 + 2 * (2 * j + (w >> 1))) * 1024);
  } else {
    res[0] = res[1] = v4u{0u, 0u, 0u, 0u};
    BAR();
    if (b >= 127) __builtin_amdgcn_s_sleep(QDELAY);
    pack_unit(b < 16 ? -1 : b < 200 ? 167 + b : b - 179, tid & 255, lds + 3 * SLOT, rs, x, wqkv, wo, wg_, wu_, wd_);
    VMWAIT(0);
    BAR();
    if (tid == 256) __hip_atomic_store((GAS unsigned*)(ws + OFF_PF + 32768) + b * 32, tag, __ATOMIC_RELAXED, __HIP_MEMORY_SCOPE_AGENT);
    {
      const int hw = w - 4, l16 = lane * 16, wa = OFF_WA + m * 84 * 1024;
      dma<1, -1>(rs, OFF_CS + m * 3072, L_CS, 0, l16);
      DMA(wa, 0); DMA(wa + SLOT, 1); DMA(wa + 2 * SLOT, 2);
    }
  }
  auto run = [&](auto QRC) AI {
  constexpr bool QR = QRC.v;
  v4u wr[28];
  if constexpr (QR) {
#pragma unroll
    for (int i = 0; i < 28; ++i) wr[i] = v4u{0u, 0u, 0u, 0u};
  }
  for (int L = 0; L < 2; ++L) {
    OPQ_S(w); OPQ_S(g); OPQ_S(m); OPQ_V(lane);
    const int t = lane & 31, h = lane >> 5;
    const int mw = w & 1, kh = (w >> 1) & 1, par = w >> 2, hw = w & 3, l16 = lane * 16;
    const int wa = OFF_WA + m * 84 * 1024, wb = OFF_WB + j * 64 * 1024, wc = OFF_WC + m * 196 * 1024, wd = OFF_WD + j * 224 * 1024;
    const int ttA = g * 4 + w;
    const int ttB = g * 4 + 2 * half + mw;
    const int ntB = 2 * j + kh;
    const int myflag = (g * 16 + m) * 4 + w;
    LAS char* lb[3] = {lds + lane * 16, lds + 2 * SLOT + lane * 16, lds + 4 * SLOT + lane * 16};
    OPQ_V(lb[1]); OPQ_V(lb[2]);
#define LSL(c) (lb[((c) % 5) >> 1] + (((c) % 5) & 1) * SLOT)
    if constexpr (QR) {
      {
        const bool hasprev = (g & 7) > 0;
        const int gp = hasprev ? g - 1 : g, ttp = gp * 4 + 3;
        VMWAIT(0);
        BAR();
        if (L) poll(ws, fi_x(gp, 3, lane), 4);
        const int t16 = lane & 15, gq = lane >> 4, tk = 24 + (t16 & 7), isv = hw >> 1, nh = hw & 1;
        v4u xf[14]; v2u st[4];
#pragma unroll
        for (int i = 0; i < 4; ++i) st[i] = ld8(rs, ((gq + 4 * i < 14 ? gq + 4 * i : 0) * 32 + tk) * 8, OFF_ST + 2 * L * SBUF + ttp * 14 * 256);
#pragma unroll
        for (int i = 0; i < 14; ++i) xf[i] = ld16(rs, (gq >> 1) * 1024 + ((gq & 1) * 32 + tk) * 16, OFF_X + 2 * L * XBUF + (ttp * 28 + 2 * i) * 1024);
        if (L) { DMA(wb, 3); DMA(wb + SLOT, 4); }
        float sm = 0.f;
#pragma unroll
        for (int i = 0; i < 4; ++i) sm += (gq + 4 * i < 14) ? asf(st[i].x) : 0.f;
        sm += __shfl_xor(sm, 16); sm = xsum32(sm);
        const float mean = sm * (1.f / D_);
        float m2 = 0.f;
#pragma unroll
        for (int i = 0; i < 4; ++i) {
          const float d = asf(st[i].x) * (1.f / 32.f) - mean;
          m2 += (gq + 4 * i < 14) ? asf(st[i].y) + 32.f * d * d : 0.f;
        }
        m2 += __shfl_xor(m2, 16); m2 = xsum32(m2);
        const float rstd = __builtin_amdgcn_rsqf(m2 * (1.f / D_) + 1e-5f);
        LAS char* wp = lds + (1 + isv) * SLOT + (gq >> 1) * 1024 + ((gq & 1) * 32 + 16 * nh + t16) * 16;
        const v4u xa = aug_x(-mean, gq == 0), wg_ = aug_w(*(LAS v2u*)(lds + L_CS + (1 + isv) * 256 + (16 * nh + t16) * 8));
        f32x4 a = {0.f, 0.f, 0.f, 0.f};
        if (isv) {
#pragma unroll
          for (int i = 0; i < 14; ++i) a = mfma16(xf[i], lds16(wp + i * 2048), a);
          a = mfma16(xa, wg_, a);
          float r[4];
#pragma unroll
          for (int i = 0; i < 4; ++i) r[i] = __shfl(rstd, 4 * gq + i);
          *(LAS v2u*)(lds + L_EX + 2048 + (16 * nh + t16 + 32 * (gq & 1)) * 16 + 8 * (gq >> 1)) = v2u{pk(a[0] * r[0], a[1] * r[1]), pk(a[2] * r[2], a[3] * r[3])};
        } else {
#pragma unroll
          for (int i = 0; i < 14; ++i) a = mfma16(lds16(wp + i * 2048), xf[i], a);
          a = mfma16(wg_, xa, a);
          *(LAS v2u*)(lds + L_EX + nh * 1024 + (16 + t16 + 32 * (gq & 1)) * 16 + 8 * (gq >> 1)) = v2u{pk(a[0] * rstd, a[1] * rstd), pk(a[2] * rstd, a[3] * rstd)};
        }
      }
      if (L == 0) {
        poll_setup(OFF_PF + 32768 + hw * 8192, IC<1>{});
        BAR();
        if (b == 0 && tid == 256) __hip_atomic_store((GAS unsigned*)(ws + OFF_EP), tag + 1u, __ATOMIC_RELAXED, __HIP_MEMORY_SCOPE_AGENT);
        dma<1, -1>(rs, OFF_CS + m * 3072, L_CS, hw < 3 ? hw : 2, l16);
        DMA(wb, 3); DMA(wb + SLOT, 4);
      }
      BAR();
      DMA(wc, 5); DMA(wc + SLOT, 6);
      VMWAIT(14);
    } else {
      constexpr int W0 = 12;
      v4u xf[28]; v2u st[7];
      const int xoff = OFF_X + 2 * L * XBUF + ttA * 28 * 1024, soff = OFF_ST + 2 * L * SBUF + ttA * 14 * 256;
      if (L) poll(ws, fi_x(g, w, lane), 4);
      TS(3 + 32 * L);
#pragma unroll
      for (int i = 0; i < W0; ++i) { xf[i] = ldf(rs, lane, xoff, i); __builtin_amdgcn_sched_barrier(0); }
      BAR();
      TS(1 + 32 * L);
      f32x16 aq = {}, ak = {}, av = {};
      float mean, rstd; v4u xa; v2u cw[3]; f32x4 rv[4];
      float bias[20];
      const bool hasprev = w > 0 || (g & 7) > 0;
      const float NEG = -__builtin_inff();
      pipe<84, 6>([&](auto I) AI { constexpr int i = I.v; return i < 84 ? lds16(LSL((i + 1) % 3) + (i / 3) * 1024) : v4u{}; },
                  [&]() AI {},
                  [&](auto I, v4u wf) AI { constexpr int i = I.v;
                    if constexpr (i % 3 == 0) mfma32p(wf, xf[i / 3], ak);
                    else if constexpr (i % 3 == 1) mfma32p(xf[i / 3], wf, av);
                    else mfma32p(wf, xf[i / 3], aq);
                    if constexpr (i % 3 == 0 && i / 3 + W0 < 28) xf[i / 3 + W0] = ldf(rs, lane, xoff, i / 3 + W0);
                    if constexpr (i % 3 == 1 && i / 3 < 7) st[i / 3] = ld8(rs, ((7 * h + i / 3) * 32 + t) * 8, soff);
                    if constexpr (i == 70) { if (L == 0) BAR(); }
                    if constexpr (i >= 2 && i < 62 && i % 3 == 2) {
                      constexpr int r = i / 3;
                      if constexpr (r < 16) bias[r] = ((unsigned)(t - 4 * h - ((r & 3) + 8 * (r >> 2))) <= 7u) ? 0.f : NEG;
                      else bias[r] = (hasprev && r - 17 + 4 * h >= t) ? 0.f : NEG;
                      OPQ_V(bias[r]);
                    }
                    if constexpr (i == 62) { fin_stats(st, mean, rstd); OPQ_V(mean); OPQ_V(rstd); }
                    if constexpr (i == 66) { xa = aug_x(-mean, h == 0); OPQ_V(xa); *(LAS float*)(lds + L_RS + w * 128 + t * 4) = rstd; }
                    if constexpr (i >= 66 && i < 69) cw[i - 66] = *(LAS v2u*)(lds + L_CS + (i - 66) * 256 + t * 8);
                    if constexpr (i == 72) mfma32p(aug_w(cw[1]), xa, ak);
                    if constexpr (i == 73) mfma32p(xa, aug_w(cw[2]), av);
                    if constexpr (i == 74) mfma32p(aug_w(cw[0]), xa, aq);
                    if constexpr (i >= 78 && i < 82) rv[i - 78] = *(LAS f32x4*)(lds + L_RS + w * 128 + (8 * (i - 78) + 4 * h) * 4);
                  });
      TS(4 + 32 * L);
      const v4u kf0 = pack8(ak, 0, rstd), kf1 = pack8(ak, 8, rstd);
      v4u vf[2];
#pragma unroll
      for (int q = 0; q < 4; ++q) {
        vf[q >> 1][(q & 1) * 2] = pk(av[4 * q] * rv[q][0], av[4 * q + 1] * rv[q][1]);
        vf[q >> 1][(q & 1) * 2 + 1] = pk(av[4 * q + 2] * rv[q][2], av[4 * q + 3] * rv[q][3]);
      }
      if (w < 3) {
        LAS char* e = lds + L_EX + (w + 1) * 3072 + lane * 16;
        *(LAS v4u*)e = kf0; *(LAS v4u*)(e + 1024) = kf1; *(LAS v4u*)(e + 2048) = vf[1];
      }
      const v4u qf0 = pack8(aq, 0, 1.f), qf1 = pack8(aq, 8, 1.f);
      f32x16 sc;
#pragma unroll
      for (int r = 0; r < 16; ++r) sc[r] = bias[r];
      mfma32p(kf0, qf0, sc); mfma32p(kf1, qf1, sc);
      TS(5 + 32 * L);
      BAR();
      TS(6 + 32 * L);
      LAS char* e = lds + L_EX + w * 3072 + lane * 16;
      f32x16 sp = {};
#pragma unroll
      for (int r = 0; r < 4; ++r) sp[12 + r] = bias[16 + r];
      mfma32p(lds16(e), qf0, sp); mfma32p(lds16(e + 1024), qf1, sp);
      const v4u vp = lds16(e + 2048);
      float p[16], pp[4], mx = NEG;
#pragma unroll
      for (int r = 0; r < 16; ++r) { p[r] = sc[r]; mx = fmaxf(mx, p[r]); }
#pragma unroll
      for (int r = 0; r < 4; ++r) { pp[r] = sp[12 + r]; mx = fmaxf(mx, pp[r]); }
      const float cf = rstd * (0.18898223650461362f * 1.4426950408889634f);
      const float mc = -xmax32(mx) * cf;
#pragma unroll
      for (int r = 0; r < 16; ++r) p[r] = __builtin_amdgcn_exp2f(__builtin_fmaf(p[r], cf, mc));
#pragma unroll
      for (int r = 0; r < 4; ++r) pp[r] = __builtin_amdgcn_exp2f(__builtin_fmaf(pp[r], cf, mc));
      const v4u pf0 = {pk(p[0], p[1]), pk(p[2], p[3]), pk(p[4], p[5]), pk(p[6], p[7])};
      const v4u pf1 = {pk(p[8], p[9]), pk(p[10], p[11]), pk(p[12], p[13]), pk(p[14], p[15])};
      const v4u pfp = {0u, 0u, pk(pp[0], pp[1]), pk(pp[2], pp[3])};
      f32x16 o = {};
      mfma32p(vf[0], pf0, o); mfma32p(vf[1], pf1, o); mfma32p(vp, pfp, o);
      float s0 = pp[0] + pp[1], s1 = pp[2] + pp[3];
#pragma unroll
      for (int r = 0; r < 8; ++r) { s0 += p[2 * r]; s1 += p[2 * r + 1]; }
      const float inv = __builtin_amdgcn_rcpf(xsum32(s0 + s1));
      st16(pack8(o, 0, inv), rs, lane * 16, OFF_O + L * OBUF + (ttA * 32 + 2 * m) * 1024);
      st16(pack8(o, 8, inv), rs, lane * 16 + 1024, OFF_O + L * OBUF + (ttA * 32 + 2 * m) * 1024);
      TS(7 + 32 * L);
      publish(ws, myflag, 4 * L + 1, lane);
      TS(8 + 32 * L);
    }
    if constexpr (QR) {
      if (active) BAR();
      DMAT(wc + 2 * SLOT, 7);
      if (active) BAR();
      VMWAIT(0);
    } else if (active) {
      constexpr int W0 = 12;
      v4u of[16];
      const int ooff = OFF_O + L * OBUF + (ttB * 32 + 16 * kh) * 1024;
      BAR();
      TS(11 + 32 * L);
      v4u wl[4];
#pragma unroll
      for (int i = 0; i < 4; ++i) wl[i] = __builtin_amdgcn_raw_buffer_load_b128(rs, lane * 16 + (i & 2) * 1024, OFF_WB + (j * 64 + 56 + kh * 4 + ((i & 1) ^ kh ^ 1)) * 1024, AUX_SC1);
      f32x16 acc[2] = {{}, {}};
      LAS char* wp0 = lds + slot_of(3) + kh * 15 * 1024 + lane * 16; LAS char* wp1 = lds + slot_of(3) + 1024 + kh * 13 * 1024 + lane * 16;
      OPQ_V(wp0); OPQ_V(wp1);
      pipe<32, 6>([&](auto I) AI { constexpr int i = I.v;
                    if constexpr (i < 28) return lds16(((i & 1) ? wp0 : wp1) + (i / 14) * SLOT + ((i % 14) >> 1) * 2048);
                    else return wl[i & 3]; },
                  [&]() AI {
                    poll(ws, fi_k(g, kh, ttB & 3, lane), 4 * L + 1);
                    TS(9 + 32 * L);
#pragma unroll
                    for (int i = 0; i < W0; ++i) { of[i] = ldf(rs, lane, ooff, i); __builtin_amdgcn_sched_barrier(0); }
                  },
                  [&](auto I, v4u wf) AI { constexpr int i = I.v;
                    mfma32p(wf, of[i >> 1], acc[(i & 1) ^ 1]);
                    if constexpr (i + W0 < 16) of[i + W0] = ldf(rs, lane, ooff, i + W0);
                  });
      TS(12 + 32 * L);
      res_epilogue<false>(acc, res, lds, rs, ws, out, w, lane, g, m, ttB, ntB, 2 * L + 1, active, false, 4 * L + 2);
      TS(13 + 32 * L);
    }
    auto phase_d = [&](auto PARC) AI {
      constexpr int PAR = PARC.v, W0 = 14;
      const int hoff = OFF_H + L * HBUF + (ttB * 112 + 56 * kh + 7 * PAR) * 1024;
      v4u hf[W0];
      f32x16 acc[2] = {{}, {}};
      LAS char* db[2];
      pipe<56, 6>(
          [&](auto I) AI { constexpr int i = I.v, r = i / 14;
            if constexpr (i % 14 == 0) {
              if constexpr (r < 2) BAR();
              if constexpr (r == 0) TS(27 + 32 * L);
              if constexpr (PAR && r == 0) DMA(wd + 4 * SLOT, 16);
              if constexpr (PAR && r == 1) DMA(wd + 6 * SLOT, 12);
              constexpr int sl = slot_of(r == 3 ? 12 : 12 + PAR + 2 * r);
              db[1] = lds + sl + kh * 15 * 1024 + l16; db[0] = lds + sl + 1024 + kh * 13 * 1024 + l16;
            }
            if constexpr (i == (PAR ? DQ : 42)) { if constexpr (PAR) VMWAIT(3); TS(28 + 32 * L); BAR(); TS(29 + 32 * L); }
            if constexpr (PAR && r >= 2) return wr[i - 28]; else return lds16(db[i & 1] + ((i % 14) >> 1) * 2048);
          },
          [&]() AI {
            poll(ws, (g * 16 + 8 * kh + PAR + 2 * (lane & 3)) * 4 + (ttB & 3), 4 * L + 3);
            TS(25 + 32 * L);
#pragma unroll
            for (int i = 0; i < W0; ++i) { hf[i] = ldf(rs, lane, hoff + (i / 7) * 14336, i % 7); __builtin_amdgcn_sched_barrier(0); }
          },
          [&](auto I, v4u wf) AI { constexpr int i = I.v;
            constexpr int k = i >> 1;
            mfma32p(wf, hf[k % W0], acc[(i & 1) ^ 1]);
            if constexpr ((i & 1) && k + W0 < 28) hf[k % W0] = ldf(rs, lane, hoff + ((k + W0) / 7) * 14336, (k + W0) % 7);
          });
      TS(30 + 32 * L);
      if constexpr (!PAR) { if (L == 0) { DMA(wa, 0); DMA(wa + SLOT, 1); } }
      res_epilogue<true>(acc, res, lds, rs, ws, out, w, lane, g, m, ttB, ntB, 2, active, L == 1, 4 * L + 4);
      TS(31 + 32 * L);
    };
    if constexpr (QR) {
      BAR();
      DMAT(wc + 3 * SLOT, 8); DMAT(wc + 4 * SLOT, 9);
      if (L == 0) {
#pragma unroll
        for (int i = 0; i < 14; ++i) { wr[14 + i] = __builtin_amdgcn_raw_buffer_load_b128(rs, l16, wd + 7 * SLOT + (kh * 14 + (i & ~1) + ((i & 1) ^ kh ^ 1)) * 1024, AUX_SC1); __builtin_amdgcn_sched_barrier(0); }
        VMWAIT(14);
      } else VMWAIT(0);
      BAR();
      DMA(wc + 5 * SLOT, 10); DMA(wc + 6 * SLOT, 11); DMA(wd, 12);
      if (L == 0) {
#pragma unroll
        for (int i = 0; i < 14; ++i) { wr[i] = __builtin_amdgcn_raw_buffer_load_b128(rs, l16, wd + 5 * SLOT + (kh * 14 + (i & ~1) + ((i & 1) ^ kh ^ 1)) * 1024, AUX_SC1); __builtin_amdgcn_sched_barrier(0); }
        VMWAIT(21);
      } else VMWAIT(7);
      BAR();
      DMA(wd + SLOT, 13); DMA(wd + 2 * SLOT, 14);
      BAR();
      DMA(wd + 3 * SLOT, 15);
      VMWAIT(7);
      __builtin_amdgcn_s_setprio(QPRIO);
      if (active) phase_d(IC<1>{}); else { VMWAIT(0); BAR(); }
      __builtin_amdgcn_s_setprio(0);
      if (L == 0) DMAT(wa + 2 * SLOT, 2);
    } else {
      constexpr int W0 = 12;
      v4u xf[28]; v2u st[7];
      const int xoff = OFF_X + (2 * L + 1) * XBUF + ttA * 28 * 1024, soff = OFF_ST + (2 * L + 1) * SBUF + ttA * 14 * 256;
      BAR();
      TS(16 + 32 * L);
      const int hoff = OFF_H + L * HBUF + (ttA * 112 + 7 * m) * 1024;
      LAS char* cs = lds + L_CS + 768 + t * 8;
      f32x16 a[4] = {{}, {}, {}, {}};
      float mean, rstd, c1, kk; v4u xa; float hv[8], ev = 0.f, tv = 0.f; v2u cw[7];
      auto epi = [&](const f32x16& acc, int e, int tile) AI {
        const int q = e >> 1;
        if (e & 1) {
          if (q > 0) { hv[q - 1] *= ev; OPQ_V(hv[q - 1]); }
          if (q < 8) { ev = __builtin_amdgcn_exp2f(tv); hv[q] = acc[q] * acc[8 + q]; OPQ_V(ev); OPQ_V(hv[q]); }
        } else if (e < 18) {
          if (q > 0) ev = __builtin_fmaf(ev, kk, kk);
          if (q < 8) { tv = acc[q] * c1; OPQ_V(tv); }
          if (q > 0) { ev = __builtin_amdgcn_rcpf(ev); OPQ_V(ev); }
        } else st16(v4u{pk(hv[0], hv[1]), pk(hv[2], hv[3]), pk(hv[4], hv[5]), pk(hv[6], hv[7])}, rs, lane * 16 + (tile & 3) * 1024, hoff + (tile >> 2) * 4096);
      };
      pipe<196, 5>(
          [&](auto I) AI { constexpr int i = I.v;
            if constexpr (i == 84 || i == 140) { TS(17 + 2 * ((i - 84) / 56) + 32 * L); BAR(); TS(18 + 2 * ((i - 84) / 56) + 32 * L); }
            if constexpr (i == 168) BAR();
            constexpr int tile = i < 84 ? i % 3 : i / 28;
            constexpr int ks = i < 84 ? i / 3 : i % 28;
            return i < 196 ? lds16(LSL(5 + tile) + ks * 1024) : v4u{};
          },
          [&]() AI {
            poll(ws, fi_x(g, w, lane), 4 * L + 2);
            TS(14 + 32 * L);
#pragma unroll
            for (int i = 0; i < W0; ++i) { xf[i] = ldf(rs, lane, xoff, i); __builtin_amdgcn_sched_barrier(0); }
          },
          [&](auto I, v4u wf) AI { constexpr int i = I.v;
            constexpr int tile = i < 84 ? i % 3 : i / 28;
            constexpr int ks = i < 84 ? i / 3 : i % 28;
            if constexpr (i >= 84 && ks == 0) { a[tile & 3] = f32x16{}; mfma32p(aug_w(cw[tile]), xa, a[tile & 3]); }
            mfma32p(wf, xf[ks], a[tile & 3]);
            if constexpr (i < 84 && i % 3 == 0 && i / 3 + W0 < 28) xf[i / 3 + W0] = ldf(rs, lane, xoff, i / 3 + W0);
            if constexpr (i % 3 == 1 && i / 3 < 7) st[i / 3] = ld8(rs, ((7 * h + i / 3) * 32 + t) * 8, soff);
            if constexpr (i >= 70 && i < 73) cw[i - 70] = *(LAS v2u*)(cs + (i - 70) * 256);
            if constexpr (i >= 78 && i % 28 == 22 && i < 170) cw[i / 28 + 1] = *(LAS v2u*)(cs + (i / 28 + 1) * 256);
            if constexpr (i == 62) { fin_stats(st, mean, rstd); OPQ_V(mean); OPQ_V(rstd); }
            if constexpr (i == 66) { xa = aug_x(-mean, h == 0); c1 = rstd * -1.4426950408889634f; kk = __builtin_amdgcn_rcpf(rstd * rstd); OPQ_V(xa); OPQ_V(c1); OPQ_V(kk); }
            if constexpr (i >= 75 && i < 78) mfma32p(aug_w(cw[i - 75]), xa, a[i - 75]);
            if constexpr (i >= 83) epi(a[((i - 83) / 19) & 3], (i - 83) % 19, (i - 83) / 19);
          });
      TS(21 + 32 * L);
      epi(a[1], 18, 5);
      st16(swiglu(a[2], rstd), rs, lane * 16 + 2048, hoff + 4096);
      TS(23 + 32 * L);
      publish(ws, myflag, 4 * L + 3, lane);
      TS(24 + 32 * L);
      if (active) phase_d(IC<0>{}); else { BAR(); if (L == 0) { DMA(wa, 0); DMA(wa + SLOT, 1); } }
    }
  }
  };
  if (w >= 4) run(IC<1>{}); else run(IC<0>{});
}

extern "C" void kernel_launch(void* const* d_in, const int* in_sizes, int n_in, void* d_out, int out_size, void* d_ws,
                              size_t ws_size, hipStream_t stream) {
  (void)in_sizes; (void)n_in; (void)out_size; (void)ws_size;
  (void)hipFuncSetAttribute((const void*)k_main, hipFuncAttributeMaxDynamicSharedMemorySize, LDS_BYTES);
  k_main<<<256, 512, LDS_BYTES, stream>>>((const char*)d_in[0], (const char*)d_in[1], (const char*)d_in[2], (const char*)d_in[4],
                                          (const char*)d_in[5], (const char*)d_in[6], (char*)d_ws, (unsigned short*)d_out);
}
```

```cpp
#include <hip/hip_runtime.h>
#include <hip/hip_bf16.h>

typedef unsigned v4u __attribute__((ext_vector_type(4)));
typedef unsigned v2u __attribute__((ext_vector_type(2)));
typedef float f32x4 __attribute__((ext_vector_type(4)));
typedef float f32x2 __attribute__((ext_vector_type(2)));
typedef float f32x16 __attribute__((ext_vector_type(16)));
typedef __bf16 bf16x8 __attribute__((ext_vector_type(8)));
typedef __bf16 bf16x2 __attribute__((ext_vector_type(2)));
#define LAS __attribute__((address_space(3)))
#define GAS __attribute__((address_space(1)))
#define DEV __device__ __forceinline__
#define AI __attribute__((always_inline))

constexpr int S_ = 2048, D_ = 448, F_ = 1792;
constexpr int OFF_FLAG = 0;
constexpr int OFF_PF = 65536, OFF_EP = OFF_PF + 65536;
constexpr int OFF_WA = 135168;
constexpr int OFF_WB = OFF_WA + 16 * 84 * 1024;
constexpr int OFF_WC = OFF_WB + 7 * 64 * 1024;
constexpr int OFF_WD = OFF_WC + 16 * 196 * 1024;
constexpr int OFF_CS = OFF_WD + 7 * 224 * 1024;
constexpr int XBUF = 64 * 28 * 1024, SBUF = 64 * 14 * 256, OBUF = 64 * 32 * 1024, HBUF = 64 * 112 * 1024;
constexpr int OFF_X = OFF_CS + 16 * 3072;
constexpr int OFF_ST = OFF_X + 4 * XBUF;
constexpr int OFF_O = OFF_ST + 4 * SBUF;
constexpr int OFF_H = OFF_O + 2 * OBUF;
constexpr int WS_BYTES = OFF_H + 2 * HBUF;
constexpr int SLOT = 28 * 1024;
constexpr int L_EX = 5 * SLOT, L_CS = L_EX + 16384, L_RS = L_CS + 3072, LDS_BYTES = L_RS + 512;
static_assert(LDS_BYTES <= 163840, "lds");
constexpr int AUX_SC1 = 16;

DEV unsigned pk(float a, float b) { bf16x2 t = {(__bf16)a, (__bf16)b}; return __builtin_bit_cast(unsigned, t); }
DEV float asf(unsigned u) { return __builtin_bit_cast(float, u); }
DEV float blo(unsigned u) { return __builtin_bit_cast(float, u << 16); }
DEV float bhi(unsigned u) { return __builtin_bit_cast(float, u & 0xffff0000u); }
DEV f32x16 mfma32(v4u a, v4u b, f32x16 c) {
  return __builtin_amdgcn_mfma_f32_32x32x16_bf16(__builtin_bit_cast(bf16x8, a), __builtin_bit_cast(bf16x8, b), c, 0, 0, 0);
}
DEV void mfma32p(v4u a, v4u b, f32x16& c) { c = mfma32(a, b, c); asm volatile("" : "+v"(c)); __builtin_amdgcn_sched_barrier(0); }
DEV f32x4 mfma16(v4u a, v4u b, f32x4 c) {
  return __builtin_amdgcn_mfma_f32_16x16x32_bf16(__builtin_bit_cast(bf16x8, a), __builtin_bit_cast(bf16x8, b), c, 0, 0, 0);
}
DEV float xsum32(float v) {
  auto r = __builtin_amdgcn_permlane32_swap(__builtin_bit_cast(unsigned, v), __builtin_bit_cast(unsigned, v), false, false);
  return __builtin_bit_cast(float, (unsigned)r[0]) + __builtin_bit_cast(float, (unsigned)r[1]);
}
DEV float xmax32(float v) {
  auto r = __builtin_amdgcn_permlane32_swap(__builtin_bit_cast(unsigned, v), __builtin_bit_cast(unsigned, v), false, false);
  return fmaxf(__builtin_bit_cast(float, (unsigned)r[0]), __builtin_bit_cast(float, (unsigned)r[1]));
}
DEV void split3(float v, unsigned& hi, unsigned& mid, unsigned& lo) {
  hi = __builtin_bit_cast(unsigned, v) & 0xffff0000u;
  float r = v - __builtin_bit_cast(float, hi);
  mid = __builtin_bit_cast(unsigned, r) & 0xffff0000u;
  float r2 = r - __builtin_bit_cast(float, mid);
  lo = __builtin_bit_cast(unsigned, r2) & 0xffff0000u;
}
DEV v4u aug_x(float negmean, bool on) {
  unsigned hi, mid, lo; split3(negmean, hi, mid, lo);
  v4u r = {hi | (hi >> 16), mid | (mid >> 16), lo | (hi >> 16), 0u};
  if (!on) r = v4u{0u, 0u, 0u, 0u};
  return r;
}
DEV v4u aug_w(v2u c) { return v4u{c.x, c.x, c.y, 0u}; }

typedef short s16x4 __attribute__((ext_vector_type(4)));
typedef __amdgpu_buffer_rsrc_t rsrc_t;
#define BAR() asm volatile("s_waitcnt lgkmcnt(0)\n\ts_barrier" ::: "memory")
#define VMWAIT(n) asm volatile("s_waitcnt vmcnt(" #n ")" ::: "memory")
DEV v4u ld16(rsrc_t rs, int voff, int soff) { return __builtin_amdgcn_raw_buffer_load_b128(rs, voff, soff, AUX_SC1); }
DEV v2u ld8(rsrc_t rs, int voff, int soff) { return __builtin_amdgcn_raw_buffer_load_b64(rs, voff, soff, AUX_SC1); }
DEV void st16(v4u v, rsrc_t rs, int voff, int soff) { __builtin_amdgcn_raw_buffer_store_b128(v, rs, voff + soff, 0, AUX_SC1); }
DEV void st8(v2u v, rsrc_t rs, int voff, int soff) { __builtin_amdgcn_raw_buffer_store_b64(v, rs, voff, soff, AUX_SC1); }
DEV v4u ldf(rsrc_t rs, int lane, int base, int i) { return ld16(rs, lane * 16 + (i & 3) * 1024, base + (i >> 2) * 4096); }
DEV v4u lds16(LAS char* p) { return *(LAS v4u*)p; }

DEV v2u tr8(LAS char* p) { return __builtin_bit_cast(v2u, __builtin_amdgcn_ds_read_tr16_b64_v4i16((LAS s16x4*)p)); }
DEV void pack_unit(int u, int tid, LAS char* sm, rsrc_t rs, const char* x, const char* wqkv, const char* wo, const char* wg,
                   const char* wu, const char* wd) {
  const int w = __builtin_amdgcn_readfirstlane(tid >> 6), lane = tid & 63, n = lane & 31, h = lane >> 5;
  if (u < 0) { BAR(); return; }
  if (u < 77) {
    const char* src; int ld;
    if (u < 21) { src = wqkv + u * 128; ld = 2688; } else if (u < 49) { src = wg + (u - 21) * 128; ld = 3584; } else { src = wu + (u - 49) * 128; ld = 3584; }
    src += (tid >> 3) * ld + (tid & 7) * 16;
    v4u v[14];
#pragma unroll
    for (int i = 0; i < 14; ++i) v[i] = *(const v4u*)(src + i * 32 * ld);
    float c[8] = {0.f, 0.f, 0.f, 0.f, 0.f, 0.f, 0.f, 0.f};
#pragma unroll
    for (int i = 0; i < 14; ++i)
#pragma unroll
      for (int q = 0; q < 4; ++q) { const unsigned e = v[i][q]; c[2 * q] += blo(e); c[2 * q + 1] += bhi(e); }
    *(LAS f32x4*)(sm + (tid >> 3) * 272 + (tid & 7) * 32) = f32x4{c[0], c[1], c[2], c[3]};
    *(LAS f32x4*)(sm + (tid >> 3) * 272 + (tid & 7) * 32 + 16) = f32x4{c[4], c[5], c[6], c[7]};
    BAR();
    if (tid < 64) {
      float cs = 0.f;
#pragma unroll
      for (int i = 0; i < 32; ++i) cs += *(LAS float*)(sm + i * 272 + tid * 4);
      unsigned hi, mid, lo; split3(cs, hi, mid, lo);
      int o;
      if (u < 21) { const int c_ = u * 64 + tid, t = c_ / 448, r = c_ % 448; o = (r / 28) * 3072 + t * 256 + (r % 28) * 8;
        if (r % 28 < 4) st8(v2u{0u, 0u}, rs, OFF_CS + o + 28 * 8, 0); }
      else { const int f = ((u - 21) % 28) * 64 + tid; o = (f / 112) * 3072 + (3 + (f % 112) / 16) * 256 + ((f & 15) + (u >= 49 ? 16 : 0)) * 8; }
      st8(v2u{(hi >> 16) | mid, (lo >> 16) | hi}, rs, OFF_CS + o, 0);
    }
    return;
  }
  const bool isx = u >= 119 && u < 183;
  const int lstride = isx ? 912 : 960;
  int dst0 = 0, dstep = 0, cstep = 128, col0 = 64 * (w >> 1), row0 = 16 * (w & 1);
  {
    const char* src; int ld = 896;
    if (u < 119) { const int q = u - 77; ld = 2688; src = wqkv + ((q / 3) * 32 + 8 * w) * 2688 + (q % 3) * 896;
      dst0 = OFF_WA + (((w >> 1) * 3 + q % 3) * 28 + 2 * (q / 3) + (w & 1)) * 1024; dstep = 168 * 1024; cstep = 112; col0 = 56 * (w >> 1); }
    else if (u < 183) { const int tt = u - 119, g = tt >> 2; ld = 1792; src = x + (2 * (128 * (g & 7) + 32 * (tt & 3) + 8 * w) + (g >> 3)) * 896; }
    else if (u < 199) { const int s = 2 * (u - 183) + (w & 1), kh = s >> 4, ks = s & 15, c = ks / 7; src = wo + ((u - 183) * 28 + 8 * w) * 896;
      dst0 = OFF_WB + (c * 28 + kh * (c < 2 ? 14 : 4) + (ks % 7) * 2 + (w >> 1)) * 1024; dstep = 64 * 1024; }
    else if (u < 311) { const int q = u - 199; ld = 3584; src = (w < 2 ? wg : wu) + ((q >> 2) * 16 + 8 * (w & 1)) * 3584 + (q & 3) * 896;
      dst0 = OFF_WC + (((q & 3) * 28 + w) * 28 + (q >> 2)) * 1024; dstep = 112 * 1024; col0 = 32 * w; }
    else { const int s = 2 * (u - 311) + (w & 1), kh = s / 56, ks = s % 56; src = wd + ((u - 311) * 32 + 8 * w) * 896;
      dst0 = OFF_WD + ((ks / 7) * 28 + kh * 14 + (ks % 7) * 2 + (w >> 1)) * 1024; dstep = 224 * 1024; }
    const bool pad = u >= 183 && u < 199 && w == 3;
    if (lane < 56) {
      v4u v[8];
#pragma unroll
      for (int i = 0; i < 8; ++i) v[i] = *(const v4u*)(src + ((pad && i >= 4) ? 0 : i * ld) + lane * 16);
#pragma unroll
      for (int i = 0; i < 8; ++i) *(LAS v4u*)(sm + (8 * w + i) * lstride + lane * 16) = (pad && i >= 4) ? v4u{0u, 0u, 0u, 0u} : v[i];
    }
  }
  BAR();
  if (isx) {
    const int tt = u - 119;
#pragma unroll
    for (int it = 0; it < 4; ++it) {
      const int nt = w + 4 * it;
      if (nt >= 14) break;
      v2u a[4];
#pragma unroll
      for (int i = 0; i < 4; ++i) a[i] = *(LAS v2u*)(sm + n * 912 + nt * 64 + i * 16 + h * 8);
      __builtin_amdgcn_raw_buffer_store_b128(v4u{a[0].x, a[0].y, a[1].x, a[1].y}, rs, OFF_X + (tt * 28 + 2 * nt) * 1024 + lane * 16, 0, AUX_SC1);
      __builtin_amdgcn_raw_buffer_store_b128(v4u{a[2].x, a[2].y, a[3].x, a[3].y}, rs, OFF_X + (tt * 28 + 2 * nt + 1) * 1024 + lane * 16, 0, AUX_SC1);
      float sm1 = 0.f;
#pragma unroll
      for (int i = 0; i < 4; ++i) sm1 += blo(a[i].x) + bhi(a[i].x) + blo(a[i].y) + bhi(a[i].y);
      sm1 = xsum32(sm1);
      const float mu = sm1 * (1.f / 32.f);
      float m2 = 0.f;
#pragma unroll
      for (int i = 0; i < 4; ++i) {
        const float d0 = blo(a[i].x) - mu, d1 = bhi(a[i].x) - mu, d2 = blo(a[i].y) - mu, d3 = bhi(a[i].y) - mu;
        m2 += d0 * d0 + d1 * d1 + d2 * d2 + d3 * d3;
      }
      m2 = xsum32(m2);
      if (h == 0) st8(v2u{__builtin_bit_cast(unsigned, sm1), __builtin_bit_cast(unsigned, m2)}, rs, OFF_ST + (tt * 14 + nt) * 256 + n * 8, 0);
    }
    return;
  }
  const int q4 = (lane & 15) >> 2, p4 = lane & 3, cb = (lane >> 4) & 1;
  const bool gu = u >= 199 && u < 311;
  LAS char* pa = sm + ((gu ? 16 * cb : row0) + 4 * h + q4) * 960 + col0 + (gu ? 0 : 32 * cb) + 8 * p4;
  v2u lo[8], hi[8];
#pragma unroll
  for (int i = 0; i < 8; ++i) { LAS char* q = pa + ((i < 7 || u < 119) ? i * cstep : 0); lo[i] = tr8(q); hi[i] = tr8(q + 8 * 960); }
  const bool z = u < 119 && n >= 28;
#pragma unroll
  for (int i = 0; i < 8; ++i)
    if (i < 7 || u < 119) st16(z ? v4u{0u, 0u, 0u, 0u} : v4u{lo[i].x, lo[i].y, hi[i].x, hi[i].y}, rs, lane * 16, dst0 + i * dstep);
}

DEV void poll(char* ws, int fi, unsigned want) {
  GAS unsigned* f = (GAS unsigned*)(ws + OFF_FLAG) + fi * 16;
  for (int spin = 0; spin < (1 << 20); ++spin) {
    const unsigned v = __hip_atomic_load(f, __ATOMIC_RELAXED, __HIP_MEMORY_SCOPE_AGENT);
    if (__all(v >= want)) break;
  }
  __builtin_amdgcn_fence(__ATOMIC_ACQUIRE, "wavefront");
}
DEV int fi_x(int g, int T, int lane) { const int i = lane < 14 ? lane : 0; return (g * 16 + 8 * (T >> 1) + (i >> 1)) * 4 + (T & 1) + 2 * (i & 1); }
DEV int fi_k(int g, int kh, int T, int lane) { return (g * 16 + 8 * kh + (lane & 7)) * 4 + T; }
DEV void publish(char* ws, int fi, unsigned val, int lane) {
  VMWAIT(0);
  if (lane == 0) __hip_atomic_store((GAS unsigned*)(ws + OFF_FLAG) + fi * 16, val, __ATOMIC_RELAXED, __HIP_MEMORY_SCOPE_AGENT);
}
template <int N, int TH> DEV void dma(rsrc_t rs, int src, int dst, int hw, int lane16) {
#pragma unroll
  for (int i = 0; i < N; ++i) {
    unsigned keep;
    asm volatile("s_mov_b32 %0, m0\n\ts_mov_b32 m0, %1\n\ts_nop 0\n\tbuffer_load_dwordx4 %2, %3, %4 offen sc1 lds\n\ts_mov_b32 m0, %0"
                 : "=&s"(keep) : "s"(dst + (hw * N + i) * 1024), "v"(lane16), "s"(rs), "s"(src + (hw * N + i) * 1024) : "memory");
    if constexpr (TH >= 0) asm volatile("s_waitcnt vmcnt(%0)" :: "i"(TH) : "memory");
  }
}
#define THR 4
#define DQ 42
#define QPRIO 1
#define QDELAY 75
#define DMA(src, c) dma<7, -1>(rs, src, slot_of(c), hw, l16)
#define DMAT(src, c) dma<7, THR>(rs, src, slot_of(c), hw, l16)
#define OPQ_S(x) asm volatile("" : "+s"(x))
#define OPQ_V(x) asm volatile("" : "+v"(x))
template <int I> struct IC { static constexpr int v = I; };
template <int I, int N, class F> DEV void sfor(F& f) { if constexpr (I < N) { f(IC<I>{}); sfor<I + 1, N>(f); } }
template <int N, int PF, class RD, class MID, class ST> DEV void pipe(RD rd, MID mid, ST st) {
  constexpr int R = PF + 1;
  v4u wb[R];
  auto pro = [&](auto I) __attribute__((always_inline)) { wb[I.v] = rd(I); };
  sfor<0, PF>(pro);
  mid();
  __builtin_amdgcn_sched_barrier(0);
  auto body = [&](auto I) __attribute__((always_inline)) {
    constexpr int i = I.v;
    st(I, wb[i % R]);
    if constexpr (i + PF < N) wb[(i + PF) % R] = rd(IC<i + PF>{});
    asm volatile("" :: "v"(wb[i % R]));
    __builtin_amdgcn_sched_barrier(0);
  };
  sfor<0, N>(body);
}
constexpr int slot_of(int c) { return (c % 5) * SLOT; }
DEV void fin_stats(const v2u (&st)[7], float& mean, float& rstd) {
  float sm = 0.f;
#pragma unroll
  for (int i = 0; i < 7; ++i) sm += asf(st[i].x);
  mean = xsum32(sm) * (1.f / D_);
  float m2 = 0.f;
#pragma unroll
  for (int i = 0; i < 7; ++i) {
    const float d = asf(st[i].x) * (1.f / 32.f) - mean;
    m2 += asf(st[i].y) + 32.f * d * d;
  }
  rstd = __builtin_amdgcn_rsqf(xsum32(m2) * (1.f / D_) + 1e-5f);
}
DEV v4u pack8(const f32x16& a, int o, float sc) {
  return v4u{pk(a[o] * sc, a[o + 1] * sc), pk(a[o + 2] * sc, a[o + 3] * sc), pk(a[o + 4] * sc, a[o + 5] * sc), pk(a[o + 6] * sc, a[o + 7] * sc)};
}
DEV v4u swiglu(const f32x16& a, float rstd) {
  float hv[8];
#pragma unroll
  for (int i = 0; i < 8; ++i) {
    const float g = a[i] * rstd, up = a[8 + i] * rstd;
    const float e = __builtin_amdgcn_exp2f(g * -1.4426950408889634f);
    hv[i] = g * up * __builtin_amdgcn_rcpf(1.f + e);
  }
  return v4u{pk(hv[0], hv[1]), pk(hv[2], hv[3]), pk(hv[4], hv[5]), pk(hv[6], hv[7])};
}
#define TS(i)

template <bool W8> DEV void res_epilogue(f32x16 (&acc)[2], v4u (&res)[2], LAS char* lds, rsrc_t rs, char* ws, unsigned short* out, int w, int lane,
                      int g, int m, int tt, int nt, int xb, bool active, bool last, unsigned flagval) {
  LAS char* sx = lds + (W8 ? 3 * SLOT : L_EX) + lane * 16;
  float v[16];
#pragma unroll
  for (int q = 0; q < 8; ++q) { const unsigned r = res[q >> 2][q & 3]; v[2 * q] = acc[0][2 * q] + blo(r); v[2 * q + 1] = acc[0][2 * q + 1] + bhi(r); }
#pragma unroll
  for (int q = 0; q < 4; ++q) *(LAS f32x4*)(sx + w * 4096 + q * 1024) = f32x4{acc[1][4 * q], acc[1][4 * q + 1], acc[1][4 * q + 2], acc[1][4 * q + 3]};
  if (W8 && w >= 4) {
#pragma unroll
    for (int q = 0; q < 4; ++q) *(LAS f32x4*)(sx + (w + 4) * 4096 + q * 1024) = f32x4{acc[0][4 * q], acc[0][4 * q + 1], acc[0][4 * q + 2], acc[0][4 * q + 3]};
  }
  BAR();
  if (W8 && w >= 4) return;
#pragma unroll
  for (int q = 0; q < 4; ++q) {
    f32x4 p0 = *(LAS f32x4*)(sx + (w ^ 2) * 4096 + q * 1024);
    if (W8) p0 += *(LAS f32x4*)(sx + (w ^ 6) * 4096 + q * 1024) + *(LAS f32x4*)(sx + (w + 8) * 4096 + q * 1024);
#pragma unroll
    for (int e = 0; e < 4; ++e) v[4 * q + e] += p0[e];
  }
#pragma unroll
  for (int q = 0; q < 8; ++q) res[q >> 2][q & 3] = pk(v[2 * q], v[2 * q + 1]);
  if (!active) return;
  if (last) {
    const int s = 2 * (128 * (g & 7) + 32 * (tt & 3) + (lane & 31)) + (g >> 3);
    unsigned short* rowp = out + (size_t)s * D_ + 32 * nt + 8 * (lane >> 5);
#pragma unroll
    for (int q = 0; q < 2; ++q) {
      auto a = __builtin_amdgcn_permlane32_swap(res[q][0], res[q][2], false, false);
      auto b = __builtin_amdgcn_permlane32_swap(res[q][1], res[q][3], false, false);
      *(v4u*)(rowp + 16 * q) = v4u{(unsigned)a[0], (unsigned)b[0], (unsigned)a[1], (unsigned)b[1]};
    }
    return;
  }
  st16(res[0], rs, lane * 16, OFF_X + xb * XBUF + (tt * 28 + 2 * nt) * 1024);
  st16(res[1], rs, lane * 16 + 1024, OFF_X + xb * XBUF + (tt * 28 + 2 * nt) * 1024);
  __builtin_amdgcn_sched_barrier(0);
  float s0 = 0.f, s1 = 0.f, q0 = 0.f, q1 = 0.f;
#pragma unroll
  for (int q = 0; q < 8; ++q) { s0 += v[2 * q]; s1 += v[2 * q + 1]; q0 += v[2 * q] * v[2 * q]; q1 += v[2 * q + 1] * v[2 * q + 1]; }
  const float sm = xsum32(s0 + s1), sq = xsum32(q0 + q1);
  const float m2 = fmaxf(sq - sm * sm * (1.f / 32.f), 0.f);
  if (lane < 32) st8(v2u{__builtin_bit_cast(unsigned, sm), __builtin_bit_cast(unsigned, m2)}, rs, lane * 8, OFF_ST + xb * SBUF + (tt * 14 + nt) * 256);
  publish(ws, (g * 16 + m) * 4 + w, flagval, lane);
}

__global__ __launch_bounds__(512, 2) void k_main(const char* x, const char* wqkv, const char* wo, const char* wg_, const char* wu_,
                                                  const char* wd_, char* ws, unsigned short* out) {
  extern __shared__ __attribute__((aligned(16))) char smem[];
  LAS char* lds = (LAS char*)smem;
  const int tid = threadIdx.x; int lane = tid & 63;
  int w = __builtin_amdgcn_readfirstlane(tid >> 6);
  const int b = blockIdx.x, xcd = b & 7, idx = b >> 3;
  int g = (xcd >> 1) * 4 + (idx >> 3), m = (xcd & 1) * 8 + (idx & 7);
  const int half = m >> 3, j = (m & 7) < 7 ? (m & 7) : 6;
  const bool active = (m & 7) < 7;
  const rsrc_t rs = __builtin_amdgcn_make_buffer_rsrc((void*)ws, 0, WS_BYTES, 0x00020000);
  TS(0);
  const unsigned tag = __hip_atomic_load((GAS unsigned*)(ws + OFF_EP), __ATOMIC_RELAXED, __HIP_MEMORY_SCOPE_AGENT) + 1u;
  auto poll_setup = [&](int base, auto NC) AI {
    for (int spin = 0; spin < (1 << 20); ++spin) {
      bool ok = true;
#pragma unroll
      for (int i = 0; i < NC.v; ++i) ok &= __builtin_amdgcn_raw_buffer_load_b32(rs, lane * 128, base + i * 8192, AUX_SC1) == tag;
      if (__all(ok)) break;
      asm volatile("s_sleep 2" ::: "memory");
    }
    __builtin_amdgcn_fence(__ATOMIC_ACQUIRE, "wavefront");
  };
  v4u res[2];
  if (w < 4) {
    if (lane == 0) __hip_atomic_store((GAS unsigned*)(ws + OFF_FLAG) + ((g * 16 + m) * 4 + w) * 16, 0u, __ATOMIC_RELAXED, __HIP_MEMORY_SCOPE_AGENT);
    LAS unsigned* cnt = (LAS unsigned*)(lds + L_RS);
    if (tid == 0) *cnt = 0u;
    pack_unit(b < 21 ? b : b < 127 ? b + 56 : -1, tid, lds, rs, x, wqkv, wo, wg_, wu_, wd_);
    VMWAIT(0);
    if (lane == 0 && __hip_atomic_fetch_add(cnt, 1u, __ATOMIC_RELAXED, __HIP_MEMORY_SCOPE_WORKGROUP) == 3u)
      __hip_atomic_store((GAS unsigned*)(ws + OFF_PF) + b * 32, tag, __ATOMIC_RELAXED, __HIP_MEMORY_SCOPE_AGENT);
    TS(2);
    BAR();
    if (w == 0) poll_setup(OFF_PF, IC<2>{});
    else {
      unsigned long long pc;
      asm volatile("s_getpc_b64 %0" : "=s"(pc));
      const unsigned cv = *(const GAS unsigned*)((pc & ~127ull) + (unsigned)((w - 1) * 64 + lane) * 128u);
      asm volatile("" :: "v"(cv));
    }
    BAR();
    res[0] = ld16(rs, lane * 16, OFF_X + ((g * 4 + 2 * half + (w & 1)) * 28 + 2 * (2 * j + (w >> 1))) * 1024);
    res[1] = ld16(rs, lane * 16 + 1024, OFF_X + ((g * 4 + 2 * half + (w & 1)) * 28 + 2 * (2 * j + (w >> 1))) * 1024);
  } else {
    res[0] = res[1] = v4u{0u, 0u, 0u, 0u};
    BAR();
    if (b >= 127) __builtin_amdgcn_s_sleep(QDELAY);
    pack_unit(b < 16 ? -1 : b < 200 ? 167 + b : b - 179, tid & 255, lds + 3 * SLOT, rs, x, wqkv, wo, wg_, wu_, wd_);
    VMWAIT(0);
    BAR();
    if (tid == 256) __hip_atomic_store((GAS unsigned*)(ws + OFF_PF + 32768) + b * 32, tag, __ATOMIC_RELAXED, __HIP_MEMORY_SCOPE_AGENT);
    {
      const int hw = w - 4, l16 = lane * 16, wa = OFF_WA + m * 84 * 1024;
      dma<1, -1>(rs, OFF_CS + m * 3072, L_CS, 0, l16);
      DMA(wa, 0); DMA(wa + SLOT, 1); DMA(wa + 2 * SLOT, 2);
    }
  }
  auto run = [&](auto QRC) AI {
  constexpr bool QR = QRC.v;
  v4u wr[28];
  if constexpr (QR) {
#pragma unroll
    for (int i = 0; i < 28; ++i) wr[i] = v4u{0u, 0u, 0u, 0u};
  }
  for (int L = 0; L < 2; ++L) {
    OPQ_S(w); OPQ_S(g); OPQ_S(m); OPQ_V(lane);
    const int t = lane & 31, h = lane >> 5;
    const int mw = w & 1, kh = (w >> 1) & 1, par = w >> 2, hw = w & 3, l16 = lane * 16;
    const int wa = OFF_WA + m * 84 * 1024, wb = OFF_WB + j * 64 * 1024, wc = OFF_WC + m * 196 * 1024, wd = OFF_WD + j * 224 * 1024;
    const int ttA = g * 4 + w;
    const int ttB = g * 4 + 2 * half + mw;
    const int ntB = 2 * j + kh;
    const int myflag = (g * 16 + m) * 4 + w;
    LAS char* lb[3] = {lds + lane * 16, lds + 2 * SLOT + lane * 16, lds + 4 * SLOT + lane * 16};
    OPQ_V(lb[1]); OPQ_V(lb[2]);
#define LSL(c) (lb[((c) % 5) >> 1] + (((c) % 5) & 1) * SLOT)
    if constexpr (QR) {
      {
        const bool hasprev = (g & 7) > 0;
        const int gp = hasprev ? g - 1 : g, ttp = gp * 4 + 3;
        VMWAIT(0);
        BAR();
        if (L) poll(ws, fi_x(gp, 3, lane), 4);
        const int t16 = lane & 15, gq = lane >> 4, tk = 24 + (t16 & 7), isv = hw >> 1, nh = hw & 1;
        v4u xf[14]; v2u st[4];
#pragma unroll
        for (int i = 0; i < 4; ++i) st[i] = ld8(rs, ((gq + 4 * i < 14 ? gq + 4 * i : 0) * 32 + tk) * 8, OFF_ST + 2 * L * SBUF + ttp * 14 * 256);
#pragma unroll
        for (int i = 0; i < 14; ++i) xf[i] = ld16(rs, (gq >> 1) * 1024 + ((gq & 1) * 32 + tk) * 16, OFF_X + 2 * L * XBUF + (ttp * 28 + 2 * i) * 1024);
        if (L) { DMA(wb, 3); DMA(wb + SLOT, 4); }
        float sm = 0.f;
#pragma unroll
        for (int i = 0; i < 4; ++i) sm += (gq + 4 * i < 14) ? asf(st[i].x) : 0.f;
        sm += __shfl_xor(sm, 16); sm = xsum32(sm);
        const float mean = sm * (1.f / D_);
        float m2 = 0.f;
#pragma unroll
        for (int i = 0; i < 4; ++i) {
          const float d = asf(st[i].x) * (1.f / 32.f) - mean;
          m2 += (gq + 4 * i < 14) ? asf(st[i].y) + 32.f * d * d : 0.f;
        }
        m2 += __shfl_xor(m2, 16); m2 = xsum32(m2);
        const float rstd = __builtin_amdgcn_rsqf(m2 * (1.f / D_) + 1e-5f);
        LAS char* wp = lds + (1 + isv) * SLOT + (gq >> 1) * 1024 + ((gq & 1) * 32 + 16 * nh + t16) * 16;
        const v4u xa = aug_x(-mean, gq == 0), wg_ = aug_w(*(LAS v2u*)(lds + L_CS + (1 + isv) * 256 + (16 * nh + t16) * 8));
        f32x4 a = {0.f, 0.f, 0.f, 0.f};
        if (isv) {
#pragma unroll
          for (int i = 0; i < 14; ++i) a = mfma16(xf[i], lds16(wp + i * 2048), a);
          a = mfma16(xa, wg_, a);
          float r[4];
#pragma unroll
          for (int i = 0; i < 4; ++i) r[i] = __shfl(rstd, 4 * gq + i);
          *(LAS v2u*)(lds + L_EX + 2048 + (16 * nh + t16 + 32 * (gq & 1)) * 16 + 8 * (gq >> 1)) = v2u{pk(a[0] * r[0], a[1] * r[1]), pk(a[2] * r[2], a[3] * r[3])};
        } else {
#pragma unroll
          for (int i = 0; i < 14; ++i) a = mfma16(lds16(wp + i * 2048), xf[i], a);
          a = mfma16(wg_, xa, a);
          *(LAS v2u*)(lds + L_EX + nh * 1024 + (16 + t16 + 32 * (gq & 1)) * 16 + 8 * (gq >> 1)) = v2u{pk(a[0] * rstd, a[1] * rstd), pk(a[2] * rstd, a[3] * rstd)};
        }
      }
      if (L == 0) {
        poll_setup(OFF_PF + 32768 + hw * 8192, IC<1>{});
        BAR();
        if (b == 0 && tid == 256) __hip_atomic_store((GAS unsigned*)(ws + OFF_EP), tag + 1u, __ATOMIC_RELAXED, __HIP_MEMORY_SCOPE_AGENT);
        dma<1, -1>(rs, OFF_CS + m * 3072, L_CS, hw < 3 ? hw : 2, l16);
        DMA(wb, 3); DMA(wb + SLOT, 4);
      }
      BAR();
      DMA(wc, 5); DMA(wc + SLOT, 6);
      VMWAIT(14);
    } else {
      constexpr int W0 = 8;
      v4u xf[28]; v2u st[7];
      const int xoff = OFF_X + 2 * L * XBUF + ttA * 28 * 1024, soff = OFF_ST + 2 * L * SBUF + ttA * 14 * 256;
      if (L) poll(ws, fi_x(g, w, lane), 4);
      TS(3 + 32 * L);
#pragma unroll
      for (int i = 0; i < W0; ++i) { xf[i] = ldf(rs, lane, xoff, i); __builtin_amdgcn_sched_barrier(0); }
      BAR();
      TS(1 + 32 * L);
      f32x16 aq = {}, ak = {}, av = {};
      float mean, rstd; v4u xa; v2u cw[3]; f32x4 rv[4];
      float bias[20];
      const bool hasprev = w > 0 || (g & 7) > 0;
      const float NEG = -__builtin_inff();
      pipe<84, 6>([&](auto I) AI { constexpr int i = I.v; return i < 84 ? lds16(LSL((i + 1) % 3) + (i / 3) * 1024) : v4u{}; },
                  [&]() AI {},
                  [&](auto I, v4u wf) AI { constexpr int i = I.v;
                    if constexpr (i % 3 == 0) mfma32p(wf, xf[i / 3], ak);
                    else if constexpr (i % 3 == 1) mfma32p(xf[i / 3], wf, av);
                    else mfma32p(wf, xf[i / 3], aq);
                    if constexpr (i % 3 == 0 && i / 3 + W0 < 28) xf[i / 3 + W0] = ldf(rs, lane, xoff, i / 3 + W0);
                    if constexpr (i % 3 == 1 && i / 3 < 7) st[i / 3] = ld8(rs, ((7 * h + i / 3) * 32 + t) * 8, soff);
                    if constexpr (i == 70) { if (L == 0) BAR(); }
                    if constexpr (i >= 2 && i < 62 && i % 3 == 2) {
                      constexpr int r = i / 3;
                      if constexpr (r < 16) bias[r] = ((unsigned)(t - 4 * h - ((r & 3) + 8 * (r >> 2))) <= 7u) ? 0.f : NEG;
                      else bias[r] = (hasprev && r - 17 + 4 * h >= t) ? 0.f : NEG;
                      OPQ_V(bias[r]);
                    }
                    if constexpr (i == 62) { fin_stats(st, mean, rstd); OPQ_V(mean); OPQ_V(rstd); }
                    if constexpr (i == 66) { xa = aug_x(-mean, h == 0); OPQ_V(xa); *(LAS float*)(lds + L_RS + w * 128 + t * 4) = rstd; }
                    if constexpr (i >= 66 && i < 69) cw[i - 66] = *(LAS v2u*)(lds + L_CS + (i - 66) * 256 + t * 8);
                    if constexpr (i == 72) mfma32p(aug_w(cw[1]), xa, ak);
                    if constexpr (i == 73) mfma32p(xa, aug_w(cw[2]), av);
                    if constexpr (i == 74) mfma32p(aug_w(cw[0]), xa, aq);
                    if constexpr (i >= 78 && i < 82) rv[i - 78] = *(LAS f32x4*)(lds + L_RS + w * 128 + (8 * (i - 78) + 4 * h) * 4);
                  });
      TS(4 + 32 * L);
      const v4u kf0 = pack8(ak, 0, rstd), kf1 = pack8(ak, 8, rstd);
      v4u vf[2];
#pragma unroll
      for (int q = 0; q < 4; ++q) {
        vf[q >> 1][(q & 1) * 2] = pk(av[4 * q] * rv[q][0], av[4 * q + 1] * rv[q][1]);
        vf[q >> 1][(q & 1) * 2 + 1] = pk(av[4 * q + 2] * rv[q][2], av[4 * q + 3] * rv[q][3]);
      }
      if (w < 3) {
        LAS char* e = lds + L_EX + (w + 1) * 3072 + lane * 16;
        *(LAS v4u*)e = kf0; *(LAS v4u*)(e + 1024) = kf1; *(LAS v4u*)(e + 2048) = vf[1];
      }
      const v4u qf0 = pack8(aq, 0, 1.f), qf1 = pack8(aq, 8, 1.f);
      f32x16 sc;
#pragma unroll
      for (int r = 0; r < 16; ++r) sc[r] = bias[r];
      mfma32p(kf0, qf0, sc); mfma32p(kf1, qf1, sc);
      TS(5 + 32 * L);
      BAR();
      TS(6 + 32 * L);
      LAS char* e = lds + L_EX + w * 3072 + lane * 16;
      f32x16 sp = {};
#pragma unroll
      for (int r = 0; r < 4; ++r) sp[12 + r] = bias[16 + r];
      mfma32p(lds16(e), qf0, sp); mfma32p(lds16(e + 1024), qf1, sp);
      const v4u vp = lds16(e + 2048);
      float p[16], pp[4], mx = NEG;
#pragma unroll
      for (int r = 0; r < 16; ++r) { p[r] = sc[r]; mx = fmaxf(mx, p[r]); }
#pragma unroll
      for (int r = 0; r < 4; ++r) { pp[r] = sp[12 + r]; mx = fmaxf(mx, pp[r]); }
      const float cf = rstd * (0.18898223650461362f * 1.4426950408889634f);
      const float mc = -xmax32(mx) * cf;
#pragma unroll
      for (int r = 0; r < 16; ++r) p[r] = __builtin_amdgcn_exp2f(__builtin_fmaf(p[r], cf, mc));
#pragma unroll
      for (int r = 0; r < 4; ++r) pp[r] = __builtin_amdgcn_exp2f(__builtin_fmaf(pp[r], cf, mc));
      const v4u pf0 = {pk(p[0], p[1]), pk(p[2], p[3]), pk(p[4], p[5]), pk(p[6], p[7])};
      const v4u pf1 = {pk(p[8], p[9]), pk(p[10], p[11]), pk(p[12], p[13]), pk(p[14], p[15])};
      const v4u pfp = {0u, 0u, pk(pp[0], pp[1]), pk(pp[2], pp[3])};
      f32x16 o = {};
      mfma32p(vf[0], pf0, o); mfma32p(vf[1], pf1, o); mfma32p(vp, pfp, o);
      float s0 = pp[0] + pp[1], s1 = pp[2] + pp[3];
#pragma unroll
      for (int r = 0; r < 8; ++r) { s0 += p[2 * r]; s1 += p[2 * r + 1]; }
      const float inv = __builtin_amdgcn_rcpf(xsum32(s0 + s1));
      st16(pack8(o, 0, inv), rs, lane * 16, OFF_O + L * OBUF + (ttA * 32 + 2 * m) * 1024);
      st16(pack8(o, 8, inv), rs, lane * 16 + 1024, OFF_O + L * OBUF + (ttA * 32 + 2 * m) * 1024);
      TS(7 + 32 * L);
      publish(ws, myflag, 4 * L + 1, lane);
      TS(8 + 32 * L);
    }
    if constexpr (QR) {
      if (active) BAR();
      DMAT(wc + 2 * SLOT, 7);
      if (active) BAR();
      VMWAIT(0);
    } else if (active) {
      constexpr int W0 = 8;
      v4u of[16];
      const int ooff = OFF_O + L * OBUF + (ttB * 32 + 16 * kh) * 1024;
      BAR();
      TS(11 + 32 * L);
      v4u wl[4];
#pragma unroll
      for (int i = 0; i < 4; ++i) wl[i] = __builtin_amdgcn_raw_buffer_load_b128(rs, lane * 16 + (i & 2) * 1024, OFF_WB + (j * 64 + 56 + kh * 4 + ((i & 1) ^ kh ^ 1)) * 1024, AUX_SC1);
      f32x16 acc[2] = {{}, {}};
      LAS char* wp0 = lds + slot_of(3) + kh * 15 * 1024 + lane * 16; LAS char* wp1 = lds + slot_of(3) + 1024 + kh * 13 * 1024 + lane * 16;
      OPQ_V(wp0); OPQ_V(wp1);
      pipe<32, 6>([&](auto I) AI { constexpr int i = I.v;
                    if constexpr (i < 28) return lds16(((i & 1) ? wp0 : wp1) + (i / 14) * SLOT + ((i % 14) >> 1) * 2048);
                    else return wl[i & 3]; },
                  [&]() AI {
                    poll(ws, fi_k(g, kh, ttB & 3, lane), 4 * L + 1);
                    TS(9 + 32 * L);
#pragma unroll
                    for (int i = 0; i < W0; ++i) { of[i] = ldf(rs, lane, ooff, i); __builtin_amdgcn_sched_barrier(0); }
                  },
                  [&](auto I, v4u wf) AI { constexpr int i = I.v;
                    mfma32p(wf, of[i >> 1], acc[(i & 1) ^ 1]);
                    if constexpr (i + W0 < 16) of[i + W0] = ldf(rs, lane, ooff, i + W0);
                  });
      TS(12 + 32 * L);
      res_epilogue<false>(acc, res, lds, rs, ws, out, w, lane, g, m, ttB, ntB, 2 * L + 1, active, false, 4 * L + 2);
      TS(13 + 32 * L);
    }
    auto phase_d = [&](auto PARC) AI {
      constexpr int PAR = PARC.v, W0 = 14;
      const int hoff = OFF_H + L * HBUF + (ttB * 112 + 56 * kh + 7 * PAR) * 1024;
      v4u hf[W0];
      f32x16 acc[2] = {{}, {}};
      LAS char* db[2];
      pipe<56, 6>(
          [&](auto I) AI { constexpr int i = I.v, r = i / 14;
            if constexpr (i % 14 == 0) {
              if constexpr (r < 2) BAR();
              if constexpr (r == 0) TS(27 + 32 * L);
              if constexpr (PAR && r == 0) DMA(wd + 4 * SLOT, 16);
              if constexpr (PAR && r == 1) DMA(wd + 6 * SLOT, 12);
              constexpr int sl = slot_of(r == 3 ? 12 : 12 + PAR + 2 * r);
              db[1] = lds + sl + kh * 15 * 1024 + l16; db[0] = lds + sl + 1024 + kh * 13 * 1024 + l16;
            }
            if constexpr (i == (PAR ? DQ : 42)) { if constexpr (PAR) VMWAIT(3); TS(28 + 32 * L); BAR(); TS(29 + 32 * L); }
            if constexpr (PAR && r >= 2) return wr[i - 28]; else return lds16(db[i & 1] + ((i % 14) >> 1) * 2048);
          },
          [&]() AI {
            poll(ws, (g * 16 + 8 * kh + PAR + 2 * (lane & 3)) * 4 + (ttB & 3), 4 * L + 3);
            TS(25 + 32 * L);
#pragma unroll
            for (int i = 0; i < W0; ++i) { hf[i] = ldf(rs, lane, hoff + (i / 7) * 14336, i % 7); __builtin_amdgcn_sched_barrier(0); }
          },
          [&](auto I, v4u wf) AI { constexpr int i = I.v;
            constexpr int k = i >> 1;
            mfma32p(wf, hf[k % W0], acc[(i & 1) ^ 1]);
            if constexpr ((i & 1) && k + W0 < 28) hf[k % W0] = ldf(rs, lane, hoff + ((k + W0) / 7) * 14336, (k + W0) % 7);
          });
      TS(30 + 32 * L);
      if constexpr (!PAR) { if (L == 0) { DMA(wa, 0); DMA(wa + SLOT, 1); } }
      res_epilogue<true>(acc, res, lds, rs, ws, out, w, lane, g, m, ttB, ntB, 2, active, L == 1, 4 * L + 4);
      TS(31 + 32 * L);
    };
    if constexpr (QR) {
      BAR();
      DMAT(wc + 3 * SLOT, 8); DMAT(wc + 4 * SLOT, 9);
      if (L == 0) {
#pragma unroll
        for (int i = 0; i < 14; ++i) { wr[14 + i] = __builtin_amdgcn_raw_buffer_load_b128(rs, l16, wd + 7 * SLOT + (kh * 14 + (i & ~1) + ((i & 1) ^ kh ^ 1)) * 1024, AUX_SC1); __builtin_amdgcn_sched_barrier(0); }
        VMWAIT(14);
      } else VMWAIT(0);
      BAR();
      DMA(wc + 5 * SLOT, 10); DMA(wc + 6 * SLOT, 11); DMA(wd, 12);
      if (L == 0) {
#pragma unroll
        for (int i = 0; i < 14; ++i) { wr[i] = __builtin_amdgcn_raw_buffer_load_b128(rs, l16, wd + 5 * SLOT + (kh * 14 + (i & ~1) + ((i & 1) ^ kh ^ 1)) * 1024, AUX_SC1); __builtin_amdgcn_sched_barrier(0); }
        VMWAIT(21);
      } else VMWAIT(7);
      BAR();
      DMA(wd + SLOT, 13); DMA(wd + 2 * SLOT, 14);
      BAR();
      DMA(wd + 3 * SLOT, 15);
      VMWAIT(7);
      __builtin_amdgcn_s_setprio(QPRIO);
      if (active) phase_d(IC<1>{}); else { VMWAIT(0); BAR(); }
      __builtin_amdgcn_s_setprio(0);
      if (L == 0) DMAT(wa + 2 * SLOT, 2);
    } else {
      constexpr int W0 = 8;
      v4u xf[28]; v2u st[7];
      const int xoff = OFF_X + (2 * L + 1) * XBUF + ttA * 28 * 1024, soff = OFF_ST + (2 * L + 1) * SBUF + ttA * 14 * 256;
      BAR();
      TS(16 + 32 * L);
      const int hoff = OFF_H + L * HBUF + (ttA * 112 + 7 * m) * 1024;
      LAS char* cs = lds + L_CS + 768 + t * 8;
      f32x16 a[4] = {{}, {}, {}, {}};
      float mean, rstd, c1, kk; v4u xa; float hv[8], ev = 0.f, tv = 0.f; v2u cw[7];
      auto epi = [&](const f32x16& acc, int e, int tile) AI {
        const int q = e >> 1;
        if (e & 1) {
          if (q > 0) { hv[q - 1] *= ev; OPQ_V(hv[q - 1]); }
          if (q < 8) { ev = __builtin_amdgcn_exp2f(tv); hv[q] = acc[q] * acc[8 + q]; OPQ_V(ev); OPQ_V(hv[q]); }
        } else if (e < 18) {
          if (q > 0) ev = __builtin_fmaf(ev, kk, kk);
          if (q < 8) { tv = acc[q] * c1; OPQ_V(tv); }
          if (q > 0) { ev = __builtin_amdgcn_rcpf(ev); OPQ_V(ev); }
        } else st16(v4u{pk(hv[0], hv[1]), pk(hv[2], hv[3]), pk(hv[4], hv[5]), pk(hv[6], hv[7])}, rs, lane * 16 + (tile & 3) * 1024, hoff + (tile >> 2) * 4096);
      };
      pipe<196, 5>(
          [&](auto I) AI { constexpr int i = I.v;
            if constexpr (i == 84 || i == 140) { TS(17 + 2 * ((i - 84) / 56) + 32 * L); BAR(); TS(18 + 2 * ((i - 84) / 56) + 32 * L); }
            if constexpr (i == 168) BAR();
            constexpr int tile = i < 84 ? i % 3 : i / 28;
            constexpr int ks = i < 84 ? i / 3 : i % 28;
            return i < 196 ? lds16(LSL(5 + tile) + ks * 1024) : v4u{};
          },
          [&]() AI {
            poll(ws, fi_x(g, w, lane), 4 * L + 2);
            TS(14 + 32 * L);
#pragma unroll
            for (int i = 0; i < W0; ++i) { xf[i] = ldf(rs, lane, xoff, i); __builtin_amdgcn_sched_barrier(0); }
          },
          [&](auto I, v4u wf) AI { constexpr int i = I.v;
            constexpr int tile = i < 84 ? i % 3 : i / 28;
            constexpr int ks = i < 84 ? i / 3 : i % 28;
            if constexpr (i >= 84 && ks == 0) { a[tile & 3] = f32x16{}; mfma32p(aug_w(cw[tile]), xa, a[tile & 3]); }
            mfma32p(wf, xf[ks], a[tile & 3]);
            if constexpr (i < 84 && i % 3 == 0 && i / 3 + W0 < 28) xf[i / 3 + W0] = ldf(rs, lane, xoff, i / 3 + W0);
            if constexpr (i % 3 == 1 && i / 3 < 7) st[i / 3] = ld8(rs, ((7 * h + i / 3) * 32 + t) * 8, soff);
            if constexpr (i >= 70 && i < 73) cw[i - 70] = *(LAS v2u*)(cs + (i - 70) * 256);
            if constexpr (i >= 78 && i % 28 == 22 && i < 170) cw[i / 28 + 1] = *(LAS v2u*)(cs + (i / 28 + 1) * 256);
            if constexpr (i == 62) { fin_stats(st, mean, rstd); OPQ_V(mean); OPQ_V(rstd); }
            if constexpr (i == 66) { xa = aug_x(-mean, h == 0); c1 = rstd * -1.4426950408889634f; kk = __builtin_amdgcn_rcpf(rstd * rstd); OPQ_V(xa); OPQ_V(c1); OPQ_V(kk); }
            if constexpr (i >= 75 && i < 78) mfma32p(aug_w(cw[i - 75]), xa, a[i - 75]);
            if constexpr (i >= 83) epi(a[((i - 83) / 19) & 3], (i - 83) % 19, (i - 83) / 19);
          });
      TS(21 + 32 * L);
      epi(a[1], 18, 5);
      st16(swiglu(a[2], rstd), rs, lane * 16 + 2048, hoff + 4096);
      TS(23 + 32 * L);
      publish(ws, myflag, 4 * L + 3, lane);
      TS(24 + 32 * L);
      if (active) phase_d(IC<0>{}); else { BAR(); if (L == 0) { DMA(wa, 0); DMA(wa + SLOT, 1); } }
    }
  }
  };
  if (w >= 4) run(IC<1>{}); else run(IC<0>{});
}

extern "C" void kernel_launch(void* const* d_in, const int* in_sizes, int n_in, void* d_out, int out_size, void* d_ws,
                              size_t ws_size, hipStream_t stream) {
  (void)in_sizes; (void)n_in; (void)out_size; (void)ws_size;
  (void)hipFuncSetAttribute((const void*)k_main, hipFuncAttributeMaxDynamicSharedMemorySize, LDS_BYTES);
  k_main<<<256, 512, LDS_BYTES, stream>>>((const char*)d_in[0], (const char*)d_in[1], (const char*)d_in[2], (const char*)d_in[4],
                                          (const char*)d_in[5], (const char*)d_in[6], (char*)d_ws, (unsigned short*)d_out);
}
```

```cpp
#include <hip/hip_runtime.h>
#include <hip/hip_bf16.h>

typedef unsigned v4u __attribute__((ext_vector_type(4)));
typedef unsigned v2u __attribute__((ext_vector_type(2)));
typedef float f32x4 __attribute__((ext_vector_type(4)));
typedef float f32x2 __attribute__((ext_vector_type(2)));
typedef float f32x16 __attribute__((ext_vector_type(16)));
typedef __bf16 bf16x8 __attribute__((ext_vector_type(8)));
typedef __bf16 bf16x2 __attribute__((ext_vector_type(2)));
#define LAS __attribute__((address_space(3)))
#define GAS __attribute__((address_space(1)))
#define DEV __device__ __forceinline__
#define AI __attribute__((always_inline))

constexpr int S_ = 2048, D_ = 448, F_ = 1792;
constexpr int OFF_FLAG = 0;
constexpr int OFF_PF = 65536, OFF_EP = OFF_PF + 65536;
constexpr int OFF_WA = 135168;
constexpr int OFF_WB = OFF_WA + 16 * 84 * 1024;
constexpr int OFF_WC = OFF_WB + 7 * 64 * 1024;
constexpr int OFF_WD = OFF_WC + 16 * 196 * 1024;
constexpr int OFF_CS = OFF_WD + 7 * 224 * 1024;
constexpr int XBUF = 64 * 28 * 1024, SBUF = 64 * 14 * 256, OBUF = 64 * 32 * 1024, HBUF = 64 * 112 * 1024;
constexpr int OFF_X = OFF_CS + 16 * 3072;
constexpr int OFF_ST = OFF_X + 4 * XBUF;
constexpr int OFF_O = OFF_ST + 4 * SBUF;
constexpr int OFF_H = OFF_O + 2 * OBUF;
constexpr int WS_BYTES = OFF_H + 2 * HBUF;
constexpr int SLOT = 28 * 1024;
constexpr int L_EX = 5 * SLOT, L_CS = L_EX + 16384, L_RS = L_CS + 3072, LDS_BYTES = L_RS + 512;
static_assert(LDS_BYTES <= 163840, "lds");
constexpr int AUX_SC1 = 16;

DEV unsigned pk(float a, float b) { bf16x2 t = {(__bf16)a, (__bf16)b}; return __builtin_bit_cast(unsigned, t); }
DEV float asf(unsigned u) { return __builtin_bit_cast(float, u); }
DEV float blo(unsigned u) { return __builtin_bit_cast(float, u << 16); }
DEV float bhi(unsigned u) { return __builtin_bit_cast(float, u & 0xffff0000u); }
DEV f32x16 mfma32(v4u a, v4u b, f32x16 c) {
  return __builtin_amdgcn_mfma_f32_32x32x16_bf16(__builtin_bit_cast(bf16x8, a), __builtin_bit_cast(bf16x8, b), c, 0, 0, 0);
}
DEV void mfma32p(v4u a, v4u b, f32x16& c) { c = mfma32(a, b, c); asm volatile("" : "+v"(c)); __builtin_amdgcn_sched_barrier(0); }
DEV f32x4 mfma16(v4u a, v4u b, f32x4 c) {
  return __builtin_amdgcn_mfma_f32_16x16x32_bf16(__builtin_bit_cast(bf16x8, a), __builtin_bit_cast(bf16x8, b), c, 0, 0, 0);
}
DEV float xsum32(float v) {
  auto r = __builtin_amdgcn_permlane32_swap(__builtin_bit_cast(unsigned, v), __builtin_bit_cast(unsigned, v), false, false);
  return __builtin_bit_cast(float, (unsigned)r[0]) + __builtin_bit_cast(float, (unsigned)r[1]);
}
DEV float xmax32(float v) {
  auto r = __builtin_amdgcn_permlane32_swap(__builtin_bit_cast(unsigned, v), __builtin_bit_cast(unsigned, v), false, false);
  return fmaxf(__builtin_bit_cast(float, (unsigned)r[0]), __builtin_bit_cast(float, (unsigned)r[1]));
}
DEV void split3(float v, unsigned& hi, unsigned& mid, unsigned& lo) {
  hi = __builtin_bit_cast(unsigned, v) & 0xffff0000u;
  float r = v - __builtin_bit_cast(float, hi);
  mid = __builtin_bit_cast(unsigned, r) & 0xffff0000u;
  float r2 = r - __builtin_bit_cast(float, mid);
  lo = __builtin_bit_cast(unsigned, r2) & 0xffff0000u;
}
DEV v4u aug_x(float negmean, bool on) {
  unsigned hi, mid, lo; split3(negmean, hi, mid, lo);
  v4u r = {hi | (hi >> 16), mid | (mid >> 16), lo | (hi >> 16), 0u};
  if (!on) r = v4u{0u, 0u, 0u, 0u};
  return r;
}
DEV v4u aug_w(v2u c) { return v4u{c.x, c.x, c.y, 0u}; }

typedef short s16x4 __attribute__((ext_vector_type(4)));
typedef __amdgpu_buffer_rsrc_t rsrc_t;
#define BAR() asm volatile("s_waitcnt lgkmcnt(0)\n\ts_barrier" ::: "memory")
#define VMWAIT(n) asm volatile("s_waitcnt vmcnt(" #n ")" ::: "memory")
DEV v4u ld16(rsrc_t rs, int voff, int soff) { return __builtin_amdgcn_raw_buffer_load_b128(rs, voff, soff, AUX_SC1); }
DEV v2u ld8(rsrc_t rs, int voff, int soff) { return __builtin_amdgcn_raw_buffer_load_b64(rs, voff, soff, AUX_SC1); }
DEV void st16(v4u v, rsrc_t rs, int voff, int soff) { __builtin_amdgcn_raw_buffer_store_b128(v, rs, voff + soff, 0, AUX_SC1); }
DEV void st8(v2u v, rsrc_t rs, int voff, int soff) { __builtin_amdgcn_raw_buffer_store_b64(v, rs, voff, soff, AUX_SC1); }
DEV v4u ldf(rsrc_t rs, int lane, int base, int i) { return ld16(rs, lane * 16 + (i & 3) * 1024, base + (i >> 2) * 4096); }
DEV v4u lds16(LAS char* p) { return *(LAS v4u*)p; }

DEV v2u tr8(LAS char* p) { return __builtin_bit_cast(v2u, __builtin_amdgcn_ds_read_tr16_b64_v4i16((LAS s16x4*)p)); }
DEV void pack_unit(int u, int tid, LAS char* sm, rsrc_t rs, const char* x, const char* wqkv, const char* wo, const char* wg,
                   const char* wu, const char* wd) {
  const int w = __builtin_amdgcn_readfirstlane(tid >> 6), lane = tid & 63, n = lane & 31, h = lane >> 5;
  if (u < 0) { BAR(); return; }
  if (u < 77) {
    const char* src; int ld;
    if (u < 21) { src = wqkv + u * 128; ld = 2688; } else if (u < 49) { src = wg + (u - 21) * 128; ld = 3584; } else { src = wu + (u - 49) * 128; ld = 3584; }
    src += (tid >> 3) * ld + (tid & 7) * 16;
    v4u v[14];
#pragma unroll
    for (int i = 0; i < 14; ++i) v[i] = *(const v4u*)(src + i * 32 * ld);
    float c[8] = {0.f, 0.f, 0.f, 0.f, 0.f, 0.f, 0.f, 0.f};
#pragma unroll
    for (int i = 0; i < 14; ++i)
#pragma unroll
      for (int q = 0; q < 4; ++q) { const unsigned e = v[i][q]; c[2 * q] += blo(e); c[2 * q + 1] += bhi(e); }
    *(LAS f32x4*)(sm + (tid >> 3) * 272 + (tid & 7) * 32) = f32x4{c[0], c[1], c[2], c[3]};
    *(LAS f32x4*)(sm + (tid >> 3) * 272 + (tid & 7) * 32 + 16) = f32x4{c[4], c[5], c[6], c[7]};
    BAR();
    if (tid < 64) {
      float cs = 0.f;
#pragma unroll
      for (int i = 0; i < 32; ++i) cs += *(LAS float*)(sm + i * 272 + tid * 4);
      unsigned hi, mid, lo; split3(cs, hi, mid, lo);
      int o;
      if (u < 21) { const int c_ = u * 64 + tid, t = c_ / 448, r = c_ % 448; o = (r / 28) * 3072 + t * 256 + (r % 28) * 8;
        if (r % 28 < 4) st8(v2u{0u, 0u}, rs, OFF_CS + o + 28 * 8, 0); }
      else { const int f = ((u - 21) % 28) * 64 + tid; o = (f / 112) * 3072 + (3 + (f % 112) / 16) * 256 + ((f & 15) + (u >= 49 ? 16 : 0)) * 8; }
      st8(v2u{(hi >> 16) | mid, (lo >> 16) | hi}, rs, OFF_CS + o, 0);
    }
    return;
  }
  const bool isx = u >= 119 && u < 183;
  const int lstride = isx ? 912 : 960;
  int dst0 = 0, dstep = 0, cstep = 128, col0 = 64 * (w >> 1), row0 = 16 * (w & 1);
  {
    const char* src; int ld = 896;
    if (u < 119) { const int q = u - 77; ld = 2688; src = wqkv + ((q / 3) * 32 + 8 * w) * 2688 + (q % 3) * 896;
      dst0 = OFF_WA + (((w >> 1) * 3 + q % 3) * 28 + 2 * (q / 3) + (w & 1)) * 1024; dstep = 168 * 1024; cstep = 112; col0 = 56 * (w >> 1); }
    else if (u < 183) { const int tt = u - 119, g = tt >> 2; ld = 1792; src = x + (2 * (128 * (g & 7) + 32 * (tt & 3) + 8 * w) + (g >> 3)) * 896; }
    else if (u < 199) { const int s = 2 * (u - 183) + (w & 1), kh = s >> 4, ks = s & 15, c = ks / 7; src = wo + ((u - 183) * 28 + 8 * w) * 896;
      dst0 = OFF_WB + (c * 28 + kh * (c < 2 ? 14 : 4) + (ks % 7) * 2 + (w >> 1)) * 1024; dstep = 64 * 1024; }
    else if (u < 311) { const int q = u - 199; ld = 3584; src = (w < 2 ? wg : wu) + ((q >> 2) * 16 + 8 * (w & 1)) * 3584 + (q & 3) * 896;
      dst0 = OFF_WC + (((q & 3) * 28 + w) * 28 + (q >> 2)) * 1024; dstep = 112 * 1024; col0 = 32 * w; }
    else { const int s = 2 * (u - 311) + (w & 1), kh = s / 56, ks = s % 56; src = wd + ((u - 311) * 32 + 8 * w) * 896;
      dst0 = OFF_WD + ((ks / 7) * 28 + kh * 14 + (ks % 7) * 2 + (w >> 1)) * 1024; dstep = 224 * 1024; }
    const bool pad = u >= 183 && u < 199 && w == 3;
    if (lane < 56) {
      v4u v[8];
#pragma unroll
      for (int i = 0; i < 8; ++i) v[i] = *(const v4u*)(src + ((pad && i >= 4) ? 0 : i * ld) + lane * 16);
#pragma unroll
      for (int i = 0; i < 8; ++i) *(LAS v4u*)(sm + (8 * w + i) * lstride + lane * 16) = (pad && i >= 4) ? v4u{0u, 0u, 0u, 0u} : v[i];
    }
  }
  BAR();
  if (isx) {
    const int tt = u - 119;
#pragma unroll
    for (int it = 0; it < 4; ++it) {
      const int nt = w + 4 * it;
      if (nt >= 14) break;
      v2u a[4];
#pragma unroll
      for (int i = 0; i < 4; ++i) a[i] = *(LAS v2u*)(sm + n * 912 + nt * 64 + i * 16 + h * 8);
      __builtin_amdgcn_raw_buffer_store_b128(v4u{a[0].x, a[0].y, a[1].x, a[1].y}, rs, OFF_X + (tt * 28 + 2 * nt) * 1024 + lane * 16, 0, AUX_SC1);
      __builtin_amdgcn_raw_buffer_store_b128(v4u{a[2].x, a[2].y, a[3].x, a[3].y}, rs, OFF_X + (tt * 28 + 2 * nt + 1) * 1024 + lane * 16, 0, AUX_SC1);
      float sm1 = 0.f;
#pragma unroll
      for (int i = 0; i < 4; ++i) sm1 += blo(a[i].x) + bhi(a[i].x) + blo(a[i].y) + bhi(a[i].y);
      sm1 = xsum32(sm1);
      const float mu = sm1 * (1.f / 32.f);
      float m2 = 0.f;
#pragma unroll
      for (int i = 0; i < 4; ++i) {
        const float d0 = blo(a[i].x) - mu, d1 = bhi(a[i].x) - mu, d2 = blo(a[i].y) - mu, d3 = bhi(a[i].y) - mu;
        m2 += d0 * d0 + d1 * d1 + d2 * d2 + d3 * d3;
      }
      m2 = xsum32(m2);
      if (h == 0) st8(v2u{__builtin_bit_cast(unsigned, sm1), __builtin_bit_cast(unsigned, m2)}, rs, OFF_ST + (tt * 14 + nt) * 256 + n * 8, 0);
    }
    return;
  }
  const int q4 = (lane & 15) >> 2, p4 = lane & 3, cb = (lane >> 4) & 1;
  const bool gu = u >= 199 && u < 311;
  LAS char* pa = sm + ((gu ? 16 * cb : row0) + 4 * h + q4) * 960 + col0 + (gu ? 0 : 32 * cb) + 8 * p4;
  v2u lo[8], hi[8];
#pragma unroll
  for (int i = 0; i < 8; ++i) { LAS char* q = pa + ((i < 7 || u < 119) ? i * cstep : 0); lo[i] = tr8(q); hi[i] = tr8(q + 8 * 960); }
  const bool z = u < 119 && n >= 28;
#pragma unroll
  for (int i = 0; i < 8; ++i)
    if (i < 7 || u < 119) st16(z ? v4u{0u, 0u, 0u, 0u} : v4u{lo[i].x, lo[i].y, hi[i].x, hi[i].y}, rs, lane * 16, dst0 + i * dstep);
}

DEV void poll(char* ws, int fi, unsigned want) {
  GAS unsigned* f = (GAS unsigned*)(ws + OFF_FLAG) + fi * 16;
  for (int spin = 0; spin < (1 << 20); ++spin) {
    const unsigned v = __hip_atomic_load(f, __ATOMIC_RELAXED, __HIP_MEMORY_SCOPE_AGENT);
    if (__all(v >= want)) break;
  }
  __builtin_amdgcn_fence(__ATOMIC_ACQUIRE, "wavefront");
}
DEV int fi_x(int g, int T, int lane) { const int i = lane < 14 ? lane : 0; return (g * 16 + 8 * (T >> 1) + (i >> 1)) * 4 + (T & 1) + 2 * (i & 1); }
DEV int fi_k(int g, int kh, int T, int lane) { return (g * 16 + 8 * kh + (lane & 7)) * 4 + T; }
DEV void publish(char* ws, int fi, unsigned val, int lane) {
  VMWAIT(0);
  if (lane == 0) __hip_atomic_store((GAS unsigned*)(ws + OFF_FLAG) + fi * 16, val, __ATOMIC_RELAXED, __HIP_MEMORY_SCOPE_AGENT);
}
template <int N, int TH> DEV void dma(rsrc_t rs, int src, int dst, int hw, int lane16) {
#pragma unroll
  for (int i = 0; i < N; ++i) {
    unsigned keep;
    asm volatile("s_mov_b32 %0, m0\n\ts_mov_b32 m0, %1\n\ts_nop 0\n\tbuffer_load_dwordx4 %2, %3, %4 offen sc1 lds\n\ts_mov_b32 m0, %0"
                 : "=&s"(keep) : "s"(dst + (hw * N + i) * 1024), "v"(lane16), "s"(rs), "s"(src + (hw * N + i) * 1024) : "memory");
    if constexpr (TH >= 0) asm volatile("s_waitcnt vmcnt(%0)" :: "i"(TH) : "memory");
  }
}
#define THR 4
#define DQ 42
#define QPRIO 1
#define QDELAY 75
#define DMA(src, c) dma<7, -1>(rs, src, slot_of(c), hw, l16)
#define DMAT(src, c) dma<7, THR>(rs, src, slot_of(c), hw, l16)
#define OPQ_S(x) asm volatile("" : "+s"(x))
#define OPQ_V(x) asm volatile("" : "+v"(x))
template <int I> struct IC { static constexpr int v = I; };
template <int I, int N, class F> DEV void sfor(F& f) { if constexpr (I < N) { f(IC<I>{}); sfor<I + 1, N>(f); } }
template <int N, int PF, class RD, class MID, class ST> DEV void pipe(RD rd, MID mid, ST st) {
  constexpr int R = PF + 1;
  v4u wb[R];
  auto pro = [&](auto I) __attribute__((always_inline)) { wb[I.v] = rd(I); };
  sfor<0, PF>(pro);
  mid();
  __builtin_amdgcn_sched_barrier(0);
  auto body = [&](auto I) __attribute__((always_inline)) {
    constexpr int i = I.v;
    st(I, wb[i % R]);
    if constexpr (i + PF < N) wb[(i + PF) % R] = rd(IC<i + PF>{});
    asm volatile("" :: "v"(wb[i % R]));
    __builtin_amdgcn_sched_barrier(0);
  };
  sfor<0, N>(body);
}
constexpr int slot_of(int c) { return (c % 5) * SLOT; }
DEV void fin_stats(const v2u (&st)[7], float& mean, float& rstd) {
  float sm = 0.f;
#pragma unroll
  for (int i = 0; i < 7; ++i) sm += asf(st[i].x);
  mean = xsum32(sm) * (1.f / D_);
  float m2 = 0.f;
#pragma unroll
  for (int i = 0; i < 7; ++i) {
    const float d = asf(st[i].x) * (1.f / 32.f) - mean;
    m2 += asf(st[i].y) + 32.f * d * d;
  }
  rstd = __builtin_amdgcn_rsqf(xsum32(m2) * (1.f / D_) + 1e-5f);
}
DEV v4u pack8(const f32x16& a, int o, float sc) {
  return v4u{pk(a[o] * sc, a[o + 1] * sc), pk(a[o + 2] * sc, a[o + 3] * sc), pk(a[o + 4] * sc, a[o + 5] * sc), pk(a[o + 6] * sc, a[o + 7] * sc)};
}
DEV v4u swiglu(const f32x16& a, float rstd) {
  float hv[8];
#pragma unroll
  for (int i = 0; i < 8; ++i) {
    const float g = a[i] * rstd, up = a[8 + i] * rstd;
    const float e = __builtin_amdgcn_exp2f(g * -1.4426950408889634f);
    hv[i] = g * up * __builtin_amdgcn_rcpf(1.f + e);
  }
  return v4u{pk(hv[0], hv[1]), pk(hv[2], hv[3]), pk(hv[4], hv[5]), pk(hv[6], hv[7])};
}
#define TS(i)

template <bool W8> DEV void res_epilogue(f32x16 (&acc)[2], v4u (&res)[2], LAS char* lds, rsrc_t rs, char* ws, unsigned short* out, int w, int lane,
                      int g, int m, int tt, int nt, int xb, bool active, bool last, unsigned flagval) {
  LAS char* sx = lds + (W8 ? 3 * SLOT : L_EX) + lane * 16;
  float v[16];
#pragma unroll
  for (int q = 0; q < 8; ++q) { const unsigned r = res[q >> 2][q & 3]; v[2 * q] = acc[0][2 * q] + blo(r); v[2 * q + 1] = acc[0][2 * q + 1] + bhi(r); }
#pragma unroll
  for (int q = 0; q < 4; ++q) *(LAS f32x4*)(sx + w * 4096 + q * 1024) = f32x4{acc[1][4 * q], acc[1][4 * q + 1], acc[1][4 * q + 2], acc[1][4 * q + 3]};
  if (W8 && w >= 4) {
#pragma unroll
    for (int q = 0; q < 4; ++q) *(LAS f32x4*)(sx + (w + 4) * 4096 + q * 1024) = f32x4{acc[0][4 * q], acc[0][4 * q + 1], acc[0][4 * q + 2], acc[0][4 * q + 3]};
  }
  BAR();
  if (W8 && w >= 4) return;
#pragma unroll
  for (int q = 0; q < 4; ++q) {
    f32x4 p0 = *(LAS f32x4*)(sx + (w ^ 2) * 4096 + q * 1024);
    if (W8) p0 += *(LAS f32x4*)(sx + (w ^ 6) * 4096 + q * 1024) + *(LAS f32x4*)(sx + (w + 8) * 4096 + q * 1024);
#pragma unroll
    for (int e = 0; e < 4; ++e) v[4 * q + e] += p0[e];
  }
#pragma unroll
  for (int q = 0; q < 8; ++q) res[q >> 2][q & 3] = pk(v[2 * q], v[2 * q + 1]);
  if (!active) return;
  if (last) {
    const int s = 2 * (128 * (g & 7) + 32 * (tt & 3) + (lane & 31)) + (g >> 3);
    unsigned short* rowp = out + (size_t)s * D_ + 32 * nt + 8 * (lane >> 5);
#pragma unroll
    for (int q = 0; q < 2; ++q) {
      auto a = __builtin_amdgcn_permlane32_swap(res[q][0], res[q][2], false, false);
      auto b = __builtin_amdgcn_permlane32_swap(res[q][1], res[q][3], false, false);
      *(v4u*)(rowp + 16 * q) = v4u{(unsigned)a[0], (unsigned)b[0], (unsigned)a[1], (unsigned)b[1]};
    }
    return;
  }
  st16(res[0], rs, lane * 16, OFF_X + xb * XBUF + (tt * 28 + 2 * nt) * 1024);
  st16(res[1], rs, lane * 16 + 1024, OFF_X + xb * XBUF + (tt * 28 + 2 * nt) * 1024);
  __builtin_amdgcn_sched_barrier(0);
  float s0 = 0.f, s1 = 0.f, q0 = 0.f, q1 = 0.f;
#pragma unroll
  for (int q = 0; q < 8; ++q) { s0 += v[2 * q]; s1 += v[2 * q + 1]; q0 += v[2 * q] * v[2 * q]; q1 += v[2 * q + 1] * v[2 * q + 1]; }
  const float sm = xsum32(s0 + s1), sq = xsum32(q0 + q1);
  const float m2 = fmaxf(sq - sm * sm * (1.f / 32.f), 0.f);
  if (lane < 32) st8(v2u{__builtin_bit_cast(unsigned, sm), __builtin_bit_cast(unsigned, m2)}, rs, lane * 8, OFF_ST + xb * SBUF + (tt * 14 + nt) * 256);
  publish(ws, (g * 16 + m) * 4 + w, flagval, lane);
}

__global__ __launch_bounds__(512, 2) void k_main(const char* x, const char* wqkv, const char* wo, const char* wg_, const char* wu_,
                                                  const char* wd_, char* ws, unsigned short* out) {
  extern __shared__ __attribute__((aligned(16))) char smem[];
  LAS char* lds = (LAS char*)smem;
  const int tid = threadIdx.x; int lane = tid & 63;
  int w = __builtin_amdgcn_readfirstlane(tid >> 6);
  const int b = blockIdx.x, xcd = b & 7, idx = b >> 3;
  int g = (xcd >> 1) * 4 + (idx >> 3), m = (xcd & 1) * 8 + (idx & 7);
  const int half = m >> 3, j = (m & 7) < 7 ? (m & 7) : 6;
  const bool active = (m & 7) < 7;
  const rsrc_t rs = __builtin_amdgcn_make_buffer_rsrc((void*)ws, 0, WS_BYTES, 0x00020000);
  TS(0);
  const unsigned tag = __hip_atomic_load((GAS unsigned*)(ws + OFF_EP), __ATOMIC_RELAXED, __HIP_MEMORY_SCOPE_AGENT) + 1u;
  auto poll_setup = [&](int base, auto NC) AI {
    for (int spin = 0; spin < (1 << 20); ++spin) {
      bool ok = true;
#pragma unroll
      for (int i = 0; i < NC.v; ++i) ok &= __builtin_amdgcn_raw_buffer_load_b32(rs, lane * 128, base + i * 8192, AUX_SC1) == tag;
      if (__all(ok)) break;
      asm volatile("s_sleep 2" ::: "memory");
    }
    __builtin_amdgcn_fence(__ATOMIC_ACQUIRE, "wavefront");
  };
  v4u res[2];
  if (w < 4) {
    if (lane == 0) __hip_atomic_store((GAS unsigned*)(ws + OFF_FLAG) + ((g * 16 + m) * 4 + w) * 16, 0u, __ATOMIC_RELAXED, __HIP_MEMORY_SCOPE_AGENT);
    LAS unsigned* cnt = (LAS unsigned*)(lds + L_RS);
    if (tid == 0) *cnt = 0u;
    pack_unit(b < 21 ? b : b < 127 ? b + 56 : -1, tid, lds, rs, x, wqkv, wo, wg_, wu_, wd_);
    VMWAIT(0);
    if (lane == 0 && __hip_atomic_fetch_add(cnt, 1u, __ATOMIC_RELAXED, __HIP_MEMORY_SCOPE_WORKGROUP) == 3u)
      __hip_atomic_store((GAS unsigned*)(ws + OFF_PF) + b * 32, tag, __ATOMIC_RELAXED, __HIP_MEMORY_SCOPE_AGENT);
    TS(2);
    BAR();
    if (w == 0) poll_setup(OFF_PF, IC<2>{});
    BAR();
    res[0] = ld16(rs, lane * 16, OFF_X + ((g * 4 + 2 * half + (w & 1)) * 28 + 2 * (2 * j + (w >> 1))) * 1024);
    res[1] = ld16(rs, lane * 16 + 1024, OFF_X + ((g * 4 + 2 * half + (w & 1)) * 28 + 2 * (2 * j + (w >> 1))) * 1024);
  } else {
    res[0] = res[1] = v4u{0u, 0u, 0u, 0u};
    BAR();
    if (b >= 127) __builtin_amdgcn_s_sleep(QDELAY);
    pack_unit(b < 16 ? -1 : b < 200 ? 167 + b : b - 179, tid & 255, lds + 3 * SLOT, rs, x, wqkv, wo, wg_, wu_, wd_);
    VMWAIT(0);
    BAR();
    if (tid == 256) __hip_atomic_store((GAS unsigned*)(ws + OFF_PF + 32768) + b * 32, tag, __ATOMIC_RELAXED, __HIP_MEMORY_SCOPE_AGENT);
    {
      const int hw = w - 4, l16 = lane * 16, wa = OFF_WA + m * 84 * 1024;
      dma<1, -1>(rs, OFF_CS + m * 3072, L_CS, 0, l16);
      DMA(wa, 0); DMA(wa + SLOT, 1); DMA(wa + 2 * SLOT, 2);
    }
  }
  auto run = [&](auto QRC) AI {
  constexpr bool QR = QRC.v;
  v4u wr[28];
  if constexpr (QR) {
#pragma unroll
    for (int i = 0; i < 28; ++i) wr[i] = v4u{0u, 0u, 0u, 0u};
  }
  for (int L = 0; L < 2; ++L) {
    OPQ_S(w); OPQ_S(g); OPQ_S(m); OPQ_V(lane);
    const int t = lane & 31, h = lane >> 5;
    const int mw = w & 1, kh = (w >> 1) & 1, par = w >> 2, hw = w & 3, l16 = lane * 16;
    const int wa = OFF_WA + m * 84 * 1024, wb = OFF_WB + j * 64 * 1024, wc = OFF_WC + m * 196 * 1024, wd = OFF_WD + j * 224 * 1024;
    const int ttA = g * 4 + w;
    const int ttB = g * 4 + 2 * half + mw;
    const int ntB = 2 * j + kh;
    const int myflag = (g * 16 + m) * 4 + w;
    LAS char* lb[3] = {lds + lane * 16, lds + 2 * SLOT + lane * 16, lds + 4 * SLOT + lane * 16};
    OPQ_V(lb[1]); OPQ_V(lb[2]);
#define LSL(c) (lb[((c) % 5) >> 1] + (((c) % 5) & 1) * SLOT)
    if constexpr (QR) {
      {
        const bool hasprev = (g & 7) > 0;
        const int gp = hasprev ? g - 1 : g, ttp = gp * 4 + 3;
        VMWAIT(0);
        BAR();
        if (L) poll(ws, fi_x(gp, 3, lane), 4);
        const int t16 = lane & 15, gq = lane >> 4, tk = 24 + (t16 & 7), isv = hw >> 1, nh = hw & 1;
        v4u xf[14]; v2u st[4];
#pragma unroll
        for (int i = 0; i < 4; ++i) st[i] = ld8(rs, ((gq + 4 * i < 14 ? gq + 4 * i : 0) * 32 + tk) * 8, OFF_ST + 2 * L * SBUF + ttp * 14 * 256);
#pragma unroll
        for (int i = 0; i < 14; ++i) xf[i] = ld16(rs, (gq >> 1) * 1024 + ((gq & 1) * 32 + tk) * 16, OFF_X + 2 * L * XBUF + (ttp * 28 + 2 * i) * 1024);
        if (L) { DMA(wb, 3); DMA(wb + SLOT, 4); }
        float sm = 0.f;
#pragma unroll
        for (int i = 0; i < 4; ++i) sm += (gq + 4 * i < 14) ? asf(st[i].x) : 0.f;
        sm += __shfl_xor(sm, 16); sm = xsum32(sm);
        const float mean = sm * (1.f / D_);
        float m2 = 0.f;
#pragma unroll
        for (int i = 0; i < 4; ++i) {
          const float d = asf(st[i].x) * (1.f / 32.f) - mean;
          m2 += (gq + 4 * i < 14) ? asf(st[i].y) + 32.f * d * d : 0.f;
        }
        m2 += __shfl_xor(m2, 16); m2 = xsum32(m2);
        const float rstd = __builtin_amdgcn_rsqf(m2 * (1.f / D_) + 1e-5f);
        LAS char* wp = lds + (1 + isv) * SLOT + (gq >> 1) * 1024 + ((gq & 1) * 32 + 16 * nh + t16) * 16;
        const v4u xa = aug_x(-mean, gq == 0), wg_ = aug_w(*(LAS v2u*)(lds + L_CS + (1 + isv) * 256 + (16 * nh + t16) * 8));
        f32x4 a = {0.f, 0.f, 0.f, 0.f};
        if (isv) {
#pragma unroll
          for (int i = 0; i < 14; ++i) a = mfma16(xf[i], lds16(wp + i * 2048), a);
          a = mfma16(xa, wg_, a);
          float r[4];
#pragma unroll
          for (int i = 0; i < 4; ++i) r[i] = __shfl(rstd, 4 * gq + i);
          *(LAS v2u*)(lds + L_EX + 2048 + (16 * nh + t16 + 32 * (gq & 1)) * 16 + 8 * (gq >> 1)) = v2u{pk(a[0] * r[0], a[1] * r[1]), pk(a[2] * r[2], a[3] * r[3])};
        } else {
#pragma unroll
          for (int i = 0; i < 14; ++i) a = mfma16(lds16(wp + i * 2048), xf[i], a);
          a = mfma16(wg_, xa, a);
          *(LAS v2u*)(lds + L_EX + nh * 1024 + (16 + t16 + 32 * (gq & 1)) * 16 + 8 * (gq >> 1)) = v2u{pk(a[0] * rstd, a[1] * rstd), pk(a[2] * rstd, a[3] * rstd)};
        }
      }
      if (L == 0) {
        poll_setup(OFF_PF + 32768 + hw * 8192, IC<1>{});
        BAR();
        if (b == 0 && tid == 256) __hip_atomic_store((GAS unsigned*)(ws + OFF_EP), tag + 1u, __ATOMIC_RELAXED, __HIP_MEMORY_SCOPE_AGENT);
        dma<1, -1>(rs, OFF_CS + m * 3072, L_CS, hw < 3 ? hw : 2, l16);
        DMA(wb, 3); DMA(wb + SLOT, 4);
      }
      BAR();
      DMA(wc, 5); DMA(wc + SLOT, 6);
      VMWAIT(14);
    } else {
      constexpr int W0 = 8;
      v4u xf[28]; v2u st[7];
      const int xoff = OFF_X + 2 * L * XBUF + ttA * 28 * 1024, soff = OFF_ST + 2 * L * SBUF + ttA * 14 * 256;
      if (L) poll(ws, fi_x(g, w, lane), 4);
      TS(3 + 32 * L);
#pragma unroll
      for (int i = 0; i < W0; ++i) { xf[i] = ldf(rs, lane, xoff, i); __builtin_amdgcn_sched_barrier(0); }
      BAR();
      TS(1 + 32 * L);
      f32x16 aq = {}, ak = {}, av = {};
      float mean, rstd; v4u xa; v2u cw[3]; f32x4 rv[4];
      float bias[20];
      const bool hasprev = w > 0 || (g & 7) > 0;
      const float NEG = -__builtin_inff();
      pipe<84, 6>([&](auto I) AI { constexpr int i = I.v; return i < 84 ? lds16(LSL((i + 1) % 3) + (i / 3) * 1024) : v4u{}; },
                  [&]() AI {},
                  [&](auto I, v4u wf) AI { constexpr int i = I.v;
                    if constexpr (i % 3 == 0) mfma32p(wf, xf[i / 3], ak);
                    else if constexpr (i % 3 == 1) mfma32p(xf[i / 3], wf, av);
                    else mfma32p(wf, xf[i / 3], aq);
                    if constexpr (i % 3 == 0 && i / 3 + W0 < 28) xf[i / 3 + W0] = ldf(rs, lane, xoff, i / 3 + W0);
                    if constexpr (i % 3 == 1 && i / 3 < 7) st[i / 3] = ld8(rs, ((7 * h + i / 3) * 32 + t) * 8, soff);
                    if constexpr (i == 70) { if (L == 0) BAR(); }
                    if constexpr (i >= 2 && i < 62 && i % 3 == 2) {
                      constexpr int r = i / 3;
                      if constexpr (r < 16) bias[r] = ((unsigned)(t - 4 * h - ((r & 3) + 8 * (r >> 2))) <= 7u) ? 0.f : NEG;
                      else bias[r] = (hasprev && r - 17 + 4 * h >= t) ? 0.f : NEG;
                      OPQ_V(bias[r]);
                    }
                    if constexpr (i == 62) { fin_stats(st, mean, rstd); OPQ_V(mean); OPQ_V(rstd); }
                    if constexpr (i == 66) { xa = aug_x(-mean, h == 0); OPQ_V(xa); *(LAS float*)(lds + L_RS + w * 128 + t * 4) = rstd; }
                    if constexpr (i >= 66 && i < 69) cw[i - 66] = *(LAS v2u*)(lds + L_CS + (i - 66) * 256 + t * 8);
                    if constexpr (i == 72) mfma32p(aug_w(cw[1]), xa, ak);
                    if constexpr (i == 73) mfma32p(xa, aug_w(cw[2]), av);
                    if constexpr (i == 74) mfma32p(aug_w(cw[0]), xa, aq);
                    if constexpr (i >= 78 && i < 82) rv[i - 78] = *(LAS f32x4*)(lds + L_RS + w * 128 + (8 * (i - 78) + 4 * h) * 4);
                  });
      TS(4 + 32 * L);
      const v4u kf0 = pack8(ak, 0, rstd), kf1 = pack8(ak, 8, rstd);
      v4u vf[2];
#pragma unroll
      for (int q = 0; q < 4; ++q) {
        vf[q >> 1][(q & 1) * 2] = pk(av[4 * q] * rv[q][0], av[4 * q + 1] * rv[q][1]);
        vf[q >> 1][(q & 1) * 2 + 1] = pk(av[4 * q + 2] * rv[q][2], av[4 * q + 3] * rv[q][3]);
      }
      if (w < 3) {
        LAS char* e = lds + L_EX + (w + 1) * 3072 + lane * 16;
        *(LAS v4u*)e = kf0; *(LAS v4u*)(e + 1024) = kf1; *(LAS v4u*)(e + 2048) = vf[1];
      }
      const v4u qf0 = pack8(aq, 0, 1.f), qf1 = pack8(aq, 8, 1.f);
      f32x16 sc;
#pragma unroll
      for (int r = 0; r < 16; ++r) sc[r] = bias[r];
      mfma32p(kf0, qf0, sc); mfma32p(kf1, qf1, sc);
      TS(5 + 32 * L);
      BAR();
      TS(6 + 32 * L);
      LAS char* e = lds + L_EX + w * 3072 + lane * 16;
      f32x16 sp = {};
#pragma unroll
      for (int r = 0; r < 4; ++r) sp[12 + r] = bias[16 + r];
      mfma32p(lds16(e), qf0, sp); mfma32p(lds16(e + 1024), qf1, sp);
      const v4u vp = lds16(e + 2048);
      float p[16], pp[4], mx = NEG;
#pragma unroll
      for (int r = 0; r < 16; ++r) { p[r] = sc[r]; mx = fmaxf(mx, p[r]); }
#pragma unroll
      for (int r = 0; r < 4; ++r) { pp[r] = sp[12 + r]; mx = fmaxf(mx, pp[r]); }
      const float cf = rstd * (0.18898223650461362f * 1.4426950408889634f);
      const float mc = -xmax32(mx) * cf;
#pragma unroll
      for (int r = 0; r < 16; ++r) p[r] = __builtin_amdgcn_exp2f(__builtin_fmaf(p[r], cf, mc));
#pragma unroll
      for (int r = 0; r < 4; ++r) pp[r] = __builtin_amdgcn_exp2f(__builtin_fmaf(pp[r], cf, mc));
      const v4u pf0 = {pk(p[0], p[1]), pk(p[2], p[3]), pk(p[4], p[5]), pk(p[6], p[7])};
      const v4u pf1 = {pk(p[8], p[9]), pk(p[10], p[11]), pk(p[12], p[13]), pk(p[14], p[15])};
      const v4u pfp = {0u, 0u, pk(pp[0], pp[1]), pk(pp[2], pp[3])};
      f32x16 o = {};
      mfma32p(vf[0], pf0, o); mfma32p(vf[1], pf1, o); mfma32p(vp, pfp, o);
      float s0 = pp[0] + pp[1], s1 = pp[2] + pp[3];
#pragma unroll
      for (int r = 0; r < 8; ++r) { s0 += p[2 * r]; s1 += p[2 * r + 1]; }
      const float inv = __builtin_amdgcn_rcpf(xsum32(s0 + s1));
      st16(pack8(o, 0, inv), rs, lane * 16, OFF_O + L * OBUF + (ttA * 32 + 2 * m) * 1024);
      st16(pack8(o, 8, inv), rs, lane * 16 + 1024, OFF_O + L * OBUF + (ttA * 32 + 2 * m) * 1024);
      TS(7 + 32 * L);
      publish(ws, myflag, 4 * L + 1, lane);
      TS(8 + 32 * L);
    }
    if constexpr (QR) {
      if (active) BAR();
      DMAT(wc + 2 * SLOT, 7);
      if (active) BAR();
      VMWAIT(0);
    } else if (active) {
      constexpr int W0 = 8;
      v4u of[16];
      const int ooff = OFF_O + L * OBUF + (ttB * 32 + 16 * kh) * 1024;
      BAR();
      TS(11 + 32 * L);
      v4u wl[4];
#pragma unroll
      for (int i = 0; i < 4; ++i) wl[i] = __builtin_amdgcn_raw_buffer_load_b128(rs, lane * 16 + (i & 2) * 1024, OFF_WB + (j * 64 + 56 + kh * 4 + ((i & 1) ^ kh ^ 1)) * 1024, AUX_SC1);
      f32x16 acc[2] = {{}, {}};
      LAS char* wp0 = lds + slot_of(3) + kh * 15 * 1024 + lane * 16; LAS char* wp1 = lds + slot_of(3) + 1024 + kh * 13 * 1024 + lane * 16;
      OPQ_V(wp0); OPQ_V(wp1);
      pipe<32, 6>([&](auto I) AI { constexpr int i = I.v;
                    if constexpr (i < 28) return lds16(((i & 1) ? wp0 : wp1) + (i / 14) * SLOT + ((i % 14) >> 1) * 2048);
                    else return wl[i & 3]; },
                  [&]() AI {
                    poll(ws, fi_k(g, kh, ttB & 3, lane), 4 * L + 1);
                    TS(9 + 32 * L);
#pragma unroll
                    for (int i = 0; i < W0; ++i) { of[i] = ldf(rs, lane, ooff, i); __builtin_amdgcn_sched_barrier(0); }
                  },
                  [&](auto I, v4u wf) AI { constexpr int i = I.v;
                    mfma32p(wf, of[i >> 1], acc[(i & 1) ^ 1]);
                    if constexpr (i + W0 < 16) of[i + W0] = ldf(rs, lane, ooff, i + W0);
                  });
      TS(12 + 32 * L);
      res_epilogue<false>(acc, res, lds, rs, ws, out, w, lane, g, m, ttB, ntB, 2 * L + 1, active, false, 4 * L + 2);
      TS(13 + 32 * L);
    }
    auto phase_d = [&](auto PARC) AI {
      constexpr int PAR = PARC.v, W0 = 14;
      const int hoff = OFF_H + L * HBUF + (ttB * 112 + 56 * kh + 7 * PAR) * 1024;
      v4u hf[W0];
      f32x16 acc[2] = {{}, {}};
      LAS char* db[2];
      pipe<56, 6>(
          [&](auto I) AI { constexpr int i = I.v, r = i / 14;
            if constexpr (i % 14 == 0) {
              if constexpr (r < 2) BAR();
              if constexpr (r == 0) TS(27 + 32 * L);
              if constexpr (PAR && r == 0) DMA(wd + 4 * SLOT, 16);
              if constexpr (PAR && r == 1) DMA(wd + 6 * SLOT, 12);
              constexpr int sl = slot_of(r == 3 ? 12 : 12 + PAR + 2 * r);
              db[1] = lds + sl + kh * 15 * 1024 + l16; db[0] = lds + sl + 1024 + kh * 13 * 1024 + l16;
            }
            if constexpr (i == (PAR ? DQ : 42)) { if constexpr (PAR) VMWAIT(3); TS(28 + 32 * L); BAR(); TS(29 + 32 * L); }
            if constexpr (PAR && r >= 2) return wr[i - 28]; else return lds16(db[i & 1] + ((i % 14) >> 1) * 2048);
          },
          [&]() AI {
            poll(ws, (g * 16 + 8 * kh + PAR + 2 * (lane & 3)) * 4 + (ttB & 3), 4 * L + 3);
            TS(25 + 32 * L);
#pragma unroll
            for (int i = 0; i < W0; ++i) { hf[i] = ldf(rs, lane, hoff + (i / 7) * 14336, i % 7); __builtin_amdgcn_sched_barrier(0); }
          },
          [&](auto I, v4u wf) AI { constexpr int i = I.v;
            constexpr int k = i >> 1;
            mfma32p(wf, hf[k % W0], acc[(i & 1) ^ 1]);
            if constexpr ((i & 1) && k + W0 < 28) hf[k % W0] = ldf(rs, lane, hoff + ((k + W0) / 7) * 14336, (k + W0) % 7);
          });
      TS(30 + 32 * L);
      if constexpr (!PAR) { if (L == 0) { DMA(wa, 0); DMA(wa + SLOT, 1); } }
      res_epilogue<true>(acc, res, lds, rs, ws, out, w, lane, g, m, ttB, ntB, 2, active, L == 1, 4 * L + 4);
      TS(31 + 32 * L);
    };
    if constexpr (QR) {
      BAR();
      DMAT(wc + 3 * SLOT, 8); DMAT(wc + 4 * SLOT, 9);
      if (L == 0) {
#pragma unroll
        for (int i = 0; i < 14; ++i) { wr[14 + i] = __builtin_amdgcn_raw_buffer_load_b128(rs, l16, wd + 7 * SLOT + (kh * 14 + (i & ~1) + ((i & 1) ^ kh ^ 1)) * 1024, AUX_SC1); __builtin_amdgcn_sched_barrier(0); }
        VMWAIT(14);
      } else VMWAIT(0);
      BAR();
      DMA(wc + 5 * SLOT, 10); DMA(wc + 6 * SLOT, 11); DMA(wd, 12);
      if (L == 0) {
#pragma unroll
        for (int i = 0; i < 14; ++i) { wr[i] = __builtin_amdgcn_raw_buffer_load_b128(rs, l16, wd + 5 * SLOT + (kh * 14 + (i & ~1) + ((i & 1) ^ kh ^ 1)) * 1024, AUX_SC1); __builtin_amdgcn_sched_barrier(0); }
        VMWAIT(21);
      } else VMWAIT(7);
      BAR();
      DMA(wd + SLOT, 13); DMA(wd + 2 * SLOT, 14);
      BAR();
      DMA(wd + 3 * SLOT, 15);
      VMWAIT(7);
      __builtin_amdgcn_s_setprio(QPRIO);
      if (active) phase_d(IC<1>{}); else { VMWAIT(0); BAR(); }
      __builtin_amdgcn_s_setprio(0);
      if (L == 0) DMAT(wa + 2 * SLOT, 2);
    } else {
      constexpr int W0 = 8;
      v4u xf[28]; v2u st[7];
      const int xoff = OFF_X + (2 * L + 1) * XBUF + ttA * 28 * 1024, soff = OFF_ST + (2 * L + 1) * SBUF + ttA * 14 * 256;
      BAR();
      TS(16 + 32 * L);
      const int hoff = OFF_H + L * HBUF + (ttA * 112 + 7 * m) * 1024;
      LAS char* cs = lds + L_CS + 768 + t * 8;
      f32x16 a[4] = {{}, {}, {}, {}};
      float mean, rstd, c1, kk; v4u xa; float hv[8], ev = 0.f, tv = 0.f; v2u cw[7];
      auto epi = [&](const f32x16& acc, int e, int tile) AI {
        const int q = e >> 1;
        if (e & 1) {
          if (q > 0) { hv[q - 1] *= ev; OPQ_V(hv[q - 1]); }
          if (q < 8) { ev = __builtin_amdgcn_exp2f(tv); hv[q] = acc[q] * acc[8 + q]; OPQ_V(ev); OPQ_V(hv[q]); }
        } else if (e < 18) {
          if (q > 0) ev = __builtin_fmaf(ev, kk, kk);
          if (q < 8) { tv = acc[q] * c1; OPQ_V(tv); }
          if (q > 0) { ev = __builtin_amdgcn_rcpf(ev); OPQ_V(ev); }
        } else st16(v4u{pk(hv[0], hv[1]), pk(hv[2], hv[3]), pk(hv[4], hv[5]), pk(hv[6], hv[7])}, rs, lane * 16 + (tile & 3) * 1024, hoff + (tile >> 2) * 4096);
      };
      pipe<196, 5>(
          [&](auto I) AI { constexpr int i = I.v;
            if constexpr (i == 84 || i == 140) { TS(17 + 2 * ((i - 84) / 56) + 32 * L); BAR(); TS(18 + 2 * ((i - 84) / 56) + 32 * L); }
            if constexpr (i == 168) BAR();
            constexpr int tile = i < 84 ? i % 3 : i / 28;
            constexpr int ks = i < 84 ? i / 3 : i % 28;
            return i < 196 ? lds16(LSL(5 + tile) + ks * 1024) : v4u{};
          },
          [&]() AI {
            poll(ws, fi_x(g, w, lane), 4 * L + 2);
            TS(14 + 32 * L);
#pragma unroll
            for (int i = 0; i < W0; ++i) { xf[i] = ldf(rs, lane, xoff, i); __builtin_amdgcn_sched_barrier(0); }
          },
          [&](auto I, v4u wf) AI { constexpr int i = I.v;
            constexpr int tile = i < 84 ? i % 3 : i / 28;
            constexpr int ks = i < 84 ? i / 3 : i % 28;
            if constexpr (i >= 84 && ks == 0) { a[tile & 3] = f32x16{}; mfma32p(aug_w(cw[tile]), xa, a[tile & 3]); }
            mfma32p(wf, xf[ks], a[tile & 3]);
            if constexpr (i < 84 && i % 3 == 0 && i / 3 + W0 < 28) xf[i / 3 + W0] = ldf(rs, lane, xoff, i / 3 + W0);
            if constexpr (i % 3 == 1 && i / 3 < 7) st[i / 3] = ld8(rs, ((7 * h + i / 3) * 32 + t) * 8, soff);
            if constexpr (i >= 70 && i < 73) cw[i - 70] = *(LAS v2u*)(cs + (i - 70) * 256);
            if constexpr (i >= 78 && i % 28 == 22 && i < 170) cw[i / 28 + 1] = *(LAS v2u*)(cs + (i / 28 + 1) * 256);
            if constexpr (i == 62) { fin_stats(st, mean, rstd); OPQ_V(mean); OPQ_V(rstd); }
            if constexpr (i == 66) { xa = aug_x(-mean, h == 0); c1 = rstd * -1.4426950408889634f; kk = __builtin_amdgcn_rcpf(rstd * rstd); OPQ_V(xa); OPQ_V(c1); OPQ_V(kk); }
            if constexpr (i >= 75 && i < 78) mfma32p(aug_w(cw[i - 75]), xa, a[i - 75]);
            if constexpr (i >= 83) epi(a[((i - 83) / 19) & 3], (i - 83) % 19, (i - 83) / 19);
          });
      TS(21 + 32 * L);
      epi(a[1], 18, 5);
      st16(swiglu(a[2], rstd), rs, lane * 16 + 2048, hoff + 4096);
      TS(23 + 32 * L);
      publish(ws, myflag, 4 * L + 3, lane);
      TS(24 + 32 * L);
      if (active) phase_d(IC<0>{}); else { BAR(); if (L == 0) { DMA(wa, 0); DMA(wa + SLOT, 1); } }
    }
  }
  };
  if (w >= 4) run(IC<1>{}); else run(IC<0>{});
}

extern "C" void kernel_launch(void* const* d_in, const int* in_sizes, int n_in, void* d_out, int out_size, void* d_ws,
                              size_t ws_size, hipStream_t stream) {
  (void)in_sizes; (void)n_in; (void)out_size; (void)ws_size;
  (void)hipFuncSetAttribute((const void*)k_main, hipFuncAttributeMaxDynamicSharedMemorySize, LDS_BYTES);
  k_main<<<256, 512, LDS_BYTES, stream>>>((const char*)d_in[0], (const char*)d_in[1], (const char*)d_in[2], (const char*)d_in[4],
                                          (const char*)d_in[5], (const char*)d_in[6], (char*)d_ws, (unsigned short*)d_out);
}
```

```cpp
#include <hip/hip_runtime.h>
#include <hip/hip_bf16.h>

typedef unsigned v4u __attribute__((ext_vector_type(4)));
typedef unsigned v2u __attribute__((ext_vector_type(2)));
typedef float f32x4 __attribute__((ext_vector_type(4)));
typedef float f32x2 __attribute__((ext_vector_type(2)));
typedef float f32x16 __attribute__((ext_vector_type(16)));
typedef __bf16 bf16x8 __attribute__((ext_vector_type(8)));
typedef __bf16 bf16x2 __attribute__((ext_vector_type(2)));
#define LAS __attribute__((address_space(3)))
#define GAS __attribute__((address_space(1)))
#define DEV __device__ __forceinline__
#define AI __attribute__((always_inline))

constexpr int S_ = 2048, D_ = 448, F_ = 1792;
constexpr int OFF_FLAG = 0;
constexpr int OFF_PF = 65536, OFF_EP = OFF_PF + 65536;
constexpr int OFF_WA = 135168;
constexpr int OFF_WB = OFF_WA + 16 * 84 * 1024;
constexpr int OFF_WC = OFF_WB + 7 * 64 * 1024;
constexpr int OFF_WD = OFF_WC + 16 * 196 * 1024;
constexpr int OFF_CS = OFF_WD + 7 * 224 * 1024;
constexpr int XBUF = 64 * 28 * 1024, SBUF = 64 * 14 * 256, OBUF = 64 * 32 * 1024, HBUF = 64 * 112 * 1024;
constexpr int OFF_X = OFF_CS + 16 * 3072;
constexpr int OFF_ST = OFF_X + 4 * XBUF;
constexpr int OFF_O = OFF_ST + 4 * SBUF;
constexpr int OFF_H = OFF_O + 2 * OBUF;
constexpr int WS_BYTES = OFF_H + 2 * HBUF;
constexpr int SLOT = 28 * 1024;
constexpr int L_EX = 5 * SLOT, L_CS = L_EX + 16384, L_RS = L_CS + 3072, LDS_BYTES = L_RS + 512;
static_assert(LDS_BYTES <= 163840, "lds");
constexpr int AUX_SC1 = 16;

DEV unsigned pk(float a, float b) { bf16x2 t = {(__bf16)a, (__bf16)b}; return __builtin_bit_cast(unsigned, t); }
DEV float asf(unsigned u) { return __builtin_bit_cast(float, u); }
DEV float blo(unsigned u) { return __builtin_bit_cast(float, u << 16); }
DEV float bhi(unsigned u) { return __builtin_bit_cast(float, u & 0xffff0000u); }
DEV f32x16 mfma32(v4u a, v4u b, f32x16 c) {
  return __builtin_amdgcn_mfma_f32_32x32x16_bf16(__builtin_bit_cast(bf16x8, a), __builtin_bit_cast(bf16x8, b), c, 0, 0, 0);
}
DEV void mfma32p(v4u a, v4u b, f32x16& c) { c = mfma32(a, b, c); asm volatile("" : "+v"(c)); __builtin_amdgcn_sched_barrier(0); }
DEV f32x4 mfma16(v4u a, v4u b, f32x4 c) {
  return __builtin_amdgcn_mfma_f32_16x16x32_bf16(__builtin_bit_cast(bf16x8, a), __builtin_bit_cast(bf16x8, b), c, 0, 0, 0);
}
DEV float xsum32(float v) {
  auto r = __builtin_amdgcn_permlane32_swap(__builtin_bit_cast(unsigned, v), __builtin_bit_cast(unsigned, v), false, false);
  return __builtin_bit_cast(float, (unsigned)r[0]) + __builtin_bit_cast(float, (unsigned)r[1]);
}
DEV float xmax32(float v) {
  auto r = __builtin_amdgcn_permlane32_swap(__builtin_bit_cast(unsigned, v), __builtin_bit_cast(unsigned, v), false, false);
  return fmaxf(__builtin_bit_cast(float, (unsigned)r[0]), __builtin_bit_cast(float, (unsigned)r[1]));
}
DEV void split3(float v, unsigned& hi, unsigned& mid, unsigned& lo) {
  hi = __builtin_bit_cast(unsigned, v) & 0xffff0000u;
  float r = v - __builtin_bit_cast(float, hi);
  mid = __builtin_bit_cast(unsigned, r) & 0xffff0000u;
  float r2 = r - __builtin_bit_cast(float, mid);
  lo = __builtin_bit_cast(unsigned, r2) & 0xffff0000u;
}
DEV v4u aug_x(float negmean, bool on) {
  unsigned hi, mid, lo; split3(negmean, hi, mid, lo);
  v4u r = {hi | (hi >> 16), mid | (mid >> 16), lo | (hi >> 16), 0u};
  if (!on) r = v4u{0u, 0u, 0u, 0u};
  return r;
}
DEV v4u aug_w(v2u c) { return v4u{c.x, c.x, c.y, 0u}; }

typedef short s16x4 __attribute__((ext_vector_type(4)));
typedef __amdgpu_buffer_rsrc_t rsrc_t;
#define BAR() asm volatile("s_waitcnt lgkmcnt(0)\n\ts_barrier" ::: "memory")
#define VMWAIT(n) asm volatile("s_waitcnt vmcnt(" #n ")" ::: "memory")
DEV v4u ld16(rsrc_t rs, int voff, int soff) { return __builtin_amdgcn_raw_buffer_load_b128(rs, voff, soff, AUX_SC1); }
DEV v2u ld8(rsrc_t rs, int voff, int soff) { return __builtin_amdgcn_raw_buffer_load_b64(rs, voff, soff, AUX_SC1); }
DEV void st16(v4u v, rsrc_t rs, int voff, int soff) { __builtin_amdgcn_raw_buffer_store_b128(v, rs, voff + soff, 0, AUX_SC1); }
DEV void st8(v2u v, rsrc_t rs, int voff, int soff) { __builtin_amdgcn_raw_buffer_store_b64(v, rs, voff, soff, AUX_SC1); }
DEV v4u ldf(rsrc_t rs, int lane, int base, int i) { return ld16(rs, lane * 16 + (i & 3) * 1024, base + (i >> 2) * 4096); }
DEV v4u lds16(LAS char* p) { return *(LAS v4u*)p; }

DEV v2u tr8(LAS char* p) { return __builtin_bit_cast(v2u, __builtin_amdgcn_ds_read_tr16_b64_v4i16((LAS s16x4*)p)); }
DEV void pack_unit(int u, int tid, LAS char* sm, rsrc_t rs, const char* x, const char* wqkv, const char* wo, const char* wg,
                   const char* wu, const char* wd) {
  const int w = __builtin_amdgcn_readfirstlane(tid >> 6), lane = tid & 63, n = lane & 31, h = lane >> 5;
  if (u < 0) { BAR(); return; }
  if (u < 77) {
    const char* src; int ld;
    if (u < 21) { src = wqkv + u * 128; ld = 2688; } else if (u < 49) { src = wg + (u - 21) * 128; ld = 3584; } else { src = wu + (u - 49) * 128; ld = 3584; }
    src += (tid >> 3) * ld + (tid & 7) * 16;
    v4u v[14];
#pragma unroll
    for (int i = 0; i < 14; ++i) v[i] = *(const v4u*)(src + i * 32 * ld);
    float c[8] = {0.f, 0.f, 0.f, 0.f, 0.f, 0.f, 0.f, 0.f};
#pragma unroll
    for (int i = 0; i < 14; ++i)
#pragma unroll
      for (int q = 0; q < 4; ++q) { const unsigned e = v[i][q]; c[2 * q] += blo(e); c[2 * q + 1] += bhi(e); }
    *(LAS f32x4*)(sm + (tid >> 3) * 272 + (tid & 7) * 32) = f32x4{c[0], c[1], c[2], c[3]};
    *(LAS f32x4*)(sm + (tid >> 3) * 272 + (tid & 7) * 32 + 16) = f32x4{c[4], c[5], c[6], c[7]};
    BAR();
    if (tid < 64) {
      float cs = 0.f;
#pragma unroll
      for (int i = 0; i < 32; ++i) cs += *(LAS float*)(sm + i * 272 + tid * 4);
      unsigned hi, mid, lo; split3(cs, hi, mid, lo);
      int o;
      if (u < 21) { const int c_ = u * 64 + tid, t = c_ / 448, r = c_ % 448; o = (r / 28) * 3072 + t * 256 + (r % 28) * 8;
        if (r % 28 < 4) st8(v2u{0u, 0u}, rs, OFF_CS + o + 28 * 8, 0); }
      else { const int f = ((u - 21) % 28) * 64 + tid; o = (f / 112) * 3072 + (3 + (f % 112) / 16) * 256 + ((f & 15) + (u >= 49 ? 16 : 0)) * 8; }
      st8(v2u{(hi >> 16) | mid, (lo >> 16) | hi}, rs, OFF_CS + o, 0);
    }
    return;
  }
  const bool isx = u >= 119 && u < 183;
  const int lstride = isx ? 912 : 960;
  int dst0 = 0, dstep = 0, cstep = 128, col0 = 64 * (w >> 1), row0 = 16 * (w & 1);
  {
    const char* src; int ld = 896;
    if (u < 119) { const int q = u - 77; ld = 2688; src = wqkv + ((q / 3) * 32 + 8 * w) * 2688 + (q % 3) * 896;
      dst0 = OFF_WA + (((w >> 1) * 3 + q % 3) * 28 + 2 * (q / 3) + (w & 1)) * 1024; dstep = 168 * 1024; cstep = 112; col0 = 56 * (w >> 1); }
    else if (u < 183) { const int tt = u - 119, g = tt >> 2; ld = 1792; src = x + (2 * (128 * (g & 7) + 32 * (tt & 3) + 8 * w) + (g >> 3)) * 896; }
    else if (u < 199) { const int s = 2 * (u - 183) + (w & 1), kh = s >> 4, ks = s & 15, c = ks / 7; src = wo + ((u - 183) * 28 + 8 * w) * 896;
      dst0 = OFF_WB + (c * 28 + kh * (c < 2 ? 14 : 4) + (ks % 7) * 2 + (w >> 1)) * 1024; dstep = 64 * 1024; }
    else if (u < 311) { const int q = u - 199; ld = 3584; src = (w < 2 ? wg : wu) + ((q >> 2) * 16 + 8 * (w & 1)) * 3584 + (q & 3) * 896;
      dst0 = OFF_WC + (((q & 3) * 28 + w) * 28 + (q >> 2)) * 1024; dstep = 112 * 1024; col0 = 32 * w; }
    else { const int s = 2 * (u - 311) + (w & 1), kh = s / 56, ks = s % 56; src = wd + ((u - 311) * 32 + 8 * w) * 896;
      dst0 = OFF_WD + ((ks / 7) * 28 + kh * 14 + (ks % 7) * 2 + (w >> 1)) * 1024; dstep = 224 * 1024; }
    const bool pad = u >= 183 && u < 199 && w == 3;
    if (lane < 56) {
      v4u v[8];
#pragma unroll
      for (int i = 0; i < 8; ++i) v[i] = *(const v4u*)(src + ((pad && i >= 4) ? 0 : i * ld) + lane * 16);
#pragma unroll
      for (int i = 0; i < 8; ++i) *(LAS v4u*)(sm + (8 * w + i) * lstride + lane * 16) = (pad && i >= 4) ? v4u{0u, 0u, 0u, 0u} : v[i];
    }
  }
  BAR();
  if (isx) {
    const int tt = u - 119;
#pragma unroll
    for (int it = 0; it < 4; ++it) {
      const int nt = w + 4 * it;
      if (nt >= 14) break;
      v2u a[4];
#pragma unroll
      for (int i = 0; i < 4; ++i) a[i] = *(LAS v2u*)(sm + n * 912 + nt * 64 + i * 16 + h * 8);
      __builtin_amdgcn_raw_buffer_store_b128(v4u{a[0].x, a[0].y, a[1].x, a[1].y}, rs, OFF_X + (tt * 28 + 2 * nt) * 1024 + lane * 16, 0, AUX_SC1);
      __builtin_amdgcn_raw_buffer_store_b128(v4u{a[2].x, a[2].y, a[3].x, a[3].y}, rs, OFF_X + (tt * 28 + 2 * nt + 1) * 1024 + lane * 16, 0, AUX_SC1);
      float sm1 = 0.f;
#pragma unroll
      for (int i = 0; i < 4; ++i) sm1 += blo(a[i].x) + bhi(a[i].x) + blo(a[i].y) + bhi(a[i].y);
      sm1 = xsum32(sm1);
      const float mu = sm1 * (1.f / 32.f);
      float m2 = 0.f;
#pragma unroll
      for (int i = 0; i < 4; ++i) {
        const float d0 = blo(a[i].x) - mu, d1 = bhi(a[i].x) - mu, d2 = blo(a[i].y) - mu, d3 = bhi(a[i].y) - mu;
        m2 += d0 * d0 + d1 * d1 + d2 * d2 + d3 * d3;
      }
      m2 = xsum32(m2);
      if (h == 0) st8(v2u{__builtin_bit_cast(unsigned, sm1), __builtin_bit_cast(unsigned, m2)}, rs, OFF_ST + (tt * 14 + nt) * 256 + n * 8, 0);
    }
    return;
  }
  const int q4 = (lane & 15) >> 2, p4 = lane & 3, cb = (lane >> 4) & 1;
  const bool gu = u >= 199 && u < 311;
  LAS char* pa = sm + ((gu ? 16 * cb : row0) + 4 * h + q4) * 960 + col0 + (gu ? 0 : 32 * cb) + 8 * p4;
  v2u lo[8], hi[8];
#pragma unroll
  for (int i = 0; i < 8; ++i) { LAS char* q = pa + ((i < 7 || u < 119) ? i * cstep : 0); lo[i] = tr8(q); hi[i] = tr8(q + 8 * 960); }
  const bool z = u < 119 && n >= 28;
#pragma unroll
  for (int i = 0; i < 8; ++i)
    if (i < 7 || u < 119) st16(z ? v4u{0u, 0u, 0u, 0u} : v4u{lo[i].x, lo[i].y, hi[i].x, hi[i].y}, rs, lane * 16, dst0 + i * dstep);
}

DEV void poll(char* ws, int fi, unsigned want) {
  GAS unsigned* f = (GAS unsigned*)(ws + OFF_FLAG) + fi * 16;
  for (int spin = 0; spin < (1 << 20); ++spin) {
    const unsigned v = __hip_atomic_load(f, __ATOMIC_RELAXED, __HIP_MEMORY_SCOPE_AGENT);
    if (__all(v >= want)) break;
  }
  __builtin_amdgcn_fence(__ATOMIC_ACQUIRE, "wavefront");
}
DEV int fi_x(int g, int T, int lane) { const int i = lane < 14 ? lane : 0; return (g * 16 + 8 * (T >> 1) + (i >> 1)) * 4 + (T & 1) + 2 * (i & 1); }
DEV int fi_k(int g, int kh, int T, int lane) { return (g * 16 + 8 * kh + (lane & 7)) * 4 + T; }
DEV void publish(char* ws, int fi, unsigned val, int lane) {
  VMWAIT(0);
  if (lane == 0) __hip_atomic_store((GAS unsigned*)(ws + OFF_FLAG) + fi * 16, val, __ATOMIC_RELAXED, __HIP_MEMORY_SCOPE_AGENT);
}
template <int N, int TH> DEV void dma(rsrc_t rs, int src, int dst, int hw, int lane16) {
#pragma unroll
  for (int i = 0; i < N; ++i) {
    unsigned keep;
    asm volatile("s_mov_b32 %0, m0\n\ts_mov_b32 m0, %1\n\ts_nop 0\n\tbuffer_load_dwordx4 %2, %3, %4 offen sc1 lds\n\ts_mov_b32 m0, %0"
                 : "=&s"(keep) : "s"(dst + (hw * N + i) * 1024), "v"(lane16), "s"(rs), "s"(src + (hw * N + i) * 1024) : "memory");
    if constexpr (TH >= 0) asm volatile("s_waitcnt vmcnt(%0)" :: "i"(TH) : "memory");
  }
}
#define THR 4
#define DQ 42
#define QPRIO 1
#define QDELAY 75
#define DMA(src, c) dma<7, -1>(rs, src, slot_of(c), hw, l16)
#define DMAT(src, c) dma<7, THR>(rs, src, slot_of(c), hw, l16)
#define OPQ_S(x) asm volatile("" : "+s"(x))
#define OPQ_V(x) asm volatile("" : "+v"(x))
template <int I> struct IC { static constexpr int v = I; };
template <int I, int N, class F> DEV void sfor(F& f) { if constexpr (I < N) { f(IC<I>{}); sfor<I + 1, N>(f); } }
template <int N, int PF, class RD, class MID, class ST> DEV void pipe(RD rd, MID mid, ST st) {
  constexpr int R = PF + 1;
  v4u wb[R];
  auto pro = [&](auto I) __attribute__((always_inline)) { wb[I.v] = rd(I); };
  sfor<0, PF>(pro);
  mid();
  __builtin_amdgcn_sched_barrier(0);
  auto body = [&](auto I) __attribute__((always_inline)) {
    constexpr int i = I.v;
    st(I, wb[i % R]);
    if constexpr (i + PF < N) wb[(i + PF) % R] = rd(IC<i + PF>{});
    asm volatile("" :: "v"(wb[i % R]));
    __builtin_amdgcn_sched_barrier(0);
  };
  sfor<0, N>(body);
}
constexpr int slot_of(int c) { return (c % 5) * SLOT; }
DEV void fin_stats(const v2u (&st)[7], float& mean, float& rstd) {
  float sm = 0.f;
#pragma unroll
  for (int i = 0; i < 7; ++i) sm += asf(st[i].x);
  mean = xsum32(sm) * (1.f / D_);
  float m2 = 0.f;
#pragma unroll
  for (int i = 0; i < 7; ++i) {
    const float d = asf(st[i].x) * (1.f / 32.f) - mean;
    m2 += asf(st[i].y) + 32.f * d * d;
  }
  rstd = __builtin_amdgcn_rsqf(xsum32(m2) * (1.f / D_) + 1e-5f);
}
DEV v4u pack8(const f32x16& a, int o, float sc) {
  return v4u{pk(a[o] * sc, a[o + 1] * sc), pk(a[o + 2] * sc, a[o + 3] * sc), pk(a[o + 4] * sc, a[o + 5] * sc), pk(a[o + 6] * sc, a[o + 7] * sc)};
}
DEV v4u swiglu(const f32x16& a, float rstd) {
  float hv[8];
#pragma unroll
  for (int i = 0; i < 8; ++i) {
    const float g = a[i] * rstd, up = a[8 + i] * rstd;
    const float e = __builtin_amdgcn_exp2f(g * -1.4426950408889634f);
    hv[i] = g * up * __builtin_amdgcn_rcpf(1.f + e);
  }
  return v4u{pk(hv[0], hv[1]), pk(hv[2], hv[3]), pk(hv[4], hv[5]), pk(hv[6], hv[7])};
}
#define TS(i)

template <bool W8> DEV void res_epilogue(f32x16 (&acc)[2], v4u (&res)[2], LAS char* lds, rsrc_t rs, char* ws, unsigned short* out, int w, int lane,
                      int g, int m, int tt, int nt, int xb, bool active, bool last, unsigned flagval) {
  LAS char* sx = lds + (W8 ? 3 * SLOT : L_EX) + lane * 16;
  float v[16];
#pragma unroll
  for (int q = 0; q < 8; ++q) { const unsigned r = res[q >> 2][q & 3]; v[2 * q] = acc[0][2 * q] + blo(r); v[2 * q + 1] = acc[0][2 * q + 1] + bhi(r); }
#pragma unroll
  for (int q = 0; q < 4; ++q) *(LAS f32x4*)(sx + w * 4096 + q * 1024) = f32x4{acc[1][4 * q], acc[1][4 * q + 1], acc[1][4 * q + 2], acc[1][4 * q + 3]};
  if (W8 && w >= 4) {
#pragma unroll
    for (int q = 0; q < 4; ++q) *(LAS f32x4*)(sx + (w + 4) * 4096 + q * 1024) = f32x4{acc[0][4 * q], acc[0][4 * q + 1], acc[0][4 * q + 2], acc[0][4 * q + 3]};
  }
  BAR();
  if (W8 && w >= 4) return;
#pragma unroll
  for (int q = 0; q < 4; ++q) {
    f32x4 p0 = *(LAS f32x4*)(sx + (w ^ 2) * 4096 + q * 1024);
    if (W8) p0 += *(LAS f32x4*)(sx + (w ^ 6) * 4096 + q * 1024) + *(LAS f32x4*)(sx + (w + 8) * 4096 + q * 1024);
#pragma unroll
    for (int e = 0; e < 4; ++e) v[4 * q + e] += p0[e];
  }
#pragma unroll
  for (int q = 0; q < 8; ++q) res[q >> 2][q & 3] = pk(v[2 * q], v[2 * q + 1]);
  if (!active) return;
  if (last) {
    const int s = 2 * (128 * (g & 7) + 32 * (tt & 3) + (lane & 31)) + (g >> 3);
    unsigned short* rowp = out + (size_t)s * D_ + 32 * nt + 8 * (lane >> 5);
#pragma unroll
    for (int q = 0; q < 2; ++q) {
      auto a = __builtin_amdgcn_permlane32_swap(res[q][0], res[q][2], false, false);
      auto b = __builtin_amdgcn_permlane32_swap(res[q][1], res[q][3], false, false);
      *(v4u*)(rowp + 16 * q) = v4u{(unsigned)a[0], (unsigned)b[0], (unsigned)a[1], (unsigned)b[1]};
    }
    return;
  }
  st16(res[0], rs, lane * 16, OFF_X + xb * XBUF + (tt * 28 + 2 * nt) * 1024);
  st16(res[1], rs, lane * 16 + 1024, OFF_X + xb * XBUF + (tt * 28 + 2 * nt) * 1024);
  __builtin_amdgcn_sched_barrier(0);
  float s0 = 0.f, s1 = 0.f, q0 = 0.f, q1 = 0.f;
#pragma unroll
  for (int q = 0; q < 8; ++q) { s0 += v[2 * q]; s1 += v[2 * q + 1]; q0 += v[2 * q] * v[2 * q]; q1 += v[2 * q + 1] * v[2 * q + 1]; }
  const float sm = xsum32(s0 + s1), sq = xsum32(q0 + q1);
  const float m2 = fmaxf(sq - sm * sm * (1.f / 32.f), 0.f);
  if (lane < 32) st8(v2u{__builtin_bit_cast(unsigned, sm), __builtin_bit_cast(unsigned, m2)}, rs, lane * 8, OFF_ST + xb * SBUF + (tt * 14 + nt) * 256);
  publish(ws, (g * 16 + m) * 4 + w, flagval, lane);
}

__global__ __launch_bounds__(512, 2) void k_main(const char* x, const char* wqkv, const char* wo, const char* wg_, const char* wu_,
                                                  const char* wd_, char* ws, unsigned short* out) {
  extern __shared__ __attribute__((aligned(16))) char smem[];
  LAS char* lds = (LAS char*)smem;
  const int tid = threadIdx.x; int lane = tid & 63;
  int w = __builtin_amdgcn_readfirstlane(tid >> 6);
  const int b = blockIdx.x, xcd = b & 7, idx = b >> 3;
  int g = (xcd >> 1) * 4 + (idx >> 3), m = (xcd & 1) * 8 + (idx & 7);
  const int half = m >> 3, j = (m & 7) < 7 ? (m & 7) : 6;
  const bool active = (m & 7) < 7;
  const rsrc_t rs = __builtin_amdgcn_make_buffer_rsrc((void*)ws, 0, WS_BYTES, 0x00020000);
  TS(0);
  const unsigned tag = __hip_atomic_load((GAS unsigned*)(ws + OFF_EP), __ATOMIC_RELAXED, __HIP_MEMORY_SCOPE_AGENT) + 1u;
  auto poll_setup = [&](int base, auto NC) AI {
    for (int spin = 0; spin < (1 << 20); ++spin) {
      bool ok = true;
#pragma unroll
      for (int i = 0; i < NC.v; ++i) ok &= __builtin_amdgcn_raw_buffer_load_b32(rs, lane * 128, base + i * 8192, AUX_SC1) == tag;
      if (__all(ok)) break;
      asm volatile("s_sleep 2" ::: "memory");
    }
    __builtin_amdgcn_fence(__ATOMIC_ACQUIRE, "wavefront");
  };
  v4u res[2];
  if (w < 4) {
    if (lane == 0) __hip_atomic_store((GAS unsigned*)(ws + OFF_FLAG) + ((g * 16 + m) * 4 + w) * 16, 0u, __ATOMIC_RELAXED, __HIP_MEMORY_SCOPE_AGENT);
    LAS unsigned* cnt = (LAS unsigned*)(lds + L_RS);
    if (tid == 0) *cnt = 0u;
    pack_unit(b < 21 ? b : b < 127 ? b + 56 : -1, tid, lds, rs, x, wqkv, wo, wg_, wu_, wd_);
    VMWAIT(0);
    if (lane == 0 && __hip_atomic_fetch_add(cnt, 1u, __ATOMIC_RELAXED, __HIP_MEMORY_SCOPE_WORKGROUP) == 3u)
      __hip_atomic_store((GAS unsigned*)(ws + OFF_PF) + b * 32, tag, __ATOMIC_RELAXED, __HIP_MEMORY_SCOPE_AGENT);
    TS(2);
    BAR();
    if (w == 0) poll_setup(OFF_PF, IC<2>{});
    else {
      unsigned tv[4];
#pragma unroll
      for (int i = 0; i < 3; ++i) tv[i] = __builtin_amdgcn_raw_buffer_load_b32(rs, ((w - 1) * 192 + i * 64 + lane) * 65536 + 65408, 0, AUX_SC1);
      tv[3] = __hip_atomic_load((GAS unsigned*)((char*)out + (lane < 28 ? lane : 0) * 65536), __ATOMIC_RELAXED, __HIP_MEMORY_SCOPE_AGENT);
      asm volatile("" :: "v"(tv[0]), "v"(tv[1]), "v"(tv[2]), "v"(tv[3]));
    }
    BAR();
    res[0] = ld16(rs, lane * 16, OFF_X + ((g * 4 + 2 * half + (w & 1)) * 28 + 2 * (2 * j + (w >> 1))) * 1024);
    res[1] = ld16(rs, lane * 16 + 1024, OFF_X + ((g * 4 + 2 * half + (w & 1)) * 28 + 2 * (2 * j + (w >> 1))) * 1024);
  } else {
    res[0] = res[1] = v4u{0u, 0u, 0u, 0u};
    BAR();
    if (b >= 127) __builtin_amdgcn_s_sleep(QDELAY);
    pack_unit(b < 16 ? -1 : b < 200 ? 167 + b : b - 179, tid & 255, lds + 3 * SLOT, rs, x, wqkv, wo, wg_, wu_, wd_);
    VMWAIT(0);
    BAR();
    if (tid == 256) __hip_atomic_store((GAS unsigned*)(ws + OFF_PF + 32768) + b * 32, tag, __ATOMIC_RELAXED, __HIP_MEMORY_SCOPE_AGENT);
    {
      const int hw = w - 4, l16 = lane * 16, wa = OFF_WA + m * 84 * 1024;
      dma<1, -1>(rs, OFF_CS + m * 3072, L_CS, 0, l16);
      DMA(wa, 0); DMA(wa + SLOT, 1); DMA(wa + 2 * SLOT, 2);
    }
  }
  auto run = [&](auto QRC) AI {
  constexpr bool QR = QRC.v;
  v4u wr[28];
  if constexpr (QR) {
#pragma unroll
    for (int i = 0; i < 28; ++i) wr[i] = v4u{0u, 0u, 0u, 0u};
  }
  for (int L = 0; L < 2; ++L) {
    OPQ_S(w); OPQ_S(g); OPQ_S(m); OPQ_V(lane);
    const int t = lane & 31, h = lane >> 5;
    const int mw = w & 1, kh = (w >> 1) & 1, par = w >> 2, hw = w & 3, l16 = lane * 16;
    const int wa = OFF_WA + m * 84 * 1024, wb = OFF_WB + j * 64 * 1024, wc = OFF_WC + m * 196 * 1024, wd = OFF_WD + j * 224 * 1024;
    const int ttA = g * 4 + w;
    const int ttB = g * 4 + 2 * half + mw;
    const int ntB = 2 * j + kh;
    const int myflag = (g * 16 + m) * 4 + w;
    LAS char* lb[3] = {lds + lane * 16, lds + 2 * SLOT + lane * 16, lds + 4 * SLOT + lane * 16};
    OPQ_V(lb[1]); OPQ_V(lb[2]);
#define LSL(c) (lb[((c) % 5) >> 1] + (((c) % 5) & 1) * SLOT)
    if constexpr (QR) {
      {
        const bool hasprev = (g & 7) > 0;
        const int gp = hasprev ? g - 1 : g, ttp = gp * 4 + 3;
        VMWAIT(0);
        BAR();
        if (L) poll(ws, fi_x(gp, 3, lane), 4);
        const int t16 = lane & 15, gq = lane >> 4, tk = 24 + (t16 & 7), isv = hw >> 1, nh = hw & 1;
        v4u xf[14]; v2u st[4];
#pragma unroll
        for (int i = 0; i < 4; ++i) st[i] = ld8(rs, ((gq + 4 * i < 14 ? gq + 4 * i : 0) * 32 + tk) * 8, OFF_ST + 2 * L * SBUF + ttp * 14 * 256);
#pragma unroll
        for (int i = 0; i < 14; ++i) xf[i] = ld16(rs, (gq >> 1) * 1024 + ((gq & 1) * 32 + tk) * 16, OFF_X + 2 * L * XBUF + (ttp * 28 + 2 * i) * 1024);
        if (L) { DMA(wb, 3); DMA(wb + SLOT, 4); }
        float sm = 0.f;
#pragma unroll
        for (int i = 0; i < 4; ++i) sm += (gq + 4 * i < 14) ? asf(st[i].x) : 0.f;
        sm += __shfl_xor(sm, 16); sm = xsum32(sm);
        const float mean = sm * (1.f / D_);
        float m2 = 0.f;
#pragma unroll
        for (int i = 0; i < 4; ++i) {
          const float d = asf(st[i].x) * (1.f / 32.f) - mean;
          m2 += (gq + 4 * i < 14) ? asf(st[i].y) + 32.f * d * d : 0.f;
        }
        m2 += __shfl_xor(m2, 16); m2 = xsum32(m2);
        const float rstd = __builtin_amdgcn_rsqf(m2 * (1.f / D_) + 1e-5f);
        LAS char* wp = lds + (1 + isv) * SLOT + (gq >> 1) * 1024 + ((gq & 1) * 32 + 16 * nh + t16) * 16;
        const v4u xa = aug_x(-mean, gq == 0), wg_ = aug_w(*(LAS v2u*)(lds + L_CS + (1 + isv) * 256 + (16 * nh + t16) * 8));
        f32x4 a = {0.f, 0.f, 0.f, 0.f};
        if (isv) {
#pragma unroll
          for (int i = 0; i < 14; ++i) a = mfma16(xf[i], lds16(wp + i * 2048), a);
          a = mfma16(xa, wg_, a);
          float r[4];
#pragma unroll
          for (int i = 0; i < 4; ++i) r[i] = __shfl(rstd, 4 * gq + i);
          *(LAS v2u*)(lds + L_EX + 2048 + (16 * nh + t16 + 32 * (gq & 1)) * 16 + 8 * (gq >> 1)) = v2u{pk(a[0] * r[0], a[1] * r[1]), pk(a[2] * r[2], a[3] * r[3])};
        } else {
#pragma unroll
          for (int i = 0; i < 14; ++i) a = mfma16(lds16(wp + i * 2048), xf[i], a);
          a = mfma16(wg_, xa, a);
          *(LAS v2u*)(lds + L_EX + nh * 1024 + (16 + t16 + 32 * (gq & 1)) * 16 + 8 * (gq >> 1)) = v2u{pk(a[0] * rstd, a[1] * rstd), pk(a[2] * rstd, a[3] * rstd)};
        }
      }
      if (L == 0) {
        poll_setup(OFF_PF + 32768 + hw * 8192, IC<1>{});
        BAR();
        if (b == 0 && tid == 256) __hip_atomic_store((GAS unsigned*)(ws + OFF_EP), tag + 1u, __ATOMIC_RELAXED, __HIP_MEMORY_SCOPE_AGENT);
        dma<1, -1>(rs, OFF_CS + m * 3072, L_CS, hw < 3 ? hw : 2, l16);
        DMA(wb, 3); DMA(wb + SLOT, 4);
      }
      BAR();
      DMA(wc, 5); DMA(wc + SLOT, 6);
      VMWAIT(14);
    } else {
      constexpr int W0 = 8;
      v4u xf[28]; v2u st[7];
      const int xoff = OFF_X + 2 * L * XBUF + ttA * 28 * 1024, soff = OFF_ST + 2 * L * SBUF + ttA * 14 * 256;
      if (L) poll(ws, fi_x(g, w, lane), 4);
      TS(3 + 32 * L);
#pragma unroll
      for (int i = 0; i < W0; ++i) { xf[i] = ldf(rs, lane, xoff, i); __builtin_amdgcn_sched_barrier(0); }
      BAR();
      TS(1 + 32 * L);
      f32x16 aq = {}, ak = {}, av = {};
      float mean, rstd; v4u xa; v2u cw[3]; f32x4 rv[4];
      float bias[20];
      const bool hasprev = w > 0 || (g & 7) > 0;
      const float NEG = -__builtin_inff();
      pipe<84, 6>([&](auto I) AI { constexpr int i = I.v; return i < 84 ? lds16(LSL((i + 1) % 3) + (i / 3) * 1024) : v4u{}; },
                  [&]() AI {},
                  [&](auto I, v4u wf) AI { constexpr int i = I.v;
                    if constexpr (i % 3 == 0) mfma32p(wf, xf[i / 3], ak);
                    else if constexpr (i % 3 == 1) mfma32p(xf[i / 3], wf, av);
                    else mfma32p(wf, xf[i / 3], aq);
                    if constexpr (i % 3 == 0 && i / 3 + W0 < 28) xf[i / 3 + W0] = ldf(rs, lane, xoff, i / 3 + W0);
                    if constexpr (i % 3 == 1 && i / 3 < 7) st[i / 3] = ld8(rs, ((7 * h + i / 3) * 32 + t) * 8, soff);
                    if constexpr (i == 70) { if (L == 0) BAR(); }
                    if constexpr (i >= 2 && i < 62 && i % 3 == 2) {
                      constexpr int r = i / 3;
                      if constexpr (r < 16) bias[r] = ((unsigned)(t - 4 * h - ((r & 3) + 8 * (r >> 2))) <= 7u) ? 0.f : NEG;
                      else bias[r] = (hasprev && r - 17 + 4 * h >= t) ? 0.f : NEG;
                      OPQ_V(bias[r]);
                    }
                    if constexpr (i == 62) { fin_stats(st, mean, rstd); OPQ_V(mean); OPQ_V(rstd); }
                    if constexpr (i == 66) { xa = aug_x(-mean, h == 0); OPQ_V(xa); *(LAS float*)(lds + L_RS + w * 128 + t * 4) = rstd; }
                    if constexpr (i >= 66 && i < 69) cw[i - 66] = *(LAS v2u*)(lds + L_CS + (i - 66) * 256 + t * 8);
                    if constexpr (i == 72) mfma32p(aug_w(cw[1]), xa, ak);
                    if constexpr (i == 73) mfma32p(xa, aug_w(cw[2]), av);
                    if constexpr (i == 74) mfma32p(aug_w(cw[0]), xa, aq);
                    if constexpr (i >= 78 && i < 82) rv[i - 78] = *(LAS f32x4*)(lds + L_RS + w * 128 + (8 * (i - 78) + 4 * h) * 4);
                  });
      TS(4 + 32 * L);
      const v4u kf0 = pack8(ak, 0, rstd), kf1 = pack8(ak, 8, rstd);
      v4u vf[2];
#pragma unroll
      for (int q = 0; q < 4; ++q) {
        vf[q >> 1][(q & 1) * 2] = pk(av[4 * q] * rv[q][0], av[4 * q + 1] * rv[q][1]);
        vf[q >> 1][(q & 1) * 2 + 1] = pk(av[4 * q + 2] * rv[q][2], av[4 * q + 3] * rv[q][3]);
      }
      if (w < 3) {
        LAS char* e = lds + L_EX + (w + 1) * 3072 + lane * 16;
        *(LAS v4u*)e = kf0; *(LAS v4u*)(e + 1024) = kf1; *(LAS v4u*)(e + 2048) = vf[1];
      }
      const v4u qf0 = pack8(aq, 0, 1.f), qf1 = pack8(aq, 8, 1.f);
      f32x16 sc;
#pragma unroll
      for (int r = 0; r < 16; ++r) sc[r] = bias[r];
      mfma32p(kf0, qf0, sc); mfma32p(kf1, qf1, sc);
      TS(5 + 32 * L);
      BAR();
      TS(6 + 32 * L);
      LAS char* e = lds + L_EX + w * 3072 + lane * 16;
      f32x16 sp = {};
#pragma unroll
      for (int r = 0; r < 4; ++r) sp[12 + r] = bias[16 + r];
      mfma32p(lds16(e), qf0, sp); mfma32p(lds16(e + 1024), qf1, sp);
      const v4u vp = lds16(e + 2048);
      float p[16], pp[4], mx = NEG;
#pragma unroll
      for (int r = 0; r < 16; ++r) { p[r] = sc[r]; mx = fmaxf(mx, p[r]); }
#pragma unroll
      for (int r = 0; r < 4; ++r) { pp[r] = sp[12 + r]; mx = fmaxf(mx, pp[r]); }
      const float cf = rstd * (0.18898223650461362f * 1.4426950408889634f);
      const float mc = -xmax32(mx) * cf;
#pragma unroll
      for (int r = 0; r < 16; ++r) p[r] = __builtin_amdgcn_exp2f(__builtin_fmaf(p[r], cf, mc));
#pragma unroll
      for (int r = 0; r < 4; ++r) pp[r] = __builtin_amdgcn_exp2f(__builtin_fmaf(pp[r], cf, mc));
      const v4u pf0 = {pk(p[0], p[1]), pk(p[2], p[3]), pk(p[4], p[5]), pk(p[6], p[7])};
      const v4u pf1 = {pk(p[8], p[9]), pk(p[10], p[11]), pk(p[12], p[13]), pk(p[14], p[15])};
      const v4u pfp = {0u, 0u, pk(pp[0], pp[1]), pk(pp[2], pp[3])};
      f32x16 o = {};
      mfma32p(vf[0], pf0, o); mfma32p(vf[1], pf1, o); mfma32p(vp, pfp, o);
      float s0 = pp[0] + pp[1], s1 = pp[2] + pp[3];
#pragma unroll
      for (int r = 0; r < 8; ++r) { s0 += p[2 * r]; s1 += p[2 * r + 1]; }
      const float inv = __builtin_amdgcn_rcpf(xsum32(s0 + s1));
      st16(pack8(o, 0, inv), rs, lane * 16, OFF_O + L * OBUF + (ttA * 32 + 2 * m) * 1024);
      st16(pack8(o, 8, inv), rs, lane * 16 + 1024, OFF_O + L * OBUF + (ttA * 32 + 2 * m) * 1024);
      TS(7 + 32 * L);
      publish(ws, myflag, 4 * L + 1, lane);
      TS(8 + 32 * L);
    }
    if constexpr (QR) {
      if (active) BAR();
      DMAT(wc + 2 * SLOT, 7);
      if (active) BAR();
      VMWAIT(0);
    } else if (active) {
      constexpr int W0 = 8;
      v4u of[16];
      const int ooff = OFF_O + L * OBUF + (ttB * 32 + 16 * kh) * 1024;
      BAR();
      TS(11 + 32 * L);
      v4u wl[4];
#pragma unroll
      for (int i = 0; i < 4; ++i) wl[i] = __builtin_amdgcn_raw_buffer_load_b128(rs, lane * 16 + (i & 2) * 1024, OFF_WB + (j * 64 + 56 + kh * 4 + ((i & 1) ^ kh ^ 1)) * 1024, AUX_SC1);
      f32x16 acc[2] = {{}, {}};
      LAS char* wp0 = lds + slot_of(3) + kh * 15 * 1024 + lane * 16; LAS char* wp1 = lds + slot_of(3) + 1024 + kh * 13 * 1024 + lane * 16;
      OPQ_V(wp0); OPQ_V(wp1);
      pipe<32, 6>([&](auto I) AI { constexpr int i = I.v;
                    if constexpr (i < 28) return lds16(((i & 1) ? wp0 : wp1) + (i / 14) * SLOT + ((i % 14) >> 1) * 2048);
                    else return wl[i & 3]; },
                  [&]() AI {
                    poll(ws, fi_k(g, kh, ttB & 3, lane), 4 * L + 1);
                    TS(9 + 32 * L);
#pragma unroll
                    for (int i = 0; i < W0; ++i) { of[i] = ldf(rs, lane, ooff, i); __builtin_amdgcn_sched_barrier(0); }
                  },
                  [&](auto I, v4u wf) AI { constexpr int i = I.v;
                    mfma32p(wf, of[i >> 1], acc[(i & 1) ^ 1]);
                    if constexpr (i + W0 < 16) of[i + W0] = ldf(rs, lane, ooff, i + W0);
                  });
      TS(12 + 32 * L);
      res_epilogue<false>(acc, res, lds, rs, ws, out, w, lane, g, m, ttB, ntB, 2 * L + 1, active, false, 4 * L + 2);
      TS(13 + 32 * L);
    }
    auto phase_d = [&](auto PARC) AI {
      constexpr int PAR = PARC.v, W0 = 14;
      const int hoff = OFF_H + L * HBUF + (ttB * 112 + 56 * kh + 7 * PAR) * 1024;
      v4u hf[W0];
      f32x16 acc[2] = {{}, {}};
      LAS char* db[2];
      pipe<56, 6>(
          [&](auto I) AI { constexpr int i = I.v, r = i / 14;
            if constexpr (i % 14 == 0) {
              if constexpr (r < 2) BAR();
              if constexpr (r == 0) TS(27 + 32 * L);
              if constexpr (PAR && r == 0) DMA(wd + 4 * SLOT, 16);
              if constexpr (PAR && r == 1) DMA(wd + 6 * SLOT, 12);
              constexpr int sl = slot_of(r == 3 ? 12 : 12 + PAR + 2 * r);
              db[1] = lds + sl + kh * 15 * 1024 + l16; db[0] = lds + sl + 1024 + kh * 13 * 1024 + l16;
            }
            if constexpr (i == (PAR ? DQ : 42)) { if constexpr (PAR) VMWAIT(3); TS(28 + 32 * L); BAR(); TS(29 + 32 * L); }
            if constexpr (PAR && r >= 2) return wr[i - 28]; else return lds16(db[i & 1] + ((i % 14) >> 1) * 2048);
          },
          [&]() AI {
            poll(ws, (g * 16 + 8 * kh + PAR + 2 * (lane & 3)) * 4 + (ttB & 3), 4 * L + 3);
            TS(25 + 32 * L);
#pragma unroll
            for (int i = 0; i < W0; ++i) { hf[i] = ldf(rs, lane, hoff + (i / 7) * 14336, i % 7); __builtin_amdgcn_sched_barrier(0); }
          },
          [&](auto I, v4u wf) AI { constexpr int i = I.v;
            constexpr int k = i >> 1;
            mfma32p(wf, hf[k % W0], acc[(i & 1) ^ 1]);
            if constexpr ((i & 1) && k + W0 < 28) hf[k % W0] = ldf(rs, lane, hoff + ((k + W0) / 7) * 14336, (k + W0) % 7);
          });
      TS(30 + 32 * L);
      if constexpr (!PAR) { if (L == 0) { DMA(wa, 0); DMA(wa + SLOT, 1); } }
      res_epilogue<true>(acc, res, lds, rs, ws, out, w, lane, g, m, ttB, ntB, 2, active, L == 1, 4 * L + 4);
      TS(31 + 32 * L);
    };
    if constexpr (QR) {
      BAR();
      DMAT(wc + 3 * SLOT, 8); DMAT(wc + 4 * SLOT, 9);
      if (L == 0) {
#pragma unroll
        for (int i = 0; i < 14; ++i) { wr[14 + i] = __builtin_amdgcn_raw_buffer_load_b128(rs, l16, wd + 7 * SLOT + (kh * 14 + (i & ~1) + ((i & 1) ^ kh ^ 1)) * 1024, AUX_SC1); __builtin_amdgcn_sched_barrier(0); }
        VMWAIT(14);
      } else VMWAIT(0);
      BAR();
      DMA(wc + 5 * SLOT, 10); DMA(wc + 6 * SLOT, 11); DMA(wd, 12);
      if (L == 0) {
#pragma unroll
        for (int i = 0; i < 14; ++i) { wr[i] = __builtin_amdgcn_raw_buffer_load_b128(rs, l16, wd + 5 * SLOT + (kh * 14 + (i & ~1) + ((i & 1) ^ kh ^ 1)) * 1024, AUX_SC1); __builtin_amdgcn_sched_barrier(0); }
        VMWAIT(21);
      } else VMWAIT(7);
      BAR();
      DMA(wd + SLOT, 13); DMA(wd + 2 * SLOT, 14);
      BAR();
      DMA(wd + 3 * SLOT, 15);
      VMWAIT(7);
      __builtin_amdgcn_s_setprio(QPRIO);
      if (active) phase_d(IC<1>{}); else { VMWAIT(0); BAR(); }
      __builtin_amdgcn_s_setprio(0);
      if (L == 0) DMAT(wa + 2 * SLOT, 2);
    } else {
      constexpr int W0 = 8;
      v4u xf[28]; v2u st[7];
      const int xoff = OFF_X + (2 * L + 1) * XBUF + ttA * 28 * 1024, soff = OFF_ST + (2 * L + 1) * SBUF + ttA * 14 * 256;
      BAR();
      TS(16 + 32 * L);
      const int hoff = OFF_H + L * HBUF + (ttA * 112 + 7 * m) * 1024;
      LAS char* cs = lds + L_CS + 768 + t * 8;
      f32x16 a[4] = {{}, {}, {}, {}};
      float mean, rstd, c1, kk; v4u xa; float hv[8], ev = 0.f, tv = 0.f; v2u cw[7];
      auto epi = [&](const f32x16& acc, int e, int tile) AI {
        const int q = e >> 1;
        if (e & 1) {
          if (q > 0) { hv[q - 1] *= ev; OPQ_V(hv[q - 1]); }
          if (q < 8) { ev = __builtin_amdgcn_exp2f(tv); hv[q] = acc[q] * acc[8 + q]; OPQ_V(ev); OPQ_V(hv[q]); }
        } else if (e < 18) {
          if (q > 0) ev = __builtin_fmaf(ev, kk, kk);
          if (q < 8) { tv = acc[q] * c1; OPQ_V(tv); }
          if (q > 0) { ev = __builtin_amdgcn_rcpf(ev); OPQ_V(ev); }
        } else st16(v4u{pk(hv[0], hv[1]), pk(hv[2], hv[3]), pk(hv[4], hv[5]), pk(hv[6], hv[7])}, rs, lane * 16 + (tile & 3) * 1024, hoff + (tile >> 2) * 4096);
      };
      pipe<196, 5>(
          [&](auto I) AI { constexpr int i = I.v;
            if constexpr (i == 84 || i == 140) { TS(17 + 2 * ((i - 84) / 56) + 32 * L); BAR(); TS(18 + 2 * ((i - 84) / 56) + 32 * L); }
            if constexpr (i == 168) BAR();
            constexpr int tile = i < 84 ? i % 3 : i / 28;
            constexpr int ks = i < 84 ? i / 3 : i % 28;
            return i < 196 ? lds16(LSL(5 + tile) + ks * 1024) : v4u{};
          },
          [&]() AI {
            poll(ws, fi_x(g, w, lane), 4 * L + 2);
            TS(14 + 32 * L);
#pragma unroll
            for (int i = 0; i < W0; ++i) { xf[i] = ldf(rs, lane, xoff, i); __builtin_amdgcn_sched_barrier(0); }
          },
          [&](auto I, v4u wf) AI { constexpr int i = I.v;
            constexpr int tile = i < 84 ? i % 3 : i / 28;
            constexpr int ks = i < 84 ? i / 3 : i % 28;
            if constexpr (i >= 84 && ks == 0) { a[tile & 3] = f32x16{}; mfma32p(aug_w(cw[tile]), xa, a[tile & 3]); }
            mfma32p(wf, xf[ks], a[tile & 3]);
            if constexpr (i < 84 && i % 3 == 0 && i / 3 + W0 < 28) xf[i / 3 + W0] = ldf(rs, lane, xoff, i / 3 + W0);
            if constexpr (i % 3 == 1 && i / 3 < 7) st[i / 3] = ld8(rs, ((7 * h + i / 3) * 32 + t) * 8, soff);
            if constexpr (i >= 70 && i < 73) cw[i - 70] = *(LAS v2u*)(cs + (i - 70) * 256);
            if constexpr (i >= 78 && i % 28 == 22 && i < 170) cw[i / 28 + 1] = *(LAS v2u*)(cs + (i / 28 + 1) * 256);
            if constexpr (i == 62) { fin_stats(st, mean, rstd); OPQ_V(mean); OPQ_V(rstd); }
            if constexpr (i == 66) { xa = aug_x(-mean, h == 0); c1 = rstd * -1.4426950408889634f; kk = __builtin_amdgcn_rcpf(rstd * rstd); OPQ_V(xa); OPQ_V(c1); OPQ_V(kk); }
            if constexpr (i >= 75 && i < 78) mfma32p(aug_w(cw[i - 75]), xa, a[i - 75]);
            if constexpr (i >= 83) epi(a[((i - 83) / 19) & 3], (i - 83) % 19, (i - 83) / 19);
          });
      TS(21 + 32 * L);
      epi(a[1], 18, 5);
      st16(swiglu(a[2], rstd), rs, lane * 16 + 2048, hoff + 4096);
      TS(23 + 32 * L);
      publish(ws, myflag, 4 * L + 3, lane);
      TS(24 + 32 * L);
      if (active) phase_d(IC<0>{}); else { BAR(); if (L == 0) { DMA(wa, 0); DMA(wa + SLOT, 1); } }
    }
  }
  };
  if (w >= 4) run(IC<1>{}); else run(IC<0>{});
}

extern "C" void kernel_launch(void* const* d_in, const int* in_sizes, int n_in, void* d_out, int out_size, void* d_ws,
                              size_t ws_size, hipStream_t stream) {
  (void)in_sizes; (void)n_in; (void)out_size; (void)ws_size;
  (void)hipFuncSetAttribute((const void*)k_main, hipFuncAttributeMaxDynamicSharedMemorySize, LDS_BYTES);
  k_main<<<256, 512, LDS_BYTES, stream>>>((const char*)d_in[0], (const char*)d_in[1], (const char*)d_in[2], (const char*)d_in[4],
                                          (const char*)d_in[5], (const char*)d_in[6], (char*)d_ws, (unsigned short*)d_out);
}
```

```cpp
#include <hip/hip_runtime.h>
#include <hip/hip_bf16.h>

typedef unsigned v4u __attribute__((ext_vector_type(4)));
typedef unsigned v2u __attribute__((ext_vector_type(2)));
typedef float f32x4 __attribute__((ext_vector_type(4)));
typedef float f32x2 __attribute__((ext_vector_type(2)));
typedef float f32x16 __attribute__((ext_vector_type(16)));
typedef __bf16 bf16x8 __attribute__((ext_vector_type(8)));
typedef __bf16 bf16x2 __attribute__((ext_vector_type(2)));
#define LAS __attribute__((address_space(3)))
#define GAS __attribute__((address_space(1)))
#define DEV __device__ __forceinline__
#define AI __attribute__((always_inline))

constexpr int S_ = 2048, D_ = 448, F_ = 1792;
constexpr int OFF_FLAG = 0;
constexpr int OFF_PF = 65536, OFF_EP = OFF_PF + 65536;
constexpr int OFF_WA = 135168;
constexpr int OFF_WB = OFF_WA + 16 * 84 * 1024;
constexpr int OFF_WC = OFF_WB + 7 * 64 * 1024;
constexpr int OFF_WD = OFF_WC + 16 * 196 * 1024;
constexpr int OFF_CS = OFF_WD + 7 * 224 * 1024;
constexpr int XBUF = 64 * 28 * 1024, SBUF = 64 * 14 * 256, OBUF = 64 * 32 * 1024, HBUF = 64 * 112 * 1024;
constexpr int OFF_X = OFF_CS + 16 * 3072;
constexpr int OFF_ST = OFF_X + 4 * XBUF;
constexpr int OFF_O = OFF_ST + 4 * SBUF;
constexpr int OFF_H = OFF_O + 2 * OBUF;
constexpr int WS_BYTES = OFF_H + 2 * HBUF;
constexpr int SLOT = 28 * 1024;
constexpr int L_EX = 5 * SLOT, L_CS = L_EX + 16384, L_RS = L_CS + 3072, LDS_BYTES = L_RS + 512;
static_assert(LDS_BYTES <= 163840, "lds");
constexpr int AUX_SC1 = 16;

DEV unsigned pk(float a, float b) { bf16x2 t = {(__bf16)a, (__bf16)b}; return __builtin_bit_cast(unsigned, t); }
DEV float asf(unsigned u) { return __builtin_bit_cast(float, u); }
DEV float blo(unsigned u) { return __builtin_bit_cast(float, u << 16); }
DEV float bhi(unsigned u) { return __builtin_bit_cast(float, u & 0xffff0000u); }
DEV f32x16 mfma32(v4u a, v4u b, f32x16 c) {
  return __builtin_amdgcn_mfma_f32_32x32x16_bf16(__builtin_bit_cast(bf16x8, a), __builtin_bit_cast(bf16x8, b), c, 0, 0, 0);
}
DEV void mfma32p(v4u a, v4u b, f32x16& c) { c = mfma32(a, b, c); asm volatile("" : "+v"(c)); __builtin_amdgcn_sched_barrier(0); }
DEV f32x4 mfma16(v4u a, v4u b, f32x4 c) {
  return __builtin_amdgcn_mfma_f32_16x16x32_bf16(__builtin_bit_cast(bf16x8, a), __builtin_bit_cast(bf16x8, b), c, 0, 0, 0);
}
DEV float xsum32(float v) {
  auto r = __builtin_amdgcn_permlane32_swap(__builtin_bit_cast(unsigned, v), __builtin_bit_cast(unsigned, v), false, false);
  return __builtin_bit_cast(float, (unsigned)r[0]) + __builtin_bit_cast(float, (unsigned)r[1]);
}
DEV float xmax32(float v) {
  auto r = __builtin_amdgcn_permlane32_swap(__builtin_bit_cast(unsigned, v), __builtin_bit_cast(unsigned, v), false, false);
  return fmaxf(__builtin_bit_cast(float, (unsigned)r[0]), __builtin_bit_cast(float, (unsigned)r[1]));
}
DEV void split3(float v, unsigned& hi, unsigned& mid, unsigned& lo) {
  hi = __builtin_bit_cast(unsigned, v) & 0xffff0000u;
  float r = v - __builtin_bit_cast(float, hi);
  mid = __builtin_bit_cast(unsigned, r) & 0xffff0000u;
  float r2 = r - __builtin_bit_cast(float, mid);
  lo = __builtin_bit_cast(unsigned, r2) & 0xffff0000u;
}
DEV v4u aug_x(float negmean, bool on) {
  unsigned hi, mid, lo; split3(negmean, hi, mid, lo);
  v4u r = {hi | (hi >> 16), mid | (mid >> 16), lo | (hi >> 16), 0u};
  if (!on) r = v4u{0u, 0u, 0u, 0u};
  return r;
}
DEV v4u aug_w(v2u c) { return v4u{c.x, c.x, c.y, 0u}; }

typedef short s16x4 __attribute__((ext_vector_type(4)));
typedef __amdgpu_buffer_rsrc_t rsrc_t;
#define BAR() asm volatile("s_waitcnt lgkmcnt(0)\n\ts_barrier" ::: "memory")
#define VMWAIT(n) asm volatile("s_waitcnt vmcnt(" #n ")" ::: "memory")
DEV v4u ld16(rsrc_t rs, int voff, int soff) { return __builtin_amdgcn_raw_buffer_load_b128(rs, voff, soff, AUX_SC1); }
DEV v2u ld8(rsrc_t rs, int voff, int soff) { return __builtin_amdgcn_raw_buffer_load_b64(rs, voff, soff, AUX_SC1); }
DEV void st16(v4u v, rsrc_t rs, int voff, int soff) { __builtin_amdgcn_raw_buffer_store_b128(v, rs, voff + soff, 0, AUX_SC1); }
DEV void st8(v2u v, rsrc_t rs, int voff, int soff) { __builtin_amdgcn_raw_buffer_store_b64(v, rs, voff, soff, AUX_SC1); }
DEV v4u ldf(rsrc_t rs, int lane, int base, int i) { return ld16(rs, lane * 16 + (i & 3) * 1024, base + (i >> 2) * 4096); }
DEV v4u lds16(LAS char* p) { return *(LAS v4u*)p; }

DEV v2u tr8(LAS char* p) { return __builtin_bit_cast(v2u, __builtin_amdgcn_ds_read_tr16_b64_v4i16((LAS s16x4*)p)); }
DEV void pack_unit(int u, int tid, LAS char* sm, rsrc_t rs, const char* x, const char* wqkv, const char* wo, const char* wg,
                   const char* wu, const char* wd) {
  const int w = __builtin_amdgcn_readfirstlane(tid >> 6), lane = tid & 63, n = lane & 31, h = lane >> 5;
  if (u < 0) { BAR(); return; }
  if (u < 77) {
    const char* src; int ld;
    if (u < 21) { src = wqkv + u * 128; ld = 2688; } else if (u < 49) { src = wg + (u - 21) * 128; ld = 3584; } else { src = wu + (u - 49) * 128; ld = 3584; }
    src += (tid >> 3) * ld + (tid & 7) * 16;
    v4u v[14];
#pragma unroll
    for (int i = 0; i < 14; ++i) v[i] = *(const v4u*)(src + i * 32 * ld);
    float c[8] = {0.f, 0.f, 0.f, 0.f, 0.f, 0.f, 0.f, 0.f};
#pragma unroll
    for (int i = 0; i < 14; ++i)
#pragma unroll
      for (int q = 0; q < 4; ++q) { const unsigned e = v[i][q]; c[2 * q] += blo(e); c[2 * q + 1] += bhi(e); }
    *(LAS f32x4*)(sm + (tid >> 3) * 272 + (tid & 7) * 32) = f32x4{c[0], c[1], c[2], c[3]};
    *(LAS f32x4*)(sm + (tid >> 3) * 272 + (tid & 7) * 32 + 16) = f32x4{c[4], c[5], c[6], c[7]};
    BAR();
    if (tid < 64) {
      float cs = 0.f;
#pragma unroll
      for (int i = 0; i < 32; ++i) cs += *(LAS float*)(sm + i * 272 + tid * 4);
      unsigned hi, mid, lo; split3(cs, hi, mid, lo);
      int o;
      if (u < 21) { const int c_ = u * 64 + tid, t = c_ / 448, r = c_ % 448; o = (r / 28) * 3072 + t * 256 + (r % 28) * 8;
        if (r % 28 < 4) st8(v2u{0u, 0u}, rs, OFF_CS + o + 28 * 8, 0); }
      else { const int f = ((u - 21) % 28) * 64 + tid; o = (f / 112) * 3072 + (3 + (f % 112) / 16) * 256 + ((f & 15) + (u >= 49 ? 16 : 0)) * 8; }
      st8(v2u{(hi >> 16) | mid, (lo >> 16) | hi}, rs, OFF_CS + o, 0);
    }
    return;
  }
  const bool isx = u >= 119 && u < 183;
  const int lstride = isx ? 912 : 960;
  int dst0 = 0, dstep = 0, cstep = 128, col0 = 64 * (w >> 1), row0 = 16 * (w & 1);
  {
    const char* src; int ld = 896;
    if (u < 119) { const int q = u - 77; ld = 2688; src = wqkv + ((q / 3) * 32 + 8 * w) * 2688 + (q % 3) * 896;
      dst0 = OFF_WA + (((w >> 1) * 3 + q % 3) * 28 + 2 * (q / 3) + (w & 1)) * 1024; dstep = 168 * 1024; cstep = 112; col0 = 56 * (w >> 1); }
    else if (u < 183) { const int tt = u - 119, g = tt >> 2; ld = 1792; src = x + (2 * (128 * (g & 7) + 32 * (tt & 3) + 8 * w) + (g >> 3)) * 896; }
    else if (u < 199) { const int s = 2 * (u - 183) + (w & 1), kh = s >> 4, ks = s & 15, c = ks / 7; src = wo + ((u - 183) * 28 + 8 * w) * 896;
      dst0 = OFF_WB + (c * 28 + kh * (c < 2 ? 14 : 4) + (ks % 7) * 2 + (w >> 1)) * 1024; dstep = 64 * 1024; }
    else if (u < 311) { const int q = u - 199; ld = 3584; src = (w < 2 ? wg : wu) + ((q >> 2) * 16 + 8 * (w & 1)) * 3584 + (q & 3) * 896;
      dst0 = OFF_WC + (((q & 3) * 28 + w) * 28 + (q >> 2)) * 1024; dstep = 112 * 1024; col0 = 32 * w; }
    else { const int s = 2 * (u - 311) + (w & 1), kh = s / 56, ks = s % 56; src = wd + ((u - 311) * 32 + 8 * w) * 896;
      dst0 = OFF_WD + ((ks / 7) * 28 + kh * 14 + (ks % 7) * 2 + (w >> 1)) * 1024; dstep = 224 * 1024; }
    const bool pad = u >= 183 && u < 199 && w == 3;
    if (lane < 56) {
      v4u v[8];
#pragma unroll
      for (int i = 0; i < 8; ++i) v[i] = *(const v4u*)(src + ((pad && i >= 4) ? 0 : i * ld) + lane * 16);
#pragma unroll
      for (int i = 0; i < 8; ++i) *(LAS v4u*)(sm + (8 * w + i) * lstride + lane * 16) = (pad && i >= 4) ? v4u{0u, 0u, 0u, 0u} : v[i];
    }
  }
  BAR();
  if (isx) {
    const int tt = u - 119;
#pragma unroll
    for (int it = 0; it < 4; ++it) {
      const int nt = w + 4 * it;
      if (nt >= 14) break;
      v2u a[4];
#pragma unroll
      for (int i = 0; i < 4; ++i) a[i] = *(LAS v2u*)(sm + n * 912 + nt * 64 + i * 16 + h * 8);
      __builtin_amdgcn_raw_buffer_store_b128(v4u{a[0].x, a[0].y, a[1].x, a[1].y}, rs, OFF_X + (tt * 28 + 2 * nt) * 1024 + lane * 16, 0, AUX_SC1);
      __builtin_amdgcn_raw_buffer_store_b128(v4u{a[2].x, a[2].y, a[3].x, a[3].y}, rs, OFF_X + (tt * 28 + 2 * nt + 1) * 1024 + lane * 16, 0, AUX_SC1);
      float sm1 = 0.f;
#pragma unroll
      for (int i = 0; i < 4; ++i) sm1 += blo(a[i].x) + bhi(a[i].x) + blo(a[i].y) + bhi(a[i].y);
      sm1 = xsum32(sm1);
      const float mu = sm1 * (1.f / 32.f);
      float m2 = 0.f;
#pragma unroll
      for (int i = 0; i < 4; ++i) {
        const float d0 = blo(a[i].x) - mu, d1 = bhi(a[i].x) - mu, d2 = blo(a[i].y) - mu, d3 = bhi(a[i].y) - mu;
        m2 += d0 * d0 + d1 * d1 + d2 * d2 + d3 * d3;
      }
      m2 = xsum32(m2);
      if (h == 0) st8(v2u{__builtin_bit_cast(unsigned, sm1), __builtin_bit_cast(unsigned, m2)}, rs, OFF_ST + (tt * 14 + nt) * 256 + n * 8, 0);
    }
    return;
  }
  const int q4 = (lane & 15) >> 2, p4 = lane & 3, cb = (lane >> 4) & 1;
  const bool gu = u >= 199 && u < 311;
  LAS char* pa = sm + ((gu ? 16 * cb : row0) + 4 * h + q4) * 960 + col0 + (gu ? 0 : 32 * cb) + 8 * p4;
  v2u lo[8], hi[8];
#pragma unroll
  for (int i = 0; i < 8; ++i) { LAS char* q = pa + ((i < 7 || u < 119) ? i * cstep : 0); lo[i] = tr8(q); hi[i] = tr8(q + 8 * 960); }
  const bool z = u < 119 && n >= 28;
#pragma unroll
  for (int i = 0; i < 8; ++i)
    if (i < 7 || u < 119) st16(z ? v4u{0u, 0u, 0u, 0u} : v4u{lo[i].x, lo[i].y, hi[i].x, hi[i].y}, rs, lane * 16, dst0 + i * dstep);
}

DEV void poll(char* ws, int fi, unsigned want) {
  GAS unsigned* f = (GAS unsigned*)(ws + OFF_FLAG) + fi * 16;
  for (int spin = 0; spin < (1 << 20); ++spin) {
    const unsigned v = __hip_atomic_load(f, __ATOMIC_RELAXED, __HIP_MEMORY_SCOPE_AGENT);
    if (__all(v >= want)) break;
  }
  __builtin_amdgcn_fence(__ATOMIC_ACQUIRE, "wavefront");
}
DEV int fi_x(int g, int T, int lane) { const int i = lane < 14 ? lane : 0; return (g * 16 + 8 * (T >> 1) + (i >> 1)) * 4 + (T & 1) + 2 * (i & 1); }
DEV int fi_k(int g, int kh, int T, int lane) { return (g * 16 + 8 * kh + (lane & 7)) * 4 + T; }
DEV void publish(char* ws, int fi, unsigned val, int lane) {
  VMWAIT(0);
  if (lane == 0) __hip_atomic_store((GAS unsigned*)(ws + OFF_FLAG) + fi * 16, val, __ATOMIC_RELAXED, __HIP_MEMORY_SCOPE_AGENT);
}
template <int N, int TH> DEV void dma(rsrc_t rs, int src, int dst, int hw, int lane16) {
#pragma unroll
  for (int i = 0; i < N; ++i) {
    unsigned keep;
    asm volatile("s_mov_b32 %0, m0\n\ts_mov_b32 m0, %1\n\ts_nop 0\n\tbuffer_load_dwordx4 %2, %3, %4 offen sc1 lds\n\ts_mov_b32 m0, %0"
                 : "=&s"(keep) : "s"(dst + (hw * N + i) * 1024), "v"(lane16), "s"(rs), "s"(src + (hw * N + i) * 1024) : "memory");
    if constexpr (TH >= 0) asm volatile("s_waitcnt vmcnt(%0)" :: "i"(TH) : "memory");
  }
}
#define THR 4
#define DQ 42
#define QPRIO 1
#define QDELAY 75
#define DMA(src, c) dma<7, -1>(rs, src, slot_of(c), hw, l16)
#define DMAT(src, c) dma<7, THR>(rs, src, slot_of(c), hw, l16)
#define OPQ_S(x) asm volatile("" : "+s"(x))
#define OPQ_V(x) asm volatile("" : "+v"(x))
template <int I> struct IC { static constexpr int v = I; };
template <int I, int N, class F> DEV void sfor(F& f) { if constexpr (I < N) { f(IC<I>{}); sfor<I + 1, N>(f); } }
template <int N, int PF, class RD, class MID, class ST> DEV void pipe(RD rd, MID mid, ST st) {
  constexpr int R = PF + 1;
  v4u wb[R];
  auto pro = [&](auto I) __attribute__((always_inline)) { wb[I.v] = rd(I); };
  sfor<0, PF>(pro);
  mid();
  __builtin_amdgcn_sched_barrier(0);
  auto body = [&](auto I) __attribute__((always_inline)) {
    constexpr int i = I.v;
    st(I, wb[i % R]);
    if constexpr (i + PF < N) wb[(i + PF) % R] = rd(IC<i + PF>{});
    asm volatile("" :: "v"(wb[i % R]));
    __builtin_amdgcn_sched_barrier(0);
  };
  sfor<0, N>(body);
}
constexpr int slot_of(int c) { return (c % 5) * SLOT; }
DEV void fin_stats(const v2u (&st)[7], float& mean, float& rstd) {
  float sm = 0.f;
#pragma unroll
  for (int i = 0; i < 7; ++i) sm += asf(st[i].x);
  mean = xsum32(sm) * (1.f / D_);
  float m2 = 0.f;
#pragma unroll
  for (int i = 0; i < 7; ++i) {
    const float d = asf(st[i].x) * (1.f / 32.f) - mean;
    m2 += asf(st[i].y) + 32.f * d * d;
  }
  rstd = __builtin_amdgcn_rsqf(xsum32(m2) * (1.f / D_) + 1e-5f);
}
DEV v4u pack8(const f32x16& a, int o, float sc) {
  return v4u{pk(a[o] * sc, a[o + 1] * sc), pk(a[o + 2] * sc, a[o + 3] * sc), pk(a[o + 4] * sc, a[o + 5] * sc), pk(a[o + 6] * sc, a[o + 7] * sc)};
}
DEV v4u swiglu(const f32x16& a, float rstd) {
  float hv[8];
#pragma unroll
  for (int i = 0; i < 8; ++i) {
    const float g = a[i] * rstd, up = a[8 + i] * rstd;
    const float e = __builtin_amdgcn_exp2f(g * -1.4426950408889634f);
    hv[i] = g * up * __builtin_amdgcn_rcpf(1.f + e);
  }
  return v4u{pk(hv[0], hv[1]), pk(hv[2], hv[3]), pk(hv[4], hv[5]), pk(hv[6], hv[7])};
}
#define TS(i)

template <bool W8> DEV void res_epilogue(f32x16 (&acc)[2], v4u (&res)[2], LAS char* lds, rsrc_t rs, char* ws, unsigned short* out, int w, int lane,
                      int g, int m, int tt, int nt, int xb, bool active, bool last, unsigned flagval) {
  LAS char* sx = lds + (W8 ? 3 * SLOT : L_EX) + lane * 16;
  float v[16];
#pragma unroll
  for (int q = 0; q < 8; ++q) { const unsigned r = res[q >> 2][q & 3]; v[2 * q] = acc[0][2 * q] + blo(r); v[2 * q + 1] = acc[0][2 * q + 1] + bhi(r); }
#pragma unroll
  for (int q = 0; q < 4; ++q) *(LAS f32x4*)(sx + w * 4096 + q * 1024) = f32x4{acc[1][4 * q], acc[1][4 * q + 1], acc[1][4 * q + 2], acc[1][4 * q + 3]};
  if (W8 && w >= 4) {
#pragma unroll
    for (int q = 0; q < 4; ++q) *(LAS f32x4*)(sx + (w + 4) * 4096 + q * 1024) = f32x4{acc[0][4 * q], acc[0][4 * q + 1], acc[0][4 * q + 2], acc[0][4 * q + 3]};
  }
  BAR();
  if (W8 && w >= 4) return;
#pragma unroll
  for (int q = 0; q < 4; ++q) {
    f32x4 p0 = *(LAS f32x4*)(sx + (w ^ 2) * 4096 + q * 1024);
    if (W8) p0 += *(LAS f32x4*)(sx + (w ^ 6) * 4096 + q * 1024) + *(LAS f32x4*)(sx + (w + 8) * 4096 + q * 1024);
#pragma unroll
    for (int e = 0; e < 4; ++e) v[4 * q + e] += p0[e];
  }
#pragma unroll
  for (int q = 0; q < 8; ++q) res[q >> 2][q & 3] = pk(v[2 * q], v[2 * q + 1]);
  if (!active) return;
  if (last) {
    const int s = 2 * (128 * (g & 7) + 32 * (tt & 3) + (lane & 31)) + (g >> 3);
    unsigned short* rowp = out + (size_t)s * D_ + 32 * nt + 8 * (lane >> 5);
#pragma unroll
    for (int q = 0; q < 2; ++q) {
      auto a = __builtin_amdgcn_permlane32_swap(res[q][0], res[q][2], false, false);
      auto b = __builtin_amdgcn_permlane32_swap(res[q][1], res[q][3], false, false);
      *(v4u*)(rowp + 16 * q) = v4u{(unsigned)a[0], (unsigned)b[0], (unsigned)a[1], (unsigned)b[1]};
    }
    return;
  }
  st16(res[0], rs, lane * 16, OFF_X + xb * XBUF + (tt * 28 + 2 * nt) * 1024);
  st16(res[1], rs, lane * 16 + 1024, OFF_X + xb * XBUF + (tt * 28 + 2 * nt) * 1024);
  __builtin_amdgcn_sched_barrier(0);
  float s0 = 0.f, s1 = 0.f, q0 = 0.f, q1 = 0.f;
#pragma unroll
  for (int q = 0; q < 8; ++q) { s0 += v[2 * q]; s1 += v[2 * q + 1]; q0 += v[2 * q] * v[2 * q]; q1 += v[2 * q + 1] * v[2 * q + 1]; }
  const float sm = xsum32(s0 + s1), sq = xsum32(q0 + q1);
  const float m2 = fmaxf(sq - sm * sm * (1.f / 32.f), 0.f);
  if (lane < 32) st8(v2u{__builtin_bit_cast(unsigned, sm), __builtin_bit_cast(unsigned, m2)}, rs, lane * 8, OFF_ST + xb * SBUF + (tt * 14 + nt) * 256);
  publish(ws, (g * 16 + m) * 4 + w, flagval, lane);
}

__global__ __launch_bounds__(512, 2) void k_main(const char* x, const char* wqkv, const char* wo, const char* wg_, const char* wu_,
                                                  const char* wd_, char* ws, unsigned short* out) {
  extern __shared__ __attribute__((aligned(16))) char smem[];
  LAS char* lds = (LAS char*)smem;
  unsigned code_touch;
  {
    unsigned long long pc;
    asm volatile("s_getpc_b64 %0" : "=s"(pc));
    code_touch = *(const GAS unsigned*)((pc & ~127ull) + (threadIdx.x < 320u ? threadIdx.x : 0u) * 128u);
    __builtin_amdgcn_sched_barrier(0);
  }
  const int tid = threadIdx.x; int lane = tid & 63;
  int w = __builtin_amdgcn_readfirstlane(tid >> 6);
  const int b = blockIdx.x, xcd = b & 7, idx = b >> 3;
  int g = (xcd >> 1) * 4 + (idx >> 3), m = (xcd & 1) * 8 + (idx & 7);
  const int half = m >> 3, j = (m & 7) < 7 ? (m & 7) : 6;
  const bool active = (m & 7) < 7;
  const rsrc_t rs = __builtin_amdgcn_make_buffer_rsrc((void*)ws, 0, WS_BYTES, 0x00020000);
  TS(0);
  const unsigned tag = __hip_atomic_load((GAS unsigned*)(ws + OFF_EP), __ATOMIC_RELAXED, __HIP_MEMORY_SCOPE_AGENT) + 1u;
  auto poll_setup = [&](int base, auto NC) AI {
    for (int spin = 0; spin < (1 << 20); ++spin) {
      bool ok = true;
#pragma unroll
      for (int i = 0; i < NC.v; ++i) ok &= __builtin_amdgcn_raw_buffer_load_b32(rs, lane * 128, base + i * 8192, AUX_SC1) == tag;
      if (__all(ok)) break;
      asm volatile("s_sleep 2" ::: "memory");
    }
    __builtin_amdgcn_fence(__ATOMIC_ACQUIRE, "wavefront");
  };
  v4u res[2];
  if (w < 4) {
    if (lane == 0) __hip_atomic_store((GAS unsigned*)(ws + OFF_FLAG) + ((g * 16 + m) * 4 + w) * 16, 0u, __ATOMIC_RELAXED, __HIP_MEMORY_SCOPE_AGENT);
    LAS unsigned* cnt = (LAS unsigned*)(lds + L_RS);
    if (tid == 0) *cnt = 0u;
    pack_unit(b < 21 ? b : b < 127 ? b + 56 : -1, tid, lds, rs, x, wqkv, wo, wg_, wu_, wd_);
    VMWAIT(0);
    if (lane == 0 && __hip_atomic_fetch_add(cnt, 1u, __ATOMIC_RELAXED, __HIP_MEMORY_SCOPE_WORKGROUP) == 3u)
      __hip_atomic_store((GAS unsigned*)(ws + OFF_PF) + b * 32, tag, __ATOMIC_RELAXED, __HIP_MEMORY_SCOPE_AGENT);
    TS(2);
    BAR();
    if (w == 0) poll_setup(OFF_PF, IC<2>{});
    BAR();
    res[0] = ld16(rs, lane * 16, OFF_X + ((g * 4 + 2 * half + (w & 1)) * 28 + 2 * (2 * j + (w >> 1))) * 1024);
    res[1] = ld16(rs, lane * 16 + 1024, OFF_X + ((g * 4 + 2 * half + (w & 1)) * 28 + 2 * (2 * j + (w >> 1))) * 1024);
  } else {
    res[0] = res[1] = v4u{0u, 0u, 0u, 0u};
    BAR();
    if (b >= 127) __builtin_amdgcn_s_sleep(QDELAY);
    pack_unit(b < 16 ? -1 : b < 200 ? 167 + b : b - 179, tid & 255, lds + 3 * SLOT, rs, x, wqkv, wo, wg_, wu_, wd_);
    VMWAIT(0);
    BAR();
    if (tid == 256) __hip_atomic_store((GAS unsigned*)(ws + OFF_PF + 32768) + b * 32, tag, __ATOMIC_RELAXED, __HIP_MEMORY_SCOPE_AGENT);
    {
      const int hw = w - 4, l16 = lane * 16, wa = OFF_WA + m * 84 * 1024;
      dma<1, -1>(rs, OFF_CS + m * 3072, L_CS, 0, l16);
      DMA(wa, 0); DMA(wa + SLOT, 1); DMA(wa + 2 * SLOT, 2);
    }
  }
  asm volatile("" :: "v"(code_touch));
  auto run = [&](auto QRC) AI {
  constexpr bool QR = QRC.v;
  v4u wr[28];
  if constexpr (QR) {
#pragma unroll
    for (int i = 0; i < 28; ++i) wr[i] = v4u{0u, 0u, 0u, 0u};
  }
  for (int L = 0; L < 2; ++L) {
    OPQ_S(w); OPQ_S(g); OPQ_S(m); OPQ_V(lane);
    const int t = lane & 31, h = lane >> 5;
    const int mw = w & 1, kh = (w >> 1) & 1, par = w >> 2, hw = w & 3, l16 = lane * 16;
    const int wa = OFF_WA + m * 84 * 1024, wb = OFF_WB + j * 64 * 1024, wc = OFF_WC + m * 196 * 1024, wd = OFF_WD + j * 224 * 1024;
    const int ttA = g * 4 + w;
    const int ttB = g * 4 + 2 * half + mw;
    const int ntB = 2 * j + kh;
    const int myflag = (g * 16 + m) * 4 + w;
    LAS char* lb[3] = {lds + lane * 16, lds + 2 * SLOT + lane * 16, lds + 4 * SLOT + lane * 16};
    OPQ_V(lb[1]); OPQ_V(lb[2]);
#define LSL(c) (lb[((c) % 5) >> 1] + (((c) % 5) & 1) * SLOT)
    if constexpr (QR) {
      {
        const bool hasprev = (g & 7) > 0;
        const int gp = hasprev ? g - 1 : g, ttp = gp * 4 + 3;
        VMWAIT(0);
        BAR();
        if (L) poll(ws, fi_x(gp, 3, lane), 4);
        const int t16 = lane & 15, gq = lane >> 4, tk = 24 + (t16 & 7), isv = hw >> 1, nh = hw & 1;
        v4u xf[14]; v2u st[4];
#pragma unroll
        for (int i = 0; i < 4; ++i) st[i] = ld8(rs, ((gq + 4 * i < 14 ? gq + 4 * i : 0) * 32 + tk) * 8, OFF_ST + 2 * L * SBUF + ttp * 14 * 256);
#pragma unroll
        for (int i = 0; i < 14; ++i) xf[i] = ld16(rs, (gq >> 1) * 1024 + ((gq & 1) * 32 + tk) * 16, OFF_X + 2 * L * XBUF + (ttp * 28 + 2 * i) * 1024);
        if (L) { DMA(wb, 3); DMA(wb + SLOT, 4); }
        float sm = 0.f;
#pragma unroll
        for (int i = 0; i < 4; ++i) sm += (gq + 4 * i < 14) ? asf(st[i].x) : 0.f;
        sm += __shfl_xor(sm, 16); sm = xsum32(sm);
        const float mean = sm * (1.f / D_);
        float m2 = 0.f;
#pragma unroll
        for (int i = 0; i < 4; ++i) {
          const float d = asf(st[i].x) * (1.f / 32.f) - mean;
          m2 += (gq + 4 * i < 14) ? asf(st[i].y) + 32.f * d * d : 0.f;
        }
        m2 += __shfl_xor(m2, 16); m2 = xsum32(m2);
        const float rstd = __builtin_amdgcn_rsqf(m2 * (1.f / D_) + 1e-5f);
        LAS char* wp = lds + (1 + isv) * SLOT + (gq >> 1) * 1024 + ((gq & 1) * 32 + 16 * nh + t16) * 16;
        const v4u xa = aug_x(-mean, gq == 0), wg_ = aug_w(*(LAS v2u*)(lds + L_CS + (1 + isv) * 256 + (16 * nh + t16) * 8));
        f32x4 a = {0.f, 0.f, 0.f, 0.f};
        if (isv) {
#pragma unroll
          for (int i = 0; i < 14; ++i) a = mfma16(xf[i], lds16(wp + i * 2048), a);
          a = mfma16(xa, wg_, a);
          float r[4];
#pragma unroll
          for (int i = 0; i < 4; ++i) r[i] = __shfl(rstd, 4 * gq + i);
          *(LAS v2u*)(lds + L_EX + 2048 + (16 * nh + t16 + 32 * (gq & 1)) * 16 + 8 * (gq >> 1)) = v2u{pk(a[0] * r[0], a[1] * r[1]), pk(a[2] * r[2], a[3] * r[3])};
        } else {
#pragma unroll
          for (int i = 0; i < 14; ++i) a = mfma16(lds16(wp + i * 2048), xf[i], a);
          a = mfma16(wg_, xa, a);
          *(LAS v2u*)(lds + L_EX + nh * 1024 + (16 + t16 + 32 * (gq & 1)) * 16 + 8 * (gq >> 1)) = v2u{pk(a[0] * rstd, a[1] * rstd), pk(a[2] * rstd, a[3] * rstd)};
        }
      }
      if (L == 0) {
        poll_setup(OFF_PF + 32768 + hw * 8192, IC<1>{});
        BAR();
        if (b == 0 && tid == 256) __hip_atomic_store((GAS unsigned*)(ws + OFF_EP), tag + 1u, __ATOMIC_RELAXED, __HIP_MEMORY_SCOPE_AGENT);
        dma<1, -1>(rs, OFF_CS + m * 3072, L_CS, hw < 3 ? hw : 2, l16);
        DMA(wb, 3); DMA(wb + SLOT, 4);
      }
      BAR();
      DMA(wc, 5); DMA(wc + SLOT, 6);
      VMWAIT(14);
    } else {
      constexpr int W0 = 8;
      v4u xf[28]; v2u st[7];
      const int xoff = OFF_X + 2 * L * XBUF + ttA * 28 * 1024, soff = OFF_ST + 2 * L * SBUF + ttA * 14 * 256;
      if (L) poll(ws, fi_x(g, w, lane), 4);
      TS(3 + 32 * L);
#pragma unroll
      for (int i = 0; i < W0; ++i) { xf[i] = ldf(rs, lane, xoff, i); __builtin_amdgcn_sched_barrier(0); }
      BAR();
      TS(1 + 32 * L);
      f32x16 aq = {}, ak = {}, av = {};
      float mean, rstd; v4u xa; v2u cw[3]; f32x4 rv[4];
      float bias[20];
      const bool hasprev = w > 0 || (g & 7) > 0;
      const float NEG = -__builtin_inff();
      pipe<84, 6>([&](auto I) AI { constexpr int i = I.v; return i < 84 ? lds16(LSL((i + 1) % 3) + (i / 3) * 1024) : v4u{}; },
                  [&]() AI {},
                  [&](auto I, v4u wf) AI { constexpr int i = I.v;
                    if constexpr (i % 3 == 0) mfma32p(wf, xf[i / 3], ak);
                    else if constexpr (i % 3 == 1) mfma32p(xf[i / 3], wf, av);
                    else mfma32p(wf, xf[i / 3], aq);
                    if constexpr (i % 3 == 0 && i / 3 + W0 < 28) xf[i / 3 + W0] = ldf(rs, lane, xoff, i / 3 + W0);
                    if constexpr (i % 3 == 1 && i / 3 < 7) st[i / 3] = ld8(rs, ((7 * h + i / 3) * 32 + t) * 8, soff);
                    if constexpr (i == 70) { if (L == 0) BAR(); }
                    if constexpr (i >= 2 && i < 62 && i % 3 == 2) {
                      constexpr int r = i / 3;
                      if constexpr (r < 16) bias[r] = ((unsigned)(t - 4 * h - ((r & 3) + 8 * (r >> 2))) <= 7u) ? 0.f : NEG;
                      else bias[r] = (hasprev && r - 17 + 4 * h >= t) ? 0.f : NEG;
                      OPQ_V(bias[r]);
                    }
                    if constexpr (i == 62) { fin_stats(st, mean, rstd); OPQ_V(mean); OPQ_V(rstd); }
                    if constexpr (i == 66) { xa = aug_x(-mean, h == 0); OPQ_V(xa); *(LAS float*)(lds + L_RS + w * 128 + t * 4) = rstd; }
                    if constexpr (i >= 66 && i < 69) cw[i - 66] = *(LAS v2u*)(lds + L_CS + (i - 66) * 256 + t * 8);
                    if constexpr (i == 72) mfma32p(aug_w(cw[1]), xa, ak);
                    if constexpr (i == 73) mfma32p(xa, aug_w(cw[2]), av);
                    if constexpr (i == 74) mfma32p(aug_w(cw[0]), xa, aq);
                    if constexpr (i >= 78 && i < 82) rv[i - 78] = *(LAS f32x4*)(lds + L_RS + w * 128 + (8 * (i - 78) + 4 * h) * 4);
                  });
      TS(4 + 32 * L);
      const v4u kf0 = pack8(ak, 0, rstd), kf1 = pack8(ak, 8, rstd);
      v4u vf[2];
#pragma unroll
      for (int q = 0; q < 4; ++q) {
        vf[q >> 1][(q & 1) * 2] = pk(av[4 * q] * rv[q][0], av[4 * q + 1] * rv[q][1]);
        vf[q >> 1][(q & 1) * 2 + 1] = pk(av[4 * q + 2] * rv[q][2], av[4 * q + 3] * rv[q][3]);
      }
      if (w < 3) {
        LAS char* e = lds + L_EX + (w + 1) * 3072 + lane * 16;
        *(LAS v4u*)e = kf0; *(LAS v4u*)(e + 1024) = kf1; *(LAS v4u*)(e + 2048) = vf[1];
      }
      const v4u qf0 = pack8(aq, 0, 1.f), qf1 = pack8(aq, 8, 1.f);
      f32x16 sc;
#pragma unroll
      for (int r = 0; r < 16; ++r) sc[r] = bias[r];
      mfma32p(kf0, qf0, sc); mfma32p(kf1, qf1, sc);
      TS(5 + 32 * L);
      BAR();
      TS(6 + 32 * L);
      LAS char* e = lds + L_EX + w * 3072 + lane * 16;
      f32x16 sp = {};
#pragma unroll
      for (int r = 0; r < 4; ++r) sp[12 + r] = bias[16 + r];
      mfma32p(lds16(e), qf0, sp); mfma32p(lds16(e + 1024), qf1, sp);
      const v4u vp = lds16(e + 2048);
      float p[16], pp[4], mx = NEG;
#pragma unroll
      for (int r = 0; r < 16; ++r) { p[r] = sc[r]; mx = fmaxf(mx, p[r]); }
#pragma unroll
      for (int r = 0; r < 4; ++r) { pp[r] = sp[12 + r]; mx = fmaxf(mx, pp[r]); }
      const float cf = rstd * (0.18898223650461362f * 1.4426950408889634f);
      const float mc = -xmax32(mx) * cf;
#pragma unroll
      for (int r = 0; r < 16; ++r) p[r] = __builtin_amdgcn_exp2f(__builtin_fmaf(p[r], cf, mc));
#pragma unroll
      for (int r = 0; r < 4; ++r) pp[r] = __builtin_amdgcn_exp2f(__builtin_fmaf(pp[r], cf, mc));
      const v4u pf0 = {pk(p[0], p[1]), pk(p[2], p[3]), pk(p[4], p[5]), pk(p[6], p[7])};
      const v4u pf1 = {pk(p[8], p[9]), pk(p[10], p[11]), pk(p[12], p[13]), pk(p[14], p[15])};
      const v4u pfp = {0u, 0u, pk(pp[0], pp[1]), pk(pp[2], pp[3])};
      f32x16 o = {};
      mfma32p(vf[0], pf0, o); mfma32p(vf[1], pf1, o); mfma32p(vp, pfp, o);
      float s0 = pp[0] + pp[1], s1 = pp[2] + pp[3];
#pragma unroll
      for (int r = 0; r < 8; ++r) { s0 += p[2 * r]; s1 += p[2 * r + 1]; }
      const float inv = __builtin_amdgcn_rcpf(xsum32(s0 + s1));
      st16(pack8(o, 0, inv), rs, lane * 16, OFF_O + L * OBUF + (ttA * 32 + 2 * m) * 1024);
      st16(pack8(o, 8, inv), rs, lane * 16 + 1024, OFF_O + L * OBUF + (ttA * 32 + 2 * m) * 1024);
      TS(7 + 32 * L);
      publish(ws, myflag, 4 * L + 1, lane);
      TS(8 + 32 * L);
    }
    if constexpr (QR) {
      if (active) BAR();
      DMAT(wc + 2 * SLOT, 7);
      if (active) BAR();
      VMWAIT(0);
    } else if (active) {
      constexpr int W0 = 8;
      v4u of[16];
      const int ooff = OFF_O + L * OBUF + (ttB * 32 + 16 * kh) * 1024;
      BAR();
      TS(11 + 32 * L);
      v4u wl[4];
#pragma unroll
      for (int i = 0; i < 4; ++i) wl[i] = __builtin_amdgcn_raw_buffer_load_b128(rs, lane * 16 + (i & 2) * 1024, OFF_WB + (j * 64 + 56 + kh * 4 + ((i & 1) ^ kh ^ 1)) * 1024, AUX_SC1);
      f32x16 acc[2] = {{}, {}};
      LAS char* wp0 = lds + slot_of(3) + kh * 15 * 1024 + lane * 16; LAS char* wp1 = lds + slot_of(3) + 1024 + kh * 13 * 1024 + lane * 16;
      OPQ_V(wp0); OPQ_V(wp1);
      pipe<32, 6>([&](auto I) AI { constexpr int i = I.v;
                    if constexpr (i < 28) return lds16(((i & 1) ? wp0 : wp1) + (i / 14) * SLOT + ((i % 14) >> 1) * 2048);
                    else return wl[i & 3]; },
                  [&]() AI {
                    poll(ws, fi_k(g, kh, ttB & 3, lane), 4 * L + 1);
                    TS(9 + 32 * L);
#pragma unroll
                    for (int i = 0; i < W0; ++i) { of[i] = ldf(rs, lane, ooff, i); __builtin_amdgcn_sched_barrier(0); }
                  },
                  [&](auto I, v4u wf) AI { constexpr int i = I.v;
                    mfma32p(wf, of[i >> 1], acc[(i & 1) ^ 1]);
                    if constexpr (i + W0 < 16) of[i + W0] = ldf(rs, lane, ooff, i + W0);
                  });
      TS(12 + 32 * L);
      res_epilogue<false>(acc, res, lds, rs, ws, out, w, lane, g, m, ttB, ntB, 2 * L + 1, active, false, 4 * L + 2);
      TS(13 + 32 * L);
    }
    auto phase_d = [&](auto PARC) AI {
      constexpr int PAR = PARC.v, W0 = 14;
      const int hoff = OFF_H + L * HBUF + (ttB * 112 + 56 * kh + 7 * PAR) * 1024;
      v4u hf[W0];
      f32x16 acc[2] = {{}, {}};
      LAS char* db[2];
      pipe<56, 6>(
          [&](auto I) AI { constexpr int i = I.v, r = i / 14;
            if constexpr (i % 14 == 0) {
              if constexpr (r < 2) BAR();
              if constexpr (r == 0) TS(27 + 32 * L);
              if constexpr (PAR && r == 0) DMA(wd + 4 * SLOT, 16);
              if constexpr (PAR && r == 1) DMA(wd + 6 * SLOT, 12);
              constexpr int sl = slot_of(r == 3 ? 12 : 12 + PAR + 2 * r);
              db[1] = lds + sl + kh * 15 * 1024 + l16; db[0] = lds + sl + 1024 + kh * 13 * 1024 + l16;
            }
            if constexpr (i == (PAR ? DQ : 42)) { if constexpr (PAR) VMWAIT(3); TS(28 + 32 * L); BAR(); TS(29 + 32 * L); }
            if constexpr (PAR && r >= 2) return wr[i - 28]; else return lds16(db[i & 1] + ((i % 14) >> 1) * 2048);
          },
          [&]() AI {
            poll(ws, (g * 16 + 8 * kh + PAR + 2 * (lane & 3)) * 4 + (ttB & 3), 4 * L + 3);
            TS(25 + 32 * L);
#pragma unroll
            for (int i = 0; i < W0; ++i) { hf[i] = ldf(rs, lane, hoff + (i / 7) * 14336, i % 7); __builtin_amdgcn_sched_barrier(0); }
          },
          [&](auto I, v4u wf) AI { constexpr int i = I.v;
            constexpr int k = i >> 1;
            mfma32p(wf, hf[k % W0], acc[(i & 1) ^ 1]);
            if constexpr ((i & 1) && k + W0 < 28) hf[k % W0] = ldf(rs, lane, hoff + ((k + W0) / 7) * 14336, (k + W0) % 7);
          });
      TS(30 + 32 * L);
      if constexpr (!PAR) { if (L == 0) { DMA(wa, 0); DMA(wa + SLOT, 1); } }
      res_epilogue<true>(acc, res, lds, rs, ws, out, w, lane, g, m, ttB, ntB, 2, active, L == 1, 4 * L + 4);
      TS(31 + 32 * L);
    };
    if constexpr (QR) {
      BAR();
      DMAT(wc + 3 * SLOT, 8); DMAT(wc + 4 * SLOT, 9);
      if (L == 0) {
#pragma unroll
        for (int i = 0; i < 14; ++i) { wr[14 + i] = __builtin_amdgcn_raw_buffer_load_b128(rs, l16, wd + 7 * SLOT + (kh * 14 + (i & ~1) + ((i & 1) ^ kh ^ 1)) * 1024, AUX_SC1); __builtin_amdgcn_sched_barrier(0); }
        VMWAIT(14);
      } else VMWAIT(0);
      BAR();
      DMA(wc + 5 * SLOT, 10); DMA(wc + 6 * SLOT, 11); DMA(wd, 12);
      if (L == 0) {
#pragma unroll
        for (int i = 0; i < 14; ++i) { wr[i] = __builtin_amdgcn_raw_buffer_load_b128(rs, l16, wd + 5 * SLOT + (kh * 14 + (i & ~1) + ((i & 1) ^ kh ^ 1)) * 1024, AUX_SC1); __builtin_amdgcn_sched_barrier(0); }
        VMWAIT(21);
      } else VMWAIT(7);
      BAR();
      DMA(wd + SLOT, 13); DMA(wd + 2 * SLOT, 14);
      BAR();
      DMA(wd + 3 * SLOT, 15);
      VMWAIT(7);
      __builtin_amdgcn_s_setprio(QPRIO);
      if (active) phase_d(IC<1>{}); else { VMWAIT(0); BAR(); }
      __builtin_amdgcn_s_setprio(0);
      if (L == 0) DMAT(wa + 2 * SLOT, 2);
    } else {
      constexpr int W0 = 8;
      v4u xf[28]; v2u st[7];
      const int xoff = OFF_X + (2 * L + 1) * XBUF + ttA * 28 * 1024, soff = OFF_ST + (2 * L + 1) * SBUF + ttA * 14 * 256;
      BAR();
      TS(16 + 32 * L);
      const int hoff = OFF_H + L * HBUF + (ttA * 112 + 7 * m) * 1024;
      LAS char* cs = lds + L_CS + 768 + t * 8;
      f32x16 a[4] = {{}, {}, {}, {}};
      float mean, rstd, c1, kk; v4u xa; float hv[8], ev = 0.f, tv = 0.f; v2u cw[7];
      auto epi = [&](const f32x16& acc, int e, int tile) AI {
        const int q = e >> 1;
        if (e & 1) {
          if (q > 0) { hv[q - 1] *= ev; OPQ_V(hv[q - 1]); }
          if (q < 8) { ev = __builtin_amdgcn_exp2f(tv); hv[q] = acc[q] * acc[8 + q]; OPQ_V(ev); OPQ_V(hv[q]); }
        } else if (e < 18) {
          if (q > 0) ev = __builtin_fmaf(ev, kk, kk);
          if (q < 8) { tv = acc[q] * c1; OPQ_V(tv); }
          if (q > 0) { ev = __builtin_amdgcn_rcpf(ev); OPQ_V(ev); }
        } else st16(v4u{pk(hv[0], hv[1]), pk(hv[2], hv[3]), pk(hv[4], hv[5]), pk(hv[6], hv[7])}, rs, lane * 16 + (tile & 3) * 1024, hoff + (tile >> 2) * 4096);
      };
      pipe<196, 5>(
          [&](auto I) AI { constexpr int i = I.v;
            if constexpr (i == 84 || i == 140) { TS(17 + 2 * ((i - 84) / 56) + 32 * L); BAR(); TS(18 + 2 * ((i - 84) / 56) + 32 * L); }
            if constexpr (i == 168) BAR();
            constexpr int tile = i < 84 ? i % 3 : i / 28;
            constexpr int ks = i < 84 ? i / 3 : i % 28;
            return i < 196 ? lds16(LSL(5 + tile) + ks * 1024) : v4u{};
          },
          [&]() AI {
            poll(ws, fi_x(g, w, lane), 4 * L + 2);
            TS(14 + 32 * L);
#pragma unroll
            for (int i = 0; i < W0; ++i) { xf[i] = ldf(rs, lane, xoff, i); __builtin_amdgcn_sched_barrier(0); }
          },
          [&](auto I, v4u wf) AI { constexpr int i = I.v;
            constexpr int tile = i < 84 ? i % 3 : i / 28;
            constexpr int ks = i < 84 ? i / 3 : i % 28;
            if constexpr (i >= 84 && ks == 0) { a[tile & 3] = f32x16{}; mfma32p(aug_w(cw[tile]), xa, a[tile & 3]); }
            mfma32p(wf, xf[ks], a[tile & 3]);
            if constexpr (i < 84 && i % 3 == 0 && i / 3 + W0 < 28) xf[i / 3 + W0] = ldf(rs, lane, xoff, i / 3 + W0);
            if constexpr (i % 3 == 1 && i / 3 < 7) st[i / 3] = ld8(rs, ((7 * h + i / 3) * 32 + t) * 8, soff);
            if constexpr (i >= 70 && i < 73) cw[i - 70] = *(LAS v2u*)(cs + (i - 70) * 256);
            if constexpr (i >= 78 && i % 28 == 22 && i < 170) cw[i / 28 + 1] = *(LAS v2u*)(cs + (i / 28 + 1) * 256);
            if constexpr (i == 62) { fin_stats(st, mean, rstd); OPQ_V(mean); OPQ_V(rstd); }
            if constexpr (i == 66) { xa = aug_x(-mean, h == 0); c1 = rstd * -1.4426950408889634f; kk = __builtin_amdgcn_rcpf(rstd * rstd); OPQ_V(xa); OPQ_V(c1); OPQ_V(kk); }
            if constexpr (i >= 75 && i < 78) mfma32p(aug_w(cw[i - 75]), xa, a[i - 75]);
            if constexpr (i >= 83) epi(a[((i - 83) / 19) & 3], (i - 83) % 19, (i - 83) / 19);
          });
      TS(21 + 32 * L);
      epi(a[1], 18, 5);
      st16(swiglu(a[2], rstd), rs, lane * 16 + 2048, hoff + 4096);
      TS(23 + 32 * L);
      publish(ws, myflag, 4 * L + 3, lane);
      TS(24 + 32 * L);
      if (active) phase_d(IC<0>{}); else { BAR(); if (L == 0) { DMA(wa, 0); DMA(wa + SLOT, 1); } }
    }
  }
  };
  if (w >= 4) run(IC<1>{}); else run(IC<0>{});
}

extern "C" void kernel_launch(void* const* d_in, const int* in_sizes, int n_in, void* d_out, int out_size, void* d_ws,
                              size_t ws_size, hipStream_t stream) {
  (void)in_sizes; (void)n_in; (void)out_size; (void)ws_size;
  (void)hipFuncSetAttribute((const void*)k_main, hipFuncAttributeMaxDynamicSharedMemorySize, LDS_BYTES);
  k_main<<<256, 512, LDS_BYTES, stream>>>((const char*)d_in[0], (const char*)d_in[1], (const char*)d_in[2], (const char*)d_in[4],
                                          (const char*)d_in[5], (const char*)d_in[6], (char*)d_ws, (unsigned short*)d_out);
}
```

```cpp
#include <hip/hip_runtime.h>
#include <hip/hip_bf16.h>

typedef unsigned v4u __attribute__((ext_vector_type(4)));
typedef unsigned v2u __attribute__((ext_vector_type(2)));
typedef float f32x4 __attribute__((ext_vector_type(4)));
typedef float f32x2 __attribute__((ext_vector_type(2)));
typedef float f32x16 __attribute__((ext_vector_type(16)));
typedef __bf16 bf16x8 __attribute__((ext_vector_type(8)));
typedef __bf16 bf16x2 __attribute__((ext_vector_type(2)));
#define LAS __attribute__((address_space(3)))
#define GAS __attribute__((address_space(1)))
#define DEV __device__ __forceinline__
#define AI __attribute__((always_inline))

constexpr int S_ = 2048, D_ = 448, F_ = 1792;
constexpr int OFF_FLAG = 0;
constexpr int OFF_PF = 65536, OFF_EP = OFF_PF + 65536;
constexpr int OFF_WA = 135168;
constexpr int OFF_WB = OFF_WA + 16 * 84 * 1024;
constexpr int OFF_WC = OFF_WB + 7 * 64 * 1024;
constexpr int OFF_WD = OFF_WC + 16 * 196 * 1024;
constexpr int OFF_CS = OFF_WD + 7 * 224 * 1024;
constexpr int XBUF = 64 * 28 * 1024, SBUF = 64 * 14 * 256, OBUF = 64 * 32 * 1024, HBUF = 64 * 112 * 1024;
constexpr int OFF_X = OFF_CS + 16 * 3072;
constexpr int OFF_ST = OFF_X + 4 * XBUF;
constexpr int OFF_O = OFF_ST + 4 * SBUF;
constexpr int OFF_H = OFF_O + 2 * OBUF;
constexpr int WS_BYTES = OFF_H + 2 * HBUF;
constexpr int SLOT = 28 * 1024;
constexpr int L_EX = 5 * SLOT, L_CS = L_EX + 16384, L_RS = L_CS + 3072, LDS_BYTES = L_RS + 512;
static_assert(LDS_BYTES <= 163840, "lds");
constexpr int AUX_SC1 = 16;

DEV unsigned pk(float a, float b) { bf16x2 t = {(__bf16)a, (__bf16)b}; return __builtin_bit_cast(unsigned, t); }
DEV float asf(unsigned u) { return __builtin_bit_cast(float, u); }
DEV float blo(unsigned u) { return __builtin_bit_cast(float, u << 16); }
DEV float bhi(unsigned u) { return __builtin_bit_cast(float, u & 0xffff0000u); }
DEV f32x16 mfma32(v4u a, v4u b, f32x16 c) {
  return __builtin_amdgcn_mfma_f32_32x32x16_bf16(__builtin_bit_cast(bf16x8, a), __builtin_bit_cast(bf16x8, b), c, 0, 0, 0);
}
DEV void mfma32p(v4u a, v4u b, f32x16& c) { c = mfma32(a, b, c); asm volatile("" : "+v"(c)); __builtin_amdgcn_sched_barrier(0); }
DEV f32x4 mfma16(v4u a, v4u b, f32x4 c) {
  return __builtin_amdgcn_mfma_f32_16x16x32_bf16(__builtin_bit_cast(bf16x8, a), __builtin_bit_cast(bf16x8, b), c, 0, 0, 0);
}
DEV float xsum32(float v) {
  auto r = __builtin_amdgcn_permlane32_swap(__builtin_bit_cast(unsigned, v), __builtin_bit_cast(unsigned, v), false, false);
  return __builtin_bit_cast(float, (unsigned)r[0]) + __builtin_bit_cast(float, (unsigned)r[1]);
}
DEV float xmax32(float v) {
  auto r = __builtin_amdgcn_permlane32_swap(__builtin_bit_cast(unsigned, v), __builtin_bit_cast(unsigned, v), false, false);
  return fmaxf(__builtin_bit_cast(float, (unsigned)r[0]), __builtin_bit_cast(float, (unsigned)r[1]));
}
DEV void split3(float v, unsigned& hi, unsigned& mid, unsigned& lo) {
  hi = __builtin_bit_cast(unsigned, v) & 0xffff0000u;
  float r = v - __builtin_bit_cast(float, hi);
  mid = __builtin_bit_cast(unsigned, r) & 0xffff0000u;
  float r2 = r - __builtin_bit_cast(float, mid);
  lo = __builtin_bit_cast(unsigned, r2) & 0xffff0000u;
}
DEV v4u aug_x(float negmean, bool on) {
  unsigned hi, mid, lo; split3(negmean, hi, mid, lo);
  v4u r = {hi | (hi >> 16), mid | (mid >> 16), lo | (hi >> 16), 0u};
  if (!on) r = v4u{0u, 0u, 0u, 0u};
  return r;
}
DEV v4u aug_w(v2u c) { return v4u{c.x, c.x, c.y, 0u}; }

typedef short s16x4 __attribute__((ext_vector_type(4)));
typedef __amdgpu_buffer_rsrc_t rsrc_t;
#define BAR() asm volatile("s_waitcnt lgkmcnt(0)\n\ts_barrier" ::: "memory")
#define VMWAIT(n) asm volatile("s_waitcnt vmcnt(" #n ")" ::: "memory")
DEV v4u ld16(rsrc_t rs, int voff, int soff) { return __builtin_amdgcn_raw_buffer_load_b128(rs, voff, soff, AUX_SC1); }
DEV v2u ld8(rsrc_t rs, int voff, int soff) { return __builtin_amdgcn_raw_buffer_load_b64(rs, voff, soff, AUX_SC1); }
DEV void st16(v4u v, rsrc_t rs, int voff, int soff) { __builtin_amdgcn_raw_buffer_store_b128(v, rs, voff + soff, 0, AUX_SC1); }
DEV void st8(v2u v, rsrc_t rs, int voff, int soff) { __builtin_amdgcn_raw_buffer_store_b64(v, rs, voff, soff, AUX_SC1); }
DEV v4u ldf(rsrc_t rs, int lane, int base, int i) { return ld16(rs, lane * 16 + (i & 3) * 1024, base + (i >> 2) * 4096); }
DEV v4u lds16(LAS char* p) { return *(LAS v4u*)p; }

DEV v2u tr8(LAS char* p) { return __builtin_bit_cast(v2u, __builtin_amdgcn_ds_read_tr16_b64_v4i16((LAS s16x4*)p)); }
DEV void pack_unit(int u, int tid, LAS char* sm, rsrc_t rs, const char* x, const char* wqkv, const char* wo, const char* wg,
                   const char* wu, const char* wd) {
  const int w = __builtin_amdgcn_readfirstlane(tid >> 6), lane = tid & 63, n = lane & 31, h = lane >> 5;
  if (u < 0) { BAR(); return; }
  if (u < 77) {
    const char* src; int ld;
    if (u < 21) { src = wqkv + u * 128; ld = 2688; } else if (u < 49) { src = wg + (u - 21) * 128; ld = 3584; } else { src = wu + (u - 49) * 128; ld = 3584; }
    src += (tid >> 3) * ld + (tid & 7) * 16;
    v4u v[14];
#pragma unroll
    for (int i = 0; i < 14; ++i) v[i] = *(const v4u*)(src + i * 32 * ld);
    float c[8] = {0.f, 0.f, 0.f, 0.f, 0.f, 0.f, 0.f, 0.f};
#pragma unroll
    for (int i = 0; i < 14; ++i)
#pragma unroll
      for (int q = 0; q < 4; ++q) { const unsigned e = v[i][q]; c[2 * q] += blo(e); c[2 * q + 1] += bhi(e); }
    *(LAS f32x4*)(sm + (tid >> 3) * 272 + (tid & 7) * 32) = f32x4{c[0], c[1], c[2], c[3]};
    *(LAS f32x4*)(sm + (tid >> 3) * 272 + (tid & 7) * 32 + 16) = f32x4{c[4], c[5], c[6], c[7]};
    BAR();
    if (tid < 64) {
      float cs = 0.f;
#pragma unroll
      for (int i = 0; i < 32; ++i) cs += *(LAS float*)(sm + i * 272 + tid * 4);
      unsigned hi, mid, lo; split3(cs, hi, mid, lo);
      int o;
      if (u < 21) { const int c_ = u * 64 + tid, t = c_ / 448, r = c_ % 448; o = (r / 28) * 3072 + t * 256 + (r % 28) * 8;
        if (r % 28 < 4) st8(v2u{0u, 0u}, rs, OFF_CS + o + 28 * 8, 0); }
      else { const int f = ((u - 21) % 28) * 64 + tid; o = (f / 112) * 3072 + (3 + (f % 112) / 16) * 256 + ((f & 15) + (u >= 49 ? 16 : 0)) * 8; }
      st8(v2u{(hi >> 16) | mid, (lo >> 16) | hi}, rs, OFF_CS + o, 0);
    }
    return;
  }
  const bool isx = u >= 119 && u < 183;
  const int lstride = isx ? 912 : 960;
  int dst0 = 0, dstep = 0, cstep = 128, col0 = 64 * (w >> 1), row0 = 16 * (w & 1);
  {
    const char* src; int ld = 896;
    if (u < 119) { const int q = u - 77; ld = 2688; src = wqkv + ((q / 3) * 32 + 8 * w) * 2688 + (q % 3) * 896;
      dst0 = OFF_WA + (((w >> 1) * 3 + q % 3) * 28 + 2 * (q / 3) + (w & 1)) * 1024; dstep = 168 * 1024; cstep = 112; col0 = 56 * (w >> 1); }
    else if (u < 183) { const int tt = u - 119, g = tt >> 2; ld = 1792; src = x + (2 * (128 * (g & 7) + 32 * (tt & 3) + 8 * w) + (g >> 3)) * 896; }
    else if (u < 199) { const int s = 2 * (u - 183) + (w & 1), kh = s >> 4, ks = s & 15, c = ks / 7; src = wo + ((u - 183) * 28 + 8 * w) * 896;
      dst0 = OFF_WB + (c * 28 + kh * (c < 2 ? 14 : 4) + (ks % 7) * 2 + (w >> 1)) * 1024; dstep = 64 * 1024; }
    else if (u < 311) { const int q = u - 199; ld = 3584; src = (w < 2 ? wg : wu) + ((q >> 2) * 16 + 8 * (w & 1)) * 3584 + (q & 3) * 896;
      dst0 = OFF_WC + (((q & 3) * 28 + w) * 28 + (q >> 2)) * 1024; dstep = 112 * 1024; col0 = 32 * w; }
    else { const int s = 2 * (u - 311) + (w & 1), kh = s / 56, ks = s % 56; src = wd + ((u - 311) * 32 + 8 * w) * 896;
      dst0 = OFF_WD + ((ks / 7) * 28 + kh * 14 + (ks % 7) * 2 + (w >> 1)) * 1024; dstep = 224 * 1024; }
    const bool pad = u >= 183 && u < 199 && w == 3;
    if (lane < 56) {
      v4u v[8];
#pragma unroll
      for (int i = 0; i < 8; ++i) v[i] = *(const v4u*)(src + ((pad && i >= 4) ? 0 : i * ld) + lane * 16);
#pragma unroll
      for (int i = 0; i < 8; ++i) *(LAS v4u*)(sm + (8 * w + i) * lstride + lane * 16) = (pad && i >= 4) ? v4u{0u, 0u, 0u, 0u} : v[i];
    }
  }
  BAR();
  if (isx) {
    const int tt = u - 119;
#pragma unroll
    for (int it = 0; it < 4; ++it) {
      const int nt = w + 4 * it;
      if (nt >= 14) break;
      v2u a[4];
#pragma unroll
      for (int i = 0; i < 4; ++i) a[i] = *(LAS v2u*)(sm + n * 912 + nt * 64 + i * 16 + h * 8);
      __builtin_amdgcn_raw_buffer_store_b128(v4u{a[0].x, a[0].y, a[1].x, a[1].y}, rs, OFF_X + (tt * 28 + 2 * nt) * 1024 + lane * 16, 0, AUX_SC1);
      __builtin_amdgcn_raw_buffer_store_b128(v4u{a[2].x, a[2].y, a[3].x, a[3].y}, rs, OFF_X + (tt * 28 + 2 * nt + 1) * 1024 + lane * 16, 0, AUX_SC1);
      float sm1 = 0.f;
#pragma unroll
      for (int i = 0; i < 4; ++i) sm1 += blo(a[i].x) + bhi(a[i].x) + blo(a[i].y) + bhi(a[i].y);
      sm1 = xsum32(sm1);
      const float mu = sm1 * (1.f / 32.f);
      float m2 = 0.f;
#pragma unroll
      for (int i = 0; i < 4; ++i) {
        const float d0 = blo(a[i].x) - mu, d1 = bhi(a[i].x) - mu, d2 = blo(a[i].y) - mu, d3 = bhi(a[i].y) - mu;
        m2 += d0 * d0 + d1 * d1 + d2 * d2 + d3 * d3;
      }
      m2 = xsum32(m2);
      if (h == 0) st8(v2u{__builtin_bit_cast(unsigned, sm1), __builtin_bit_cast(unsigned, m2)}, rs, OFF_ST + (tt * 14 + nt) * 256 + n * 8, 0);
    }
    return;
  }
  const int q4 = (lane & 15) >> 2, p4 = lane & 3, cb = (lane >> 4) & 1;
  const bool gu = u >= 199 && u < 311;
  LAS char* pa = sm + ((gu ? 16 * cb : row0) + 4 * h + q4) * 960 + col0 + (gu ? 0 : 32 * cb) + 8 * p4;
  v2u lo[8], hi[8];
#pragma unroll
  for (int i = 0; i < 8; ++i) { LAS char* q = pa + ((i < 7 || u < 119) ? i * cstep : 0); lo[i] = tr8(q); hi[i] = tr8(q + 8 * 960); }
  const bool z = u < 119 && n >= 28;
#pragma unroll
  for (int i = 0; i < 8; ++i)
    if (i < 7 || u < 119) st16(z ? v4u{0u, 0u, 0u, 0u} : v4u{lo[i].x, lo[i].y, hi[i].x, hi[i].y}, rs, lane * 16, dst0 + i * dstep);
}

DEV void poll(char* ws, int fi, unsigned want) {
  GAS unsigned* f = (GAS unsigned*)(ws + OFF_FLAG) + fi * 16;
  for (int spin = 0; spin < (1 << 20); ++spin) {
    const unsigned v = __hip_atomic_load(f, __ATOMIC_RELAXED, __HIP_MEMORY_SCOPE_AGENT);
    if (__all(v >= want)) break;
  }
  __builtin_amdgcn_fence(__ATOMIC_ACQUIRE, "wavefront");
}
DEV int fi_x(int g, int T, int lane) { const int i = lane < 14 ? lane : 0; return (g * 16 + 8 * (T >> 1) + (i >> 1)) * 4 + (T & 1) + 2 * (i & 1); }
DEV int fi_k(int g, int kh, int T, int lane) { return (g * 16 + 8 * kh + (lane & 7)) * 4 + T; }
DEV void publish(char* ws, int fi, unsigned val, int lane) {
  VMWAIT(0);
  if (lane == 0) __hip_atomic_store((GAS unsigned*)(ws + OFF_FLAG) + fi * 16, val, __ATOMIC_RELAXED, __HIP_MEMORY_SCOPE_AGENT);
}
template <int N, int TH> DEV void dma(rsrc_t rs, int src, int dst, int hw, int lane16) {
#pragma unroll
  for (int i = 0; i < N; ++i) {
    unsigned keep;
    asm volatile("s_mov_b32 %0, m0\n\ts_mov_b32 m0, %1\n\ts_nop 0\n\tbuffer_load_dwordx4 %2, %3, %4 offen sc1 lds\n\ts_mov_b32 m0, %0"
                 : "=&s"(keep) : "s"(dst + (hw * N + i) * 1024), "v"(lane16), "s"(rs), "s"(src + (hw * N + i) * 1024) : "memory");
    if constexpr (TH >= 0) asm volatile("s_waitcnt vmcnt(%0)" :: "i"(TH) : "memory");
  }
}
#define THR 4
#define DQ 42
#define QPRIO 1
#define QDELAY 50
#define DMA(src, c) dma<7, -1>(rs, src, slot_of(c), hw, l16)
#define DMAT(src, c) dma<7, THR>(rs, src, slot_of(c), hw, l16)
#define OPQ_S(x) asm volatile("" : "+s"(x))
#define OPQ_V(x) asm volatile("" : "+v"(x))
template <int I> struct IC { static constexpr int v = I; };
template <int I, int N, class F> DEV void sfor(F& f) { if constexpr (I < N) { f(IC<I>{}); sfor<I + 1, N>(f); } }
template <int N, int PF, class RD, class MID, class ST> DEV void pipe(RD rd, MID mid, ST st) {
  constexpr int R = PF + 1;
  v4u wb[R];
  auto pro = [&](auto I) __attribute__((always_inline)) { wb[I.v] = rd(I); };
  sfor<0, PF>(pro);
  mid();
  __builtin_amdgcn_sched_barrier(0);
  auto body = [&](auto I) __attribute__((always_inline)) {
    constexpr int i = I.v;
    st(I, wb[i % R]);
    if constexpr (i + PF < N) wb[(i + PF) % R] = rd(IC<i + PF>{});
    asm volatile("" :: "v"(wb[i % R]));
    __builtin_amdgcn_sched_barrier(0);
  };
  sfor<0, N>(body);
}
constexpr int slot_of(int c) { return (c % 5) * SLOT; }
DEV void fin_stats(const v2u (&st)[7], float& mean, float& rstd) {
  float sm = 0.f;
#pragma unroll
  for (int i = 0; i < 7; ++i) sm += asf(st[i].x);
  mean = xsum32(sm) * (1.f / D_);
  float m2 = 0.f;
#pragma unroll
  for (int i = 0; i < 7; ++i) {
    const float d = asf(st[i].x) * (1.f / 32.f) - mean;
    m2 += asf(st[i].y) + 32.f * d * d;
  }
  rstd = __builtin_amdgcn_rsqf(xsum32(m2) * (1.f / D_) + 1e-5f);
}
DEV v4u pack8(const f32x16& a, int o, float sc) {
  return v4u{pk(a[o] * sc, a[o + 1] * sc), pk(a[o + 2] * sc, a[o + 3] * sc), pk(a[o + 4] * sc, a[o + 5] * sc), pk(a[o + 6] * sc, a[o + 7] * sc)};
}
DEV v4u swiglu(const f32x16& a, float rstd) {
  float hv[8];
#pragma unroll
  for (int i = 0; i < 8; ++i) {
    const float g = a[i] * rstd, up = a[8 + i] * rstd;
    const float e = __builtin_amdgcn_exp2f(g * -1.4426950408889634f);
    hv[i] = g * up * __builtin_amdgcn_rcpf(1.f + e);
  }
  return v4u{pk(hv[0], hv[1]), pk(hv[2], hv[3]), pk(hv[4], hv[5]), pk(hv[6], hv[7])};
}

template <bool W8> DEV void res_epilogue(f32x16 (&acc)[2], v4u (&res)[2], LAS char* lds, rsrc_t rs, char* ws, unsigned short* out, int w, int lane,
                      int g, int m, int tt, int nt, int xb, bool active, bool last, unsigned flagval) {
  LAS char* sx = lds + (W8 ? 3 * SLOT : L_EX) + lane * 16;
  float v[16];
#pragma unroll
  for (int q = 0; q < 8; ++q) { const unsigned r = res[q >> 2][q & 3]; v[2 * q] = acc[0][2 * q] + blo(r); v[2 * q + 1] = acc[0][2 * q + 1] + bhi(r); }
#pragma unroll
  for (int q = 0; q < 4; ++q) *(LAS f32x4*)(sx + w * 4096 + q * 1024) = f32x4{acc[1][4 * q], acc[1][4 * q + 1], acc[1][4 * q + 2], acc[1][4 * q + 3]};
  if (W8 && w >= 4) {
#pragma unroll
    for (int q = 0; q < 4; ++q) *(LAS f32x4*)(sx + (w + 4) * 4096 + q * 1024) = f32x4{acc[0][4 * q], acc[0][4 * q + 1], acc[0][4 * q + 2], acc[0][4 * q + 3]};
  }
  BAR();
  if (W8 && w >= 4) return;
#pragma unroll
  for (int q = 0; q < 4; ++q) {
    f32x4 p0 = *(LAS f32x4*)(sx + (w ^ 2) * 4096 + q * 1024);
    if (W8) p0 += *(LAS f32x4*)(sx + (w ^ 6) * 4096 + q * 1024) + *(LAS f32x4*)(sx + (w + 8) * 4096 + q * 1024);
#pragma unroll
    for (int e = 0; e < 4; ++e) v[4 * q + e] += p0[e];
  }
#pragma unroll
  for (int q = 0; q < 8; ++q) res[q >> 2][q & 3] = pk(v[2 * q], v[2 * q + 1]);
  if (!active) return;
  if (last) {
    const int s = 2 * (128 * (g & 7) + 32 * (tt & 3) + (lane & 31)) + (g >> 3);
    unsigned short* rowp = out + (size_t)s * D_ + 32 * nt + 8 * (lane >> 5);
#pragma unroll
    for (int q = 0; q < 2; ++q) {
      auto a = __builtin_amdgcn_permlane32_swap(res[q][0], res[q][2], false, false);
      auto b = __builtin_amdgcn_permlane32_swap(res[q][1], res[q][3], false, false);
      *(v4u*)(rowp + 16 * q) = v4u{(unsigned)a[0], (unsigned)b[0], (unsigned)a[1], (unsigned)b[1]};
    }
    return;
  }
  st16(res[0], rs, lane * 16, OFF_X + xb * XBUF + (tt * 28 + 2 * nt) * 1024);
  st16(res[1], rs, lane * 16 + 1024, OFF_X + xb * XBUF + (tt * 28 + 2 * nt) * 1024);
  __builtin_amdgcn_sched_barrier(0);
  float s0 = 0.f, s1 = 0.f, q0 = 0.f, q1 = 0.f;
#pragma unroll
  for (int q = 0; q < 8; ++q) { s0 += v[2 * q]; s1 += v[2 * q + 1]; q0 += v[2 * q] * v[2 * q]; q1 += v[2 * q + 1] * v[2 * q + 1]; }
  const float sm = xsum32(s0 + s1), sq = xsum32(q0 + q1);
  const float m2 = fmaxf(sq - sm * sm * (1.f / 32.f), 0.f);
  if (lane < 32) st8(v2u{__builtin_bit_cast(unsigned, sm), __builtin_bit_cast(unsigned, m2)}, rs, lane * 8, OFF_ST + xb * SBUF + (tt * 14 + nt) * 256);
  publish(ws, (g * 16 + m) * 4 + w, flagval, lane);
}

__global__ __launch_bounds__(512, 2) void k_main(const char* x, const char* wqkv, const char* wo, const char* wg_, const char* wu_,
                                                  const char* wd_, char* ws, unsigned short* out) {
  extern __shared__ __attribute__((aligned(16))) char smem[];
  LAS char* lds = (LAS char*)smem;
  unsigned code_touch;
  {
    unsigned long long pc;
    asm volatile("s_getpc_b64 %0" : "=s"(pc));
    code_touch = *(const GAS unsigned*)((pc & ~127ull) + (threadIdx.x < 320u ? threadIdx.x : 0u) * 128u);
    __builtin_amdgcn_sched_barrier(0);
  }
  const int tid = threadIdx.x; int lane = tid & 63;
  int w = __builtin_amdgcn_readfirstlane(tid >> 6);
  const int b = blockIdx.x, xcd = b & 7, idx = b >> 3;
  int g = (xcd >> 1) * 4 + (idx >> 3), m = (xcd & 1) * 8 + (idx & 7);
  const int half = m >> 3, j = (m & 7) < 7 ? (m & 7) : 6;
  const bool active = (m & 7) < 7;
  const rsrc_t rs = __builtin_amdgcn_make_buffer_rsrc((void*)ws, 0, WS_BYTES, 0x00020000);
  const unsigned tag = __hip_atomic_load((GAS unsigned*)(ws + OFF_EP), __ATOMIC_RELAXED, __HIP_MEMORY_SCOPE_AGENT) + 1u;
  auto poll_setup = [&](int base, auto NC) AI {
    for (int spin = 0; spin < (1 << 20); ++spin) {
      bool ok = true;
#pragma unroll
      for (int i = 0; i < NC.v; ++i) ok &= __builtin_amdgcn_raw_buffer_load_b32(rs, lane * 128, base + i * 8192, AUX_SC1) == tag;
      if (__all(ok)) break;
      asm volatile("s_sleep 2" ::: "memory");
    }
    __builtin_amdgcn_fence(__ATOMIC_ACQUIRE, "wavefront");
  };
  v4u res[2];
  if (w < 4) {
    if (lane == 0) __hip_atomic_store((GAS unsigned*)(ws + OFF_FLAG) + ((g * 16 + m) * 4 + w) * 16, 0u, __ATOMIC_RELAXED, __HIP_MEMORY_SCOPE_AGENT);
    LAS unsigned* cnt = (LAS unsigned*)(lds + L_RS);
    if (tid == 0) *cnt = 0u;
    pack_unit(b < 21 ? b : b < 127 ? b + 56 : -1, tid, lds, rs, x, wqkv, wo, wg_, wu_, wd_);
    VMWAIT(0);
    if (lane == 0 && __hip_atomic_fetch_add(cnt, 1u, __ATOMIC_RELAXED, __HIP_MEMORY_SCOPE_WORKGROUP) == 3u)
      __hip_atomic_store((GAS unsigned*)(ws + OFF_PF) + b * 32, tag, __ATOMIC_RELAXED, __HIP_MEMORY_SCOPE_AGENT);
    BAR();
    if (w == 0) poll_setup(OFF_PF, IC<2>{});
    BAR();
    res[0] = ld16(rs, lane * 16, OFF_X + ((g * 4 + 2 * half + (w & 1)) * 28 + 2 * (2 * j + (w >> 1))) * 1024);
    res[1] = ld16(rs, lane * 16 + 1024, OFF_X + ((g * 4 + 2 * half + (w & 1)) * 28 + 2 * (2 * j + (w >> 1))) * 1024);
  } else {
    res[0] = res[1] = v4u{0u, 0u, 0u, 0u};
    BAR();
    if (b >= 127) __builtin_amdgcn_s_sleep(QDELAY);
    pack_unit(b < 16 ? -1 : b < 200 ? 167 + b : b - 179, tid & 255, lds + 3 * SLOT, rs, x, wqkv, wo, wg_, wu_, wd_);
    VMWAIT(0);
    BAR();
    if (tid == 256) __hip_atomic_store((GAS unsigned*)(ws + OFF_PF + 32768) + b * 32, tag, __ATOMIC_RELAXED, __HIP_MEMORY_SCOPE_AGENT);
    {
      const int hw = w - 4, l16 = lane * 16, wa = OFF_WA + m * 84 * 1024;
      dma<1, -1>(rs, OFF_CS + m * 3072, L_CS, 0, l16);
      DMA(wa, 0); DMA(wa + SLOT, 1); DMA(wa + 2 * SLOT, 2);
    }
  }
  asm volatile("" :: "v"(code_touch));
  auto run = [&](auto QRC) AI {
  constexpr bool QR = QRC.v;
  v4u wr[28];
  if constexpr (QR) {
#pragma unroll
    for (int i = 0; i < 28; ++i) wr[i] = v4u{0u, 0u, 0u, 0u};
  }
  for (int L = 0; L < 2; ++L) {
    OPQ_S(w); OPQ_S(g); OPQ_S(m); OPQ_V(lane);
    const int t = lane & 31, h = lane >> 5;
    const int mw = w & 1, kh = (w >> 1) & 1, par = w >> 2, hw = w & 3, l16 = lane * 16;
    const int wa = OFF_WA + m * 84 * 1024, wb = OFF_WB + j * 64 * 1024, wc = OFF_WC + m * 196 * 1024, wd = OFF_WD + j * 224 * 1024;
    const int ttA = g * 4 + w;
    const int ttB = g * 4 + 2 * half + mw;
    const int ntB = 2 * j + kh;
    const int myflag = (g * 16 + m) * 4 + w;
    LAS char* lb[3] = {lds + lane * 16, lds + 2 * SLOT + lane * 16, lds + 4 * SLOT + lane * 16};
    OPQ_V(lb[1]); OPQ_V(lb[2]);
#define LSL(c) (lb[((c) % 5) >> 1] + (((c) % 5) & 1) * SLOT)
    if constexpr (QR) {
      {
        const bool hasprev = (g & 7) > 0;
        const int gp = hasprev ? g - 1 : g, ttp = gp * 4 + 3;
        VMWAIT(0);
        BAR();
        if (L) poll(ws, fi_x(gp, 3, lane), 4);
        const int t16 = lane & 15, gq = lane >> 4, tk = 24 + (t16 & 7), isv = hw >> 1, nh = hw & 1;
        v4u xf[14]; v2u st[4];
#pragma unroll
        for (int i = 0; i < 4; ++i) st[i] = ld8(rs, ((gq + 4 * i < 14 ? gq + 4 * i : 0) * 32 + tk) * 8, OFF_ST + 2 * L * SBUF + ttp * 14 * 256);
#pragma unroll
        for (int i = 0; i < 14; ++i) xf[i] = ld16(rs, (gq >> 1) * 1024 + ((gq & 1) * 32 + tk) * 16, OFF_X + 2 * L * XBUF + (ttp * 28 + 2 * i) * 1024);
        if (L) { DMA(wb, 3); DMA(wb + SLOT, 4); }
        float sm = 0.f;
#pragma unroll
        for (int i = 0; i < 4; ++i) sm += (gq + 4 * i < 14) ? asf(st[i].x) : 0.f;
        sm += __shfl_xor(sm, 16); sm = xsum32(sm);
        const float mean = sm * (1.f / D_);
        float m2 = 0.f;
#pragma unroll
        for (int i = 0; i < 4; ++i) {
          const float d = asf(st[i].x) * (1.f / 32.f) - mean;
          m2 += (gq + 4 * i < 14) ? asf(st[i].y) + 32.f * d * d : 0.f;
        }
        m2 += __shfl_xor(m2, 16); m2 = xsum32(m2);
        const float rstd = __builtin_amdgcn_rsqf(m2 * (1.f / D_) + 1e-5f);
        LAS char* wp = lds + (1 + isv) * SLOT + (gq >> 1) * 1024 + ((gq & 1) * 32 + 16 * nh + t16) * 16;
        const v4u xa = aug_x(-mean, gq == 0), wg_ = aug_w(*(LAS v2u*)(lds + L_CS + (1 + isv) * 256 + (16 * nh + t16) * 8));
        f32x4 a = {0.f, 0.f, 0.f, 0.f};
        if (isv) {
#pragma unroll
          for (int i = 0; i < 14; ++i) a = mfma16(xf[i], lds16(wp + i * 2048), a);
          a = mfma16(xa, wg_, a);
          float r[4];
#pragma unroll
          for (int i = 0; i < 4; ++i) r[i] = __shfl(rstd, 4 * gq + i);
          *(LAS v2u*)(lds + L_EX + 2048 + (16 * nh + t16 + 32 * (gq & 1)) * 16 + 8 * (gq >> 1)) = v2u{pk(a[0] * r[0], a[1] * r[1]), pk(a[2] * r[2], a[3] * r[3])};
        } else {
#pragma unroll
          for (int i = 0; i < 14; ++i) a = mfma16(lds16(wp + i * 2048), xf[i], a);
          a = mfma16(wg_, xa, a);
          *(LAS v2u*)(lds + L_EX + nh * 1024 + (16 + t16 + 32 * (gq & 1)) * 16 + 8 * (gq >> 1)) = v2u{pk(a[0] * rstd, a[1] * rstd), pk(a[2] * rstd, a[3] * rstd)};
        }
      }
      if (L == 0) {
        poll_setup(OFF_PF + 32768 + hw * 8192, IC<1>{});
        BAR();
        if (b == 0 && tid == 256) __hip_atomic_store((GAS unsigned*)(ws + OFF_EP), tag + 1u, __ATOMIC_RELAXED, __HIP_MEMORY_SCOPE_AGENT);
        dma<1, -1>(rs, OFF_CS + m * 3072, L_CS, hw < 3 ? hw : 2, l16);
        DMA(wb, 3); DMA(wb + SLOT, 4);
      }
      BAR();
      DMA(wc, 5); DMA(wc + SLOT, 6);
      VMWAIT(14);
    } else {
      constexpr int W0 = 8;
      v4u xf[28]; v2u st[7];
      const int xoff = OFF_X + 2 * L * XBUF + ttA * 28 * 1024, soff = OFF_ST + 2 * L * SBUF + ttA * 14 * 256;
      if (L) poll(ws, fi_x(g, w, lane), 4);
#pragma unroll
      for (int i = 0; i < W0; ++i) { xf[i] = ldf(rs, lane, xoff, i); __builtin_amdgcn_sched_barrier(0); }
      BAR();
      f32x16 aq = {}, ak = {}, av = {};
      float mean, rstd; v4u xa; v2u cw[3]; f32x4 rv[4];
      float bias[20];
      const bool hasprev = w > 0 || (g & 7) > 0;
      const float NEG = -__builtin_inff();
      pipe<84, 6>([&](auto I) AI { constexpr int i = I.v; return i < 84 ? lds16(LSL((i + 1) % 3) + (i / 3) * 1024) : v4u{}; },
                  [&]() AI {},
                  [&](auto I, v4u wf) AI { constexpr int i = I.v;
                    if constexpr (i % 3 == 0) mfma32p(wf, xf[i / 3], ak);
                    else if constexpr (i % 3 == 1) mfma32p(xf[i / 3], wf, av);
                    else mfma32p(wf, xf[i / 3], aq);
                    if constexpr (i % 3 == 0 && i / 3 + W0 < 28) xf[i / 3 + W0] = ldf(rs, lane, xoff, i / 3 + W0);
                    if constexpr (i % 3 == 1 && i / 3 < 7) st[i / 3] = ld8(rs, ((7 * h + i / 3) * 32 + t) * 8, soff);
                    if constexpr (i == 70) { if (L == 0) BAR(); }
                    if constexpr (i >= 2 && i < 62 && i % 3 == 2) {
                      constexpr int r = i / 3;
                      if constexpr (r < 16) bias[r] = ((unsigned)(t - 4 * h - ((r & 3) + 8 * (r >> 2))) <= 7u) ? 0.f : NEG;
                      else bias[r] = (hasprev && r - 17 + 4 * h >= t) ? 0.f : NEG;
                      OPQ_V(bias[r]);
                    }
                    if constexpr (i == 62) { fin_stats(st, mean, rstd); OPQ_V(mean); OPQ_V(rstd); }
                    if constexpr (i == 66) { xa = aug_x(-mean, h == 0); OPQ_V(xa); *(LAS float*)(lds + L_RS + w * 128 + t * 4) = rstd; }
                    if constexpr (i >= 66 && i < 69) cw[i - 66] = *(LAS v2u*)(lds + L_CS + (i - 66) * 256 + t * 8);
                    if constexpr (i == 72) mfma32p(aug_w(cw[1]), xa, ak);
                    if constexpr (i == 73) mfma32p(xa, aug_w(cw[2]), av);
                    if constexpr (i == 74) mfma32p(aug_w(cw[0]), xa, aq);
                    if constexpr (i >= 78 && i < 82) rv[i - 78] = *(LAS f32x4*)(lds + L_RS + w * 128 + (8 * (i - 78) + 4 * h) * 4);
                  });
      const v4u kf0 = pack8(ak, 0, rstd), kf1 = pack8(ak, 8, rstd);
      v4u vf[2];
#pragma unroll
      for (int q = 0; q < 4; ++q) {
        vf[q >> 1][(q & 1) * 2] = pk(av[4 * q] * rv[q][0], av[4 * q + 1] * rv[q][1]);
        vf[q >> 1][(q & 1) * 2 + 1] = pk(av[4 * q + 2] * rv[q][2], av[4 * q + 3] * rv[q][3]);
      }
      if (w < 3) {
        LAS char* e = lds + L_EX + (w + 1) * 3072 + lane * 16;
        *(LAS v4u*)e = kf0; *(LAS v4u*)(e + 1024) = kf1; *(LAS v4u*)(e + 2048) = vf[1];
      }
      const v4u qf0 = pack8(aq, 0, 1.f), qf1 = pack8(aq, 8, 1.f);
      f32x16 sc;
#pragma unroll
      for (int r = 0; r < 16; ++r) sc[r] = bias[r];
      mfma32p(kf0, qf0, sc); mfma32p(kf1, qf1, sc);
      BAR();
      LAS char* e = lds + L_EX + w * 3072 + lane * 16;
      f32x16 sp = {};
#pragma unroll
      for (int r = 0; r < 4; ++r) sp[12 + r] = bias[16 + r];
      mfma32p(lds16(e), qf0, sp); mfma32p(lds16(e + 1024), qf1, sp);
      const v4u vp = lds16(e + 2048);
      float p[16], pp[4], mx = NEG;
#pragma unroll
      for (int r = 0; r < 16; ++r) { p[r] = sc[r]; mx = fmaxf(mx, p[r]); }
#pragma unroll
      for (int r = 0; r < 4; ++r) { pp[r] = sp[12 + r]; mx = fmaxf(mx, pp[r]); }
      const float cf = rstd * (0.18898223650461362f * 1.4426950408889634f);
      const float mc = -xmax32(mx) * cf;
#pragma unroll
      for (int r = 0; r < 16; ++r) p[r] = __builtin_amdgcn_exp2f(__builtin_fmaf(p[r], cf, mc));
#pragma unroll
      for (int r = 0; r < 4; ++r) pp[r] = __builtin_amdgcn_exp2f(__builtin_fmaf(pp[r], cf, mc));
      const v4u pf0 = {pk(p[0], p[1]), pk(p[2], p[3]), pk(p[4], p[5]), pk(p[6], p[7])};
      const v4u pf1 = {pk(p[8], p[9]), pk(p[10], p[11]), pk(p[12], p[13]), pk(p[14], p[15])};
      const v4u pfp = {0u, 0u, pk(pp[0], pp[1]), pk(pp[2], pp[3])};
      f32x16 o = {};
      mfma32p(vf[0], pf0, o); mfma32p(vf[1], pf1, o); mfma32p(vp, pfp, o);
      float s0 = pp[0] + pp[1], s1 = pp[2] + pp[3];
#pragma unroll
      for (int r = 0; r < 8; ++r) { s0 += p[2 * r]; s1 += p[2 * r + 1]; }
      const float inv = __builtin_amdgcn_rcpf(xsum32(s0 + s1));
      st16(pack8(o, 0, inv), rs, lane * 16, OFF_O + L * OBUF + (ttA * 32 + 2 * m) * 1024);
      st16(pack8(o, 8, inv), rs, lane * 16 + 1024, OFF_O + L * OBUF + (ttA * 32 + 2 * m) * 1024);
      publish(ws, myflag, 4 * L + 1, lane);
    }
    if constexpr (QR) {
      if (active) BAR();
      DMAT(wc + 2 * SLOT, 7);
      if (active) BAR();
      VMWAIT(0);
    } else if (active) {
      constexpr int W0 = 8;
      v4u of[16];
      const int ooff = OFF_O + L * OBUF + (ttB * 32 + 16 * kh) * 1024;
      BAR();
      v4u wl[4];
#pragma unroll
      for (int i = 0; i < 4; ++i) wl[i] = __builtin_amdgcn_raw_buffer_load_b128(rs, lane * 16 + (i & 2) * 1024, OFF_WB + (j * 64 + 56 + kh * 4 + ((i & 1) ^ kh ^ 1)) * 1024, AUX_SC1);
      f32x16 acc[2] = {{}, {}};
      LAS char* wp0 = lds + slot_of(3) + kh * 15 * 1024 + lane * 16; LAS char* wp1 = lds + slot_of(3) + 1024 + kh * 13 * 1024 + lane * 16;
      OPQ_V(wp0); OPQ_V(wp1);
      pipe<32, 6>([&](auto I) AI { constexpr int i = I.v;
                    if constexpr (i < 28) return lds16(((i & 1) ? wp0 : wp1) + (i / 14) * SLOT + ((i % 14) >> 1) * 2048);
                    else return wl[i & 3]; },
                  [&]() AI {
                    poll(ws, fi_k(g, kh, ttB & 3, lane), 4 * L + 1);
#pragma unroll
                    for (int i = 0; i < W0; ++i) { of[i] = ldf(rs, lane, ooff, i); __builtin_amdgcn_sched_barrier(0); }
                  },
                  [&](auto I, v4u wf) AI { constexpr int i = I.v;
                    mfma32p(wf, of[i >> 1], acc[(i & 1) ^ 1]);
                    if constexpr (i + W0 < 16) of[i + W0] = ldf(rs, lane, ooff, i + W0);
                  });
      res_epilogue<false>(acc, res, lds, rs, ws, out, w, lane, g, m, ttB, ntB, 2 * L + 1, active, false, 4 * L + 2);
    }
    auto phase_d = [&](auto PARC) AI {
      constexpr int PAR = PARC.v, W0 = 14;
      const int hoff = OFF_H + L * HBUF + (ttB * 112 + 56 * kh + 7 * PAR) * 1024;
      v4u hf[W0];
      f32x16 acc[2] = {{}, {}};
      LAS char* db[2];
      pipe<56, 6>(
          [&](auto I) AI { constexpr int i = I.v, r = i / 14;
            if constexpr (i % 14 == 0) {
              if constexpr (r < 2) BAR();
              if constexpr (PAR && r == 0) DMA(wd + 4 * SLOT, 16);
              if constexpr (PAR && r == 1) DMA(wd + 6 * SLOT, 12);
              constexpr int sl = slot_of(r == 3 ? 12 : 12 + PAR + 2 * r);
              db[1] = lds + sl + kh * 15 * 1024 + l16; db[0] = lds + sl + 1024 + kh * 13 * 1024 + l16;
            }
            if constexpr (i == (PAR ? DQ : 42)) { if constexpr (PAR) VMWAIT(3); BAR(); }
            if constexpr (PAR && r >= 2) return wr[i - 28]; else return lds16(db[i & 1] + ((i % 14) >> 1) * 2048);
          },
          [&]() AI {
            poll(ws, (g * 16 + 8 * kh + PAR + 2 * (lane & 3)) * 4 + (ttB & 3), 4 * L + 3);
#pragma unroll
            for (int i = 0; i < W0; ++i) { hf[i] = ldf(rs, lane, hoff + (i / 7) * 14336, i % 7); __builtin_amdgcn_sched_barrier(0); }
          },
          [&](auto I, v4u wf) AI { constexpr int i = I.v;
            constexpr int k = i >> 1;
            mfma32p(wf, hf[k % W0], acc[(i & 1) ^ 1]);
            if constexpr ((i & 1) && k + W0 < 28) hf[k % W0] = ldf(rs, lane, hoff + ((k + W0) / 7) * 14336, (k + W0) % 7);
          });
      if constexpr (!PAR) { if (L == 0) { DMA(wa, 0); DMA(wa + SLOT, 1); } }
      res_epilogue<true>(acc, res, lds, rs, ws, out, w, lane, g, m, ttB, ntB, 2, active, L == 1, 4 * L + 4);
    };
    if constexpr (QR) {
      BAR();
      DMAT(wc + 3 * SLOT, 8); DMAT(wc + 4 * SLOT, 9);
      if (L == 0) {
#pragma unroll
        for (int i = 0; i < 14; ++i) { wr[14 + i] = __builtin_amdgcn_raw_buffer_load_b128(rs, l16, wd + 7 * SLOT + (kh * 14 + (i & ~1) + ((i & 1) ^ kh ^ 1)) * 1024, AUX_SC1); __builtin_amdgcn_sched_barrier(0); }
        VMWAIT(14);
      } else VMWAIT(0);
      BAR();
      DMA(wc + 5 * SLOT, 10); DMA(wc + 6 * SLOT, 11); DMA(wd, 12);
      if (L == 0) {
#pragma unroll
        for (int i = 0; i < 14; ++i) { wr[i] = __builtin_amdgcn_raw_buffer_load_b128(rs, l16, wd + 5 * SLOT + (kh * 14 + (i & ~1) + ((i & 1) ^ kh ^ 1)) * 1024, AUX_SC1); __builtin_amdgcn_sched_barrier(0); }
        VMWAIT(21);
      } else VMWAIT(7);
      BAR();
      DMA(wd + SLOT, 13); DMA(wd + 2 * SLOT, 14);
      BAR();
      DMA(wd + 3 * SLOT, 15);
      VMWAIT(7);
      __builtin_amdgcn_s_setprio(QPRIO);
      if (active) phase_d(IC<1>{}); else { VMWAIT(0); BAR(); }
      __builtin_amdgcn_s_setprio(0);
      if (L == 0) DMAT(wa + 2 * SLOT, 2);
    } else {
      constexpr int W0 = 8;
      v4u xf[28]; v2u st[7];
      const int xoff = OFF_X + (2 * L + 1) * XBUF + ttA * 28 * 1024, soff = OFF_ST + (2 * L + 1) * SBUF + ttA * 14 * 256;
      BAR();
      const int hoff = OFF_H + L * HBUF + (ttA * 112 + 7 * m) * 1024;
      LAS char* cs = lds + L_CS + 768 + t * 8;
      f32x16 a[4] = {{}, {}, {}, {}};
      float mean, rstd, c1, kk; v4u xa; float hv[8], ev = 0.f, tv = 0.f; v2u cw[7];
      auto epi = [&](const f32x16& acc, int e, int tile) AI {
        const int q = e >> 1;
        if (e & 1) {
          if (q > 0) { hv[q - 1] *= ev; OPQ_V(hv[q - 1]); }
          if (q < 8) { ev = __builtin_amdgcn_exp2f(tv); hv[q] = acc[q] * acc[8 + q]; OPQ_V(ev); OPQ_V(hv[q]); }
        } else if (e < 18) {
          if (q > 0) ev = __builtin_fmaf(ev, kk, kk);
          if (q < 8) { tv = acc[q] * c1; OPQ_V(tv); }
          if (q > 0) { ev = __builtin_amdgcn_rcpf(ev); OPQ_V(ev); }
        } else st16(v4u{pk(hv[0], hv[1]), pk(hv[2], hv[3]), pk(hv[4], hv[5]), pk(hv[6], hv[7])}, rs, lane * 16 + (tile & 3) * 1024, hoff + (tile >> 2) * 4096);
      };
      pipe<196, 5>(
          [&](auto I) AI { constexpr int i = I.v;
            if constexpr (i == 84 || i == 140) { BAR(); }
            if constexpr (i == 168) BAR();
            constexpr int tile = i < 84 ? i % 3 : i / 28;
            constexpr int ks = i < 84 ? i / 3 : i % 28;
            return i < 196 ? lds16(LSL(5 + tile) + ks * 1024) : v4u{};
          },
          [&]() AI {
            poll(ws, fi_x(g, w, lane), 4 * L + 2);
#pragma unroll
            for (int i = 0; i < W0; ++i) { xf[i] = ldf(rs, lane, xoff, i); __builtin_amdgcn_sched_barrier(0); }
          },
          [&](auto I, v4u wf) AI { constexpr int i = I.v;
            constexpr int tile = i < 84 ? i % 3 : i / 28;
            constexpr int ks = i < 84 ? i / 3 : i % 28;
            if constexpr (i >= 84 && ks == 0) { a[tile & 3] = f32x16{}; mfma32p(aug_w(cw[tile]), xa, a[tile & 3]); }
            mfma32p(wf, xf[ks], a[tile & 3]);
            if constexpr (i < 84 && i % 3 == 0 && i / 3 + W0 < 28) xf[i / 3 + W0] = ldf(rs, lane, xoff, i / 3 + W0);
            if constexpr (i % 3 == 1 && i / 3 < 7) st[i / 3] = ld8(rs, ((7 * h + i / 3) * 32 + t) * 8, soff);
            if constexpr (i >= 70 && i < 73) cw[i - 70] = *(LAS v2u*)(cs + (i - 70) * 256);
            if constexpr (i >= 78 && i % 28 == 22 && i < 170) cw[i / 28 + 1] = *(LAS v2u*)(cs + (i / 28 + 1) * 256);
            if constexpr (i == 62) { fin_stats(st, mean, rstd); OPQ_V(mean); OPQ_V(rstd); }
            if constexpr (i == 66) { xa = aug_x(-mean, h == 0); c1 = rstd * -1.4426950408889634f; kk = __builtin_amdgcn_rcpf(rstd * rstd); OPQ_V(xa); OPQ_V(c1); OPQ_V(kk); }
            if constexpr (i >= 75 && i < 78) mfma32p(aug_w(cw[i - 75]), xa, a[i - 75]);
            if constexpr (i >= 83) epi(a[((i - 83) / 19) & 3], (i - 83) % 19, (i - 83) / 19);
          });
      epi(a[1], 18, 5);
      st16(swiglu(a[2], rstd), rs, lane * 16 + 2048, hoff + 4096);
      publish(ws, myflag, 4 * L + 3, lane);
      if (active) phase_d(IC<0>{}); else { BAR(); if (L == 0) { DMA(wa, 0); DMA(wa + SLOT, 1); } }
    }
  }
  };
  if (w >= 4) run(IC<1>{}); else run(IC<0>{});
}

extern "C" void kernel_launch(void* const* d_in, const int* in_sizes, int n_in, void* d_out, int out_size, void* d_ws,
                              size_t ws_size, hipStream_t stream) {
  (void)in_sizes; (void)n_in; (void)out_size; (void)ws_size;
  (void)hipFuncSetAttribute((const void*)k_main, hipFuncAttributeMaxDynamicSharedMemorySize, LDS_BYTES);
  k_main<<<256, 512, LDS_BYTES, stream>>>((const char*)d_in[0], (const char*)d_in[1], (const char*)d_in[2], (const char*)d_in[4],
                                          (const char*)d_in[5], (const char*)d_in[6], (char*)d_ws, (unsigned short*)d_out);
}
```
